```python
import torch

def _softmax(x, axis=-1):
    return torch.softmax(x, dim=axis)
def _gelu(x):
    return torch.nn.functional.gelu(x, approximate="tanh")
def _silu(x):
    return torch.nn.functional.silu(x)
def _sigmoid(x):
    return torch.sigmoid(x)
def _layernorm(x, axis=-1, eps=1e-5):
    m = x.mean(dim=axis, keepdim=True)
    v = x.var(dim=axis, keepdim=True, unbiased=False)
    return (x - m) / torch.sqrt(v + eps)
def _scatter_add(tgt, idx, src, axis=0):
    return tgt.clone().index_add_(axis, idx, src)
def _segment_sum(src, seg, n):
    out = torch.zeros((n,) + src.shape[1:], dtype=src.dtype)
    return out.index_add_(0, seg, src)
def _pad(x, pw):
    flat = tuple(v for pair in reversed(pw) for v in pair)
    return torch.nn.functional.pad(x, flat)

kernel_name = 'gather_scatter__cheap_equivariant_egnn__graph'

def setup_inputs():

    _w = (lambda a: (lambda t: t.to(torch.bfloat16) if t.is_floating_point() else t)(torch.as_tensor(a)))
    N, deg, D, De = 10000, 32, 128, max(4, 1)
    E = N * deg
    return {
        "h": _w((lambda *s: torch.randn(*s, dtype=torch.bfloat16) * 0.1)(N, D)),
        "pos": _w((lambda *s: torch.randn(*s, dtype=torch.bfloat16) * 0.8)(N, 3)),
        "e_attr": _w((lambda *s: torch.randn(*s, dtype=torch.bfloat16) * 0.8)(E, De)),
        "src": _w((lambda lo, hi, *s: torch.randint(lo, hi, s))(0, N, E).to(torch.int64)),
        "dst": _w((lambda lo, hi, *s: torch.randint(lo, hi, s))(0, N, E).to(torch.int64)),
        "w_e": _w(0.1 * (lambda *s: torch.randn(*s, dtype=torch.bfloat16) * 0.8)(2 * D + 1 + De, D)),
        "w_x": _w(0.1 * (lambda *s: torch.randn(*s, dtype=torch.bfloat16) * 0.1)(D, 1)),
        "w_h": _w(0.1 * (lambda *s: torch.randn(*s, dtype=torch.bfloat16) * 0.1)(2 * D, D)),
    }

def reference(h, pos, e_attr, src, dst, w_e, w_x, w_h):
    N, D = h.shape[0], h.shape[1]
    diff = pos[dst] - pos[src]
    dist2 = torch.sum(diff * diff, axis=-1, keepdims=True)
    e_in = torch.concatenate([h[dst], h[src], dist2, e_attr], axis=-1)
    m_ij = _silu(e_in @ w_e)
    coord_scale = m_ij @ w_x
    pos_new = pos + _segment_sum(diff * coord_scale, dst, N)
    m_i = _segment_sum(m_ij, dst, N)
    h_new = h + _silu(torch.concatenate([h, m_i], axis=-1) @ w_h)
    return h_new, pos_new
```

```cpp
#include <hip/hip_runtime.h>
#include <hip/hip_bf16.h>
typedef unsigned short u16;
__device__ __forceinline__ float bf2f(u16 v){ return __uint_as_float((unsigned)v<<16);}
__device__ __forceinline__ u16 f2bf(float f){ __bf16 b=(__bf16)f; return __builtin_bit_cast(u16,b);}
__device__ __forceinline__ float rbf(float f){ return bf2f(f2bf(f)); }
#define N 10000
#define E 320000
__global__ void k_node(const u16* h,const u16* we,float* A,float* B,float* macc,float* pacc){
  int n=blockIdx.x, c=threadIdx.x; float a=0,b=0;
  for(int k=0;k<128;k++){ float hv=bf2f(h[n*128+k]); a+=hv*bf2f(we[k*128+c]); b+=hv*bf2f(we[(128+k)*128+c]); }
  A[n*128+c]=a; B[n*128+c]=b; macc[n*128+c]=0; if(c<4) pacc[n*4+c]=0;
}
__global__ void k_edge(const u16* pos,const u16* ea,const int* src,const int* dst,const u16* we,const u16* wx,const float* A,const float* B,float* macc,float* pacc){
  int e=blockIdx.x, c=threadIdx.x; int s=src[e], d=dst[e];
  float df[3]; float d2=0; for(int k=0;k<3;k++){ df[k]=rbf(bf2f(pos[d*3+k])-bf2f(pos[s*3+k])); d2+=rbf(df[k]*df[k]); } d2=rbf(d2);
  float x=A[d*128+c]+B[s*128+c]+d2*bf2f(we[256*128+c]); for(int k=0;k<4;k++) x+=bf2f(ea[e*4+k])*bf2f(we[(257+k)*128+c]);
  x=rbf(x); float m=rbf(x/(1.f+__expf(-x)));
  atomicAdd(&macc[d*128+c],m);
  __shared__ float red[128]; red[c]=m*bf2f(wx[c]); __syncthreads();
  for(int o=64;o>0;o>>=1){ if(c<o) red[c]+=red[c+o]; __syncthreads(); }
  if(c<3){ float cs=rbf(red[0]); atomicAdd(&pacc[d*4+c], rbf(df[c]*cs)); }
}
__global__ void k_fin(const u16* h,const u16* pos,const u16* wh,const float* macc,const float* pacc,u16* out){
  int n=blockIdx.x, c=threadIdx.x; float z=0;
  for(int k=0;k<128;k++){ z+=bf2f(h[n*128+k])*bf2f(wh[k*128+c])+rbf(macc[n*128+k])*bf2f(wh[(128+k)*128+c]); }
  z=rbf(z); float s=rbf(z/(1.f+__expf(-z))); out[n*128+c]=f2bf(bf2f(h[n*128+c])+s);
  if(c<3) out[N*128+n*3+c]=f2bf(bf2f(pos[n*3+c])+rbf(pacc[n*4+c]));
}
extern "C" void kernel_launch(void* const* d_in, const int* in_sizes, int n_in, void* d_out, int out_size, void* d_ws, size_t ws_size, hipStream_t stream) {
  const u16* h=(const u16*)d_in[0]; const u16* pos=(const u16*)d_in[1]; const u16* ea=(const u16*)d_in[2]; const int* src=(const int*)d_in[3]; const int* dst=(const int*)d_in[4];
  const u16* we=(const u16*)d_in[5]; const u16* wx=(const u16*)d_in[6]; const u16* wh=(const u16*)d_in[7];
  float* A=(float*)d_ws; float* B=A+N*128; float* macc=B+N*128; float* pacc=macc+N*128;
  k_node<<<N,128,0,stream>>>(h,we,A,B,macc,pacc);
  k_edge<<<E,128,0,stream>>>(pos,ea,src,dst,we,wx,A,B,macc,pacc);
  k_fin<<<N,128,0,stream>>>(h,pos,wh,macc,pacc,(u16*)d_out);
}
```

```cpp
#include <hip/hip_runtime.h>
#include <hip/hip_bf16.h>

typedef unsigned short u16;
typedef unsigned int u32;
typedef float f32x2 __attribute__((ext_vector_type(2)));
typedef float f32x4 __attribute__((ext_vector_type(4)));
typedef float f32x16 __attribute__((ext_vector_type(16)));
typedef __bf16 bf16x8 __attribute__((ext_vector_type(8)));
typedef __bf16 bf16x2 __attribute__((ext_vector_type(2)));
typedef u32 u32x2 __attribute__((ext_vector_type(2)));
typedef u32 u32x3 __attribute__((ext_vector_type(3)));
typedef u32 u32x4 __attribute__((ext_vector_type(4)));
typedef int i32x4 __attribute__((ext_vector_type(4)));
typedef short s16x4 __attribute__((ext_vector_type(4)));
typedef u16 u16x2 __attribute__((ext_vector_type(2)));
#define LAS __attribute__((address_space(3)))

constexpr int NN = 10000;
constexpr int NE = 320000;
constexpr int NCH = 250;
constexpr int CH = 1280;
constexpr int NB = 256;
constexpr int CAP = 1408;
constexpr float NLOG2E = -1.4426950408889634f;
constexpr float NLN2 = -0.6931471805599453f;
constexpr u32 OH = 0x3F81u;
constexpr u32 OHINV = 0x81u;

constexpr size_t WS_T = 0;
constexpr size_t WS_R = WS_T + (size_t)NB * 256 * 4;
constexpr size_t WS_B = WS_R + (size_t)NE * 12;
constexpr size_t WS_W1 = WS_B + (size_t)NN * 256;
constexpr size_t WS_WH = WS_W1 + 32768;

__device__ __forceinline__ float bf2f(u32 v) { return __uint_as_float(v << 16); }
__device__ __forceinline__ u32 pk2(float a, float b) {
  f32x2 v = {a, b};
  return __builtin_bit_cast(u32, __builtin_convertvector(v, bf16x2));
}
__device__ __forceinline__ float rbf(float a) { return __uint_as_float(pk2(a, 0.f) << 16); }
__device__ __forceinline__ int wave_incl_scan(int x) {
  x += __builtin_amdgcn_update_dpp(0, x, 0x111, 0xf, 0xf, false);
  x += __builtin_amdgcn_update_dpp(0, x, 0x112, 0xf, 0xf, false);
  x += __builtin_amdgcn_update_dpp(0, x, 0x114, 0xf, 0xf, false);
  x += __builtin_amdgcn_update_dpp(0, x, 0x118, 0xf, 0xf, false);
  x += __builtin_amdgcn_update_dpp(0, x, 0x142, 0xa, 0xf, false);
  x += __builtin_amdgcn_update_dpp(0, x, 0x143, 0xc, 0xf, false);
  return x;
}
__device__ __forceinline__ bf16x8 asbf(u32x4 v) { return __builtin_bit_cast(bf16x8, v); }
__device__ __forceinline__ u32x2 trread(const char* p) {
  return __builtin_bit_cast(u32x2, __builtin_amdgcn_ds_read_tr16_b64_v4i16((LAS s16x4*)p));
}
#define MFMA32(a, b, c) __builtin_amdgcn_mfma_f32_32x32x16_bf16(a, b, c, 0, 0, 0)
#define MFMA16(a, b, c) __builtin_amdgcn_mfma_f32_16x16x32_bf16(a, b, c, 0, 0, 0)
#define SB() __builtin_amdgcn_sched_barrier(0)

constexpr int K1_WSTR = 320;
constexpr int K1_HOFF = 128 * K1_WSTR;
constexpr int K1_LDS = K1_HOFF + 48 * 272;

__global__ __launch_bounds__(320) void egnn_k1(const u16* __restrict__ h, const u16* __restrict__ ea,
                                               const int* __restrict__ src, const int* __restrict__ dst,
                                               const u16* __restrict__ we, const u16* __restrict__ wh,
                                               char* __restrict__ ws) {
  __shared__ __attribute__((aligned(16))) char lds[K1_LDS];
  const int tid = threadIdx.x, lane = tid & 63;
  const int w = __builtin_amdgcn_readfirstlane(tid >> 6);
  if (blockIdx.x < NCH) {
    const int c = blockIdx.x, e0 = c * CH + tid * 4;
    const i32x4 d4 = *(const i32x4*)(dst + e0);
    const i32x4 s4 = *(const i32x4*)(src + e0);
    const u32x4 a0 = *(const u32x4*)(ea + (size_t)e0 * 4);
    const u32x4 a1 = *(const u32x4*)(ea + (size_t)e0 * 4 + 8);
    u32* img = (u32*)lds;
    u32* hist = (u32*)(lds + CH * 12);
    u32* base = hist + NB;
    if (tid < NB) hist[tid] = 0;
    __syncthreads();
    int bk[4], rk[4];
    u32 wd[4];
#pragma unroll
    for (int i = 0; i < 4; i++) {
      const u32 d = (u32)d4[i];
      const u32 b = (d * 16u) / 625u;
      bk[i] = b;
      wd[i] = (u32)s4[i] | ((d - ((b * 625u + 15u) >> 4)) << 16);
      rk[i] = atomicAdd(&hist[b], 1u);
    }
    __syncthreads();
    {
      const u32x4 v = *(const u32x4*)(hist + 4 * lane);
      const int s = v[0] + v[1] + v[2] + v[3];
      const int ex = wave_incl_scan(s) - s;
      u32x4 bs = {(u32)ex, ex + v[0], ex + v[0] + v[1], ex + v[0] + v[1] + v[2]};
      *(u32x4*)(base + 4 * lane) = bs;
      if (w < 4) {
        const u32 bb = w == 0 ? bs[0] : w == 1 ? bs[1] : w == 2 ? bs[2] : bs[3];
        const u32 vv = w == 0 ? v[0] : w == 1 ? v[1] : w == 2 ? v[2] : v[3];
        ((u32*)(ws + WS_T))[(4 * lane + w) * 256 + c] = bb | (vv << 16);
      }
    }
#pragma unroll
    for (int i = 0; i < 4; i++) {
      const int p = 3 * (base[bk[i]] + rk[i]);
      img[p] = i < 2 ? a0[2 * (i & 1)] : a1[2 * (i & 1)];
      img[p + 1] = i < 2 ? a0[2 * (i & 1) + 1] : a1[2 * (i & 1) + 1];
      img[p + 2] = wd[i];
    }
    __syncthreads();
    u32x4* out = (u32x4*)(ws + WS_R + (size_t)c * (CH * 12));
#pragma unroll
    for (int i = 0; i < 3; i++) out[tid + 320 * i] = ((const u32x4*)img)[tid + 320 * i];
  } else {
    const int g = blockIdx.x - NCH, n0 = 40 * g;
    u32x4 wv[7];
#pragma unroll
    for (int i = 0; i < 7; i++) {
      const int q = tid + 320 * i;
      if (q < 2048) wv[i] = *(const u32x4*)(we + 128 * 128 + q * 8);
    }
    u32x4 hv[2];
#pragma unroll
    for (int i = 0; i < 2; i++) hv[i] = *(const u32x4*)(h + (size_t)n0 * 128 + (tid + 320 * i) * 8);
    u32 pe[8];
    const bool dopack = (w == 4) && (g < 96);
    if (dopack) {
      const u16* wsrc = g < 32 ? we : wh;
      const int f = g < 32 ? g : g - 32;
      const int ww = g < 32 ? (f >> 2) : (f >> 3), ss = g < 32 ? (f & 3) : (f & 7);
#pragma unroll
      for (int j = 0; j < 8; j++) pe[j] = wsrc[(32 * ss + 8 * (lane >> 4) + j) * 128 + 16 * ww + (lane & 15)];
    }
#pragma unroll
    for (int i = 0; i < 7; i++) {
      const int q = tid + 320 * i;
      if (q < 2048) *(u32x4*)(lds + (q >> 4) * K1_WSTR + (q & 15) * 16) = wv[i];
    }
#pragma unroll
    for (int i = 0; i < 2; i++) {
      const int q = tid + 320 * i;
      *(u32x4*)(lds + K1_HOFF + (q >> 4) * 272 + (q & 15) * 16) = hv[i];
    }
    __syncthreads();
    if (w < 4) {
      const int gq = lane >> 4, c = lane & 15;
      f32x4 acc[3][2];
#pragma unroll
      for (int rt = 0; rt < 3; rt++) acc[rt][0] = acc[rt][1] = (f32x4)(0.f);
      const char* wb = lds + (8 * gq + (c >> 2)) * K1_WSTR + 64 * w + 16 * (c & 3);
#pragma unroll
      for (int s = 0; s < 4; s++) {
        const u32x2 x0 = trread(wb + (32 * s) * K1_WSTR), x1 = trread(wb + (32 * s + 4) * K1_WSTR);
        const u32x2 y0 = trread(wb + (32 * s) * K1_WSTR + 8), y1 = trread(wb + (32 * s + 4) * K1_WSTR + 8);
        const u32x4 fa = {x0[0], x0[1], x1[0], x1[1]}, fb = {y0[0], y0[1], y1[0], y1[1]};
#pragma unroll
        for (int rt = 0; rt < 3; rt++) {
          const u32x4 hf = *(const u32x4*)(lds + K1_HOFF + (16 * rt + c) * 272 + 64 * s + 16 * gq);
          acc[rt][0] = MFMA16(asbf(fa), asbf(hf), acc[rt][0]);
          acc[rt][1] = MFMA16(asbf(fb), asbf(hf), acc[rt][1]);
        }
      }
#pragma unroll
      for (int rt = 0; rt < 3; rt++) {
        const int nl = 16 * rt + c;
        if (nl < 40) {
          const f32x4 a = acc[rt][0] * NLOG2E, b = acc[rt][1] * NLOG2E;
          const u32x4 o = {pk2(a[0], a[1]), pk2(a[2], a[3]), pk2(b[0], b[1]), pk2(b[2], b[3])};
          *(u32x4*)(ws + WS_B + (size_t)(n0 + nl) * 256 + 64 * w + 16 * gq) = o;
        }
      }
    } else if (dopack) {
      const u32x4 o = {pe[0] | (pe[1] << 16), pe[2] | (pe[3] << 16), pe[4] | (pe[5] << 16), pe[6] | (pe[7] << 16)};
      *(u32x4*)(ws + WS_W1 + (size_t)g * 1024 + lane * 16) = o;
    }
  }
}

constexpr int L_STG = 0;
constexpr int L_REC = 65536;
constexpr int L_IDX = L_REC + 16384;
constexpr int L_CV = L_REC + 32 * CAP;
constexpr int L_SRC = L_CV + 6 * CAP;
constexpr int L_MACC = L_SRC + 2 * CAP;
constexpr int L_AT = L_MACC + 40 * 528;
constexpr int L_WX = L_AT + 128 * 96;
constexpr int L_PACC = L_WX + 512;
constexpr int L_CNT = L_PACC + 640;
constexpr int L_TI = L_CNT + 256;
constexpr int L_WS = L_TI + 128;
constexpr int L_CVC = L_WS + 32;
constexpr int K2_LDS = L_CVC + 64;
static_assert(K2_LDS <= 163840, "LDS");
static_assert(L_CV % 16 == 0 && L_SRC % 16 == 0 && L_MACC % 16 == 0 && L_AT % 16 == 0 && L_CVC % 16 == 0, "align");

__global__ __launch_bounds__(512, 2) void egnn_k2(const u16* __restrict__ h, const u16* __restrict__ pos,
                                                  const u16* __restrict__ we, const u16* __restrict__ wx,
                                                  const char* __restrict__ ws, u16* __restrict__ out) {
  __shared__ __attribute__((aligned(16))) char lds[K2_LDS];
  const int tid = threadIdx.x, lane = tid & 63;
  const int w = __builtin_amdgcn_readfirstlane(tid >> 6);
  const int b = blockIdx.x;
  const int n0 = (b * 625 + 15) >> 4, nn = (((b + 1) * 625 + 15) >> 4) - n0;
  const int gq = lane >> 4, c16 = lane & 15, hh = lane >> 5, n32 = lane & 31;

  const __amdgpu_buffer_rsrc_t rs_pos = __builtin_amdgcn_make_buffer_rsrc((void*)pos, 0, NN * 6, 0x00020000);
  const __amdgpu_buffer_rsrc_t rs_rec = __builtin_amdgcn_make_buffer_rsrc((void*)(ws + WS_R), 0, NE * 12, 0x00020000);
  const __amdgpu_buffer_rsrc_t rs_b = __builtin_amdgcn_make_buffer_rsrc((void*)(ws + WS_B), 0, NN * 256, 0x00020000);

  u32 tv = 0;
  if (tid < NCH) tv = ((const u32*)(ws + WS_T))[b * 256 + tid];
  u32x4 hv[2];
#pragma unroll
  for (int i = 0; i < 2; i++) {
    const int q = tid + 512 * i;
    hv[i] = (u32x4)(0u);
    if ((q >> 4) < nn) hv[i] = *(const u32x4*)(h + (size_t)n0 * 128 + q * 8);
  }
  u32x4 w1f[4];
#pragma unroll
  for (int s = 0; s < 4; s++) w1f[s] = *(const u32x4*)(ws + WS_W1 + ((w * 4 + s) * 64 + lane) * 16);
  float cw[5], wxv = 0.f;
  if (tid < 128) {
#pragma unroll
    for (int k = 0; k < 5; k++) cw[k] = bf2f(we[(256 + k) * 128 + tid]) * NLOG2E;
    wxv = bf2f(wx[tid]);
  }
  for (int p = w; p < 59; p += 8)
    __builtin_amdgcn_raw_ptr_buffer_load_lds(rs_pos, (LAS void*)(lds + L_STG + p * 1024), 16, p * 1024 + lane * 16, 0, 0, 0);
  for (int q = tid; q < 40 * 33; q += 512) *(f32x4*)(lds + L_MACC + q * 16) = (f32x4)(0.f);
  if (tid < 160) ((float*)(lds + L_PACC))[tid] = 0.f;
  if (tid < 32) ((u16*)(lds + L_CVC))[tid] = (u16)0xFF80u;
  if (tid < 128) {
    const u32 d_hi = pk2(cw[0], 0.f) & 0xffffu;
    const u32x4 o = {d_hi | (pk2(cw[1], 0.f) << 16), pk2(cw[2], cw[3]), pk2(cw[4], cw[0] - bf2f(d_hi)), 0u};
    *(u32x4*)(lds + L_AT + tid * 96 + 80) = o;
    ((float*)(lds + L_WX))[tid] = wxv;
  }
#pragma unroll
  for (int i = 0; i < 2; i++) {
    const int q = tid + 512 * i;
    if (q < 768) *(u32x4*)(lds + L_REC + (q >> 4) * 272 + (q & 15) * 16) = hv[i];
  }
  const int scnt = tv >> 16;
  const int sinc = wave_incl_scan(scnt);
  if (lane == 63 && w < 4) ((u32*)(lds + L_WS))[w] = sinc;
  __syncthreads();
  int mtot, sexc;
  {
    const u32x4 wsum = *(const u32x4*)(lds + L_WS);
    mtot = __builtin_amdgcn_readfirstlane(wsum[0] + wsum[1] + wsum[2] + wsum[3]);
    sexc = sinc - scnt + (w > 0 ? wsum[0] : 0) + (w > 1 ? wsum[1] : 0) + (w > 2 ? wsum[2] : 0);
  }
  {
    f32x4 acc[3];
#pragma unroll
    for (int rt = 0; rt < 3; rt++) acc[rt] = (f32x4)(0.f);
#pragma unroll
    for (int s = 0; s < 4; s++)
#pragma unroll
      for (int rt = 0; rt < 3; rt++) {
        const u32x4 hf = *(const u32x4*)(lds + L_REC + (16 * rt + c16) * 272 + 64 * s + 16 * gq);
        acc[rt] = MFMA16(asbf(hf), asbf(w1f[s]), acc[rt]);
      }
#pragma unroll
    for (int rt = 0; rt < 3; rt++)
      if (16 * rt + 4 * gq < 40) {
        const f32x4 a = acc[rt] * (NLOG2E / 1.0078125f);
        const u32x2 o = {pk2(a[0], a[1]), pk2(a[2], a[3])};
        *(u32x2*)(lds + L_AT + (16 * w + c16) * 96 + (16 * rt + 4 * gq) * 2) = o;
      }
  }

  u32x4 id0, id1;
  {
    const int k0 = n32 - 8 * hh, k1 = k0 - 16;
#pragma unroll
    for (int i = 0; i < 4; i++) {
      id0[i] = k0 == 2 * i ? 0x3F80u : k0 == 2 * i + 1 ? 0x3F800000u : 0u;
      id1[i] = k1 == 2 * i ? 0x3F80u : k1 == 2 * i + 1 ? 0x3F800000u : 0u;
    }
  }
  char* const stg = lds + L_STG + w * 8192;
  const int ga0 = n32 * 256 + 16 * (hh ^ (n32 & 15));
  f32x16 Y[4];
#pragma unroll
  for (int t = 0; t < 4; t++) Y[t] = (f32x16)(0.f);

  for (int r0 = 0;; r0 += CAP) {
    const int m = min(mtot - r0, CAP);
    const int nt = (m + 31) >> 5;
    if (r0 > 0) {
      __syncthreads();
      for (int p = w; p < 59; p += 8)
        __builtin_amdgcn_raw_ptr_buffer_load_lds(rs_pos, (LAS void*)(lds + L_STG + p * 1024), 16, p * 1024 + lane * 16, 0, 0, 0);
    }
    if (tid < 64) ((u32*)(lds + L_CNT))[tid] = 0;
    {
      const int gs = tid * CH + (tv & 0xffff);
      const int i0 = max(0, r0 - sexc), i1 = min(scnt, r0 + CAP - sexc);
      for (int i = i0; i < i1; i++) ((u32*)(lds + L_IDX))[sexc + i - r0] = gs + i;
    }
    asm volatile("s_waitcnt vmcnt(0)" ::: "memory");
    __syncthreads();
    u32x3 rec[3];
#pragma unroll
    for (int k = 0; k < 3; k++) {
      const int f = tid + 512 * k;
      rec[k] = (u32x3)(0u);
      if (f < m) rec[k] = __builtin_amdgcn_raw_buffer_load_b96(rs_rec, ((const u32*)(lds + L_IDX))[f] * 12, 0, 0);
    }
    int rk[3];
    u32 dd[3], dx[3], dy[3], dz[3];
#pragma unroll
    for (int k = 0; k < 3; k++) {
      const int f = tid + 512 * k;
      rk[k] = 0; dd[k] = dx[k] = dy[k] = dz[k] = 0;
      if (f < m) {
        const u32 sv = rec[k][2] & 0xffffu, dl = rec[k][2] >> 16;
        rk[k] = atomicAdd((u32*)(lds + L_CNT) + dl, 1u);
        const u16* ps = (const u16*)(lds + L_STG) + sv * 3;
        const u16* pd = (const u16*)(lds + L_STG) + (n0 + dl) * 3;
        const float fx = rbf(bf2f(pd[0]) - bf2f(ps[0])), fy = rbf(bf2f(pd[1]) - bf2f(ps[1])), fz = rbf(bf2f(pd[2]) - bf2f(ps[2]));
        dd[k] = pk2(rbf(fx * fx) + rbf(fy * fy) + rbf(fz * fz), 0.f) & 0xffffu;
        dx[k] = ((__float_as_uint(fx) >> 16) * OHINV) & 0xffffu;
        dy[k] = ((__float_as_uint(fy) >> 16) * OHINV) & 0xffffu;
        dz[k] = ((__float_as_uint(fz) >> 16) * OHINV) & 0xffffu;
      }
    }
    __syncthreads();
    {
      const int cn = ((const u32*)(lds + L_CNT))[lane];
      const int st = wave_incl_scan(cn) - cn;
#pragma unroll
      for (int k = 0; k < 3; k++) {
        const int f = tid + 512 * k;
        const u32 dl = rec[k][2] >> 16;
        const int sp = __shfl(st, (int)dl, 64);
        if (f < nt * 32) {
          const bool ok = f < m;
          const int p = ok ? sp + rk[k] : f;
          const u32 ohv = ok ? (OH << (16 * (dl & 1))) : 0u, ohi = (dl >> 1) & 3;
          const u32x4 ra = {dd[k] | (rec[k][0] << 16), (rec[k][0] >> 16) | (rec[k][1] << 16), (rec[k][1] >> 16) | (dd[k] << 16), dl};
          const u32x4 rb = {ohi == 0 ? ohv : 0u, ohi == 1 ? ohv : 0u, ohi == 2 ? ohv : 0u, ohi == 3 ? ohv : 0u};
          *(u32x4*)(lds + L_REC + p * 32) = ra;
          *(u32x4*)(lds + L_REC + p * 32 + 16) = rb;
          const int kp = (p & ~12) | ((p & 4) << 1) | ((p & 8) >> 1);
          ((u16*)(lds + L_CV))[kp] = (u16)dx[k];
          ((u16*)(lds + L_CV))[CAP + kp] = (u16)dy[k];
          ((u16*)(lds + L_CV))[2 * CAP + kp] = (u16)dz[k];
          ((u16*)(lds + L_SRC))[(p & ~31) | ((p & 3) << 3) | ((p >> 2) & 7)] = (u16)rec[k][2];
          if (ok && (p & 31) == 0) ((unsigned char*)(lds + L_TI))[2 * (p >> 5)] = (unsigned char)dl;
          if (ok && ((p & 31) == 31 || p == m - 1)) ((unsigned char*)(lds + L_TI))[2 * (p >> 5) + 1] = (unsigned char)dl;
        }
      }
    }
    __syncthreads();

    const int tq = nt >> 3, trm = nt & 7;
    int tile = w * tq + min(w, trm);
    const int tend = tile + tq + (w < trm ? 1 : 0);
    if (tile < tend) {
      const int tinfo = ((const u16*)(lds + L_TI))[min(lane, 43)];
      auto issue = [&](int t) {
        const u32x4 sv = *(const u32x4*)(lds + L_SRC + t * 64 + gq * 16);
#pragma unroll
        for (int j = 0; j < 8; j++) {
          const u32 s = (j & 1) ? (sv[j >> 1] >> 16) : (sv[j >> 1] & 0xffffu);
          __builtin_amdgcn_raw_ptr_buffer_load_lds(rs_b, (LAS void*)(stg + j * 1024), 16,
                                                   s * 256 + 16 * (c16 ^ (4 * (j & 3) + gq)), 0, 0, 0);
        }
      };
      u32x4 wex[4];
      auto loadw = [&](int base) {
        const int g0 = base >> 3, bm = base & 7;
        const char* pa = lds + L_AT + n32 * 96 + (hh ? g0 * 16 : 80);
        const char* pb = lds + L_AT + n32 * 96 + (hh ? g0 * 16 + 16 : 80);
#pragma unroll
        for (int t = 0; t < 4; t++) {
          const u32x4 a = *(const u32x4*)(pa + t * 3072), bb = *(const u32x4*)(pb + t * 3072);
#pragma unroll
          for (int i = 0; i < 4; i++) {
            const u32 mk = (2 * i >= bm ? 0xffffu : 0u) | (2 * i + 1 >= bm ? 0xffff0000u : 0u);
            wex[t][i] = (a[i] & mk) | (bb[i] & ~mk);
          }
        }
      };
      auto flush = [&](int base) {
        const int node = base + (((n32 & 7) - base) & 7), comp = n32 >> 3;
        f32x2 pa = {0.f, 0.f};
#pragma unroll
        for (int t = 0; t < 4; t++)
#pragma unroll
          for (int q = 0; q < 4; q++) {
            const f32x4 wv = *(const f32x4*)(lds + L_WX + (32 * t + 8 * q + 4 * hh) * 4);
            pa += (f32x2){Y[t][4 * q], Y[t][4 * q + 1]} * (f32x2){wv[0], wv[1]};
            pa += (f32x2){Y[t][4 * q + 2], Y[t][4 * q + 3]} * (f32x2){wv[2], wv[3]};
          }
        if (node < nn) {
          if (comp == 0) {
            float* mp = (float*)(lds + L_MACC) + node * 132 + 4 * hh;
#pragma unroll
            for (int t = 0; t < 4; t++)
#pragma unroll
              for (int r = 0; r < 16; r++) atomicAdd(mp + 32 * t + 8 * (r >> 2) + (r & 3), Y[t][r]);
          } else {
            atomicAdd((float*)(lds + L_PACC) + node * 4 + comp, pa[0] + pa[1]);
          }
        }
#pragma unroll
        for (int t = 0; t < 4; t++) Y[t] = (f32x16)(0.f);
      };
      issue(tile);
      int base = __builtin_amdgcn_readlane(tinfo, tile) & 0xff;
      int done = 0;
      loadw(base);
      const int cvinc = n32 < 8 ? 0 : 64;
      const int cvoff = (n32 < 8 ? L_CVC : L_CV + ((n32 >> 3) - 1) * (2 * CAP)) + 16 * hh;
      const int troff = L_REC + (4 * hh + (c16 >> 2)) * 32 + 16 + 8 * (c16 & 1);
      while (tile < tend) {
        const int ti = __builtin_amdgcn_readlane(tinfo, tile);
        const int first = ti & 0xff, last = ti >> 8;
        const int lo = max(first, done);
        if (last >= base + 8 && lo != base) {
          flush(base);
          base = lo;
          loadw(base);
        }
        const bool more = last >= base + 8;
        asm volatile("s_waitcnt vmcnt(0)" ::: "memory");
        u32x4 G[8];
#pragma unroll
        for (int s = 0; s < 8; s++) G[s] = *(const u32x4*)(stg + (ga0 ^ (32 * s)));
        const u32x4 aex = *(const u32x4*)(lds + L_REC + tile * 1024 + n32 * 32 + 16 * hh);
        u32x4 sp[2];
#pragma unroll
        for (int s = 0; s < 2; s++) {
          const u32x2 o0 = trread(lds + troff + tile * 1024 + s * 512), o1 = trread(lds + troff + tile * 1024 + s * 512 + 256);
          const u32x4 cv = *(const u32x4*)(lds + cvoff + tile * cvinc + 32 * s);
          const u32x4 oh = {o0[0], o0[1], o1[0], o1[1]};
#pragma unroll
          for (int i = 0; i < 4; i++) {
            const u32 oa = oh[i], ca = cv[i];
            sp[s][i] = __builtin_bit_cast(u32, __builtin_bit_cast(u16x2, oa) * __builtin_bit_cast(u16x2, ca));
          }
        }
        if (more || lo > first) {
#pragma unroll
          for (int s = 0; s < 2; s++)
#pragma unroll
            for (int j = 0; j < 8; j++) {
              const int row = 16 * s + 8 * (j >> 2) + 4 * hh + (j & 3);
              const int dl = *(const u32*)(lds + L_REC + (tile * 32 + row) * 32 + 12);
              if (dl < lo || dl >= base + 8) sp[s][j >> 1] &= (j & 1) ? 0x0000ffffu : 0xffff0000u;
            }
        }
        asm volatile("s_waitcnt lgkmcnt(0)" ::: "memory");
        SB();
        if (!more && tile + 1 < tend) issue(tile + 1);
        SB();
        f32x16 X[2];
        X[0] = MFMA32(asbf(aex), asbf(wex[0]), (f32x16)(0.f));
        X[0] = MFMA32(asbf(G[0]), asbf(id0), X[0]);
        X[0] = MFMA32(asbf(G[1]), asbf(id1), X[0]);
        u32x4 pa, pb;
#pragma unroll
        for (int t = 0; t < 4; t++) {
          const f32x16 xc = X[t & 1];
          f32x16 e;
          SB();
          if (t < 3) X[(t + 1) & 1] = MFMA32(asbf(aex), asbf(wex[t + 1]), (f32x16)(0.f));
          SB();
#pragma unroll
          for (int r = 0; r < 8; r++) e[r] = __builtin_amdgcn_exp2f(xc[r]);
          SB();
          if (t < 3) X[(t + 1) & 1] = MFMA32(asbf(G[2 * t + 2]), asbf(id0), X[(t + 1) & 1]);
          SB();
#pragma unroll
          for (int r = 8; r < 16; r++) e[r] = __builtin_amdgcn_exp2f(xc[r]);
          SB();
          e = e + 1.0f;
          SB();
          if (t < 3) X[(t + 1) & 1] = MFMA32(asbf(G[2 * t + 3]), asbf(id1), X[(t + 1) & 1]);
          SB();
#pragma unroll
          for (int r = 0; r < 8; r++) e[r] = __builtin_amdgcn_rcpf(e[r]);
          SB();
          if (t > 0) Y[t - 1] = MFMA32(asbf(pa), asbf(sp[0]), Y[t - 1]);
          SB();
#pragma unroll
          for (int r = 8; r < 16; r++) e[r] = __builtin_amdgcn_rcpf(e[r]);
          SB();
          e = e * xc;
          SB();
          if (t > 0) Y[t - 1] = MFMA32(asbf(pb), asbf(sp[1]), Y[t - 1]);
          SB();
#pragma unroll
          for (int i = 0; i < 4; i++) {
            pa[i] = pk2(e[2 * i], e[2 * i + 1]);
            pb[i] = pk2(e[8 + 2 * i], e[8 + 2 * i + 1]);
          }
        }
        SB();
        Y[3] = MFMA32(asbf(pa), asbf(sp[0]), Y[3]);
        Y[3] = MFMA32(asbf(pb), asbf(sp[1]), Y[3]);
        if (more) {
          done = base + 8;
        } else {
          done = 0;
          tile++;
        }
      }
      flush(base);
    }
    if (r0 + CAP >= mtot) break;
  }

  u32x4 whf[8];
#pragma unroll
  for (int s = 0; s < 8; s++) whf[s] = *(const u32x4*)(ws + WS_WH + ((w * 8 + s) * 64 + lane) * 16);
#pragma unroll
  for (int i = 0; i < 2; i++) {
    const int q = tid + 512 * i;
    hv[i] = (u32x4)(0u);
    if ((q >> 4) < nn) hv[i] = *(const u32x4*)(h + (size_t)n0 * 128 + q * 8);
  }
  u32 posv = 0;
  if (tid < 3 * nn) posv = pos[n0 * 3 + tid];
  __syncthreads();
#pragma unroll
  for (int i = 0; i < 2; i++) {
    const int q = tid + 512 * i;
    if (q < 768) {
      const int row = q >> 4, ch = q & 15;
      *(u32x4*)(lds + L_STG + row * 528 + ch * 16) = hv[i];
      u32x4 o = (u32x4)(0u);
      if (row < 40) {
        const f32x4 a = *(const f32x4*)(lds + L_MACC + row * 528 + ch * 32) * NLN2;
        const f32x4 bb = *(const f32x4*)(lds + L_MACC + row * 528 + ch * 32 + 16) * NLN2;
        o = (u32x4){pk2(a[0], a[1]), pk2(a[2], a[3]), pk2(bb[0], bb[1]), pk2(bb[2], bb[3])};
      }
      *(u32x4*)(lds + L_STG + row * 528 + 256 + ch * 16) = o;
    }
  }
  if (tid < 3 * nn) {
    const int nl = tid / 3, k = tid - 3 * nl;
    const float dp = rbf(((const float*)(lds + L_PACC))[nl * 4 + 1 + k] * NLN2);
    out[(size_t)NN * 128 + n0 * 3 + tid] = (u16)pk2(bf2f(posv) + dp, 0.f);
  }
  __syncthreads();
  {
    f32x4 acc[3];
#pragma unroll
    for (int rt = 0; rt < 3; rt++) acc[rt] = (f32x4)(0.f);
#pragma unroll
    for (int s = 0; s < 8; s++)
#pragma unroll
      for (int rt = 0; rt < 3; rt++) {
        const u32x4 xf = *(const u32x4*)(lds + L_STG + (16 * rt + c16) * 528 + 64 * s + 16 * gq);
        acc[rt] = MFMA16(asbf(whf[s]), asbf(xf), acc[rt]);
      }
#pragma unroll
    for (int rt = 0; rt < 3; rt++) {
      const int nl = 16 * rt + c16;
      const u32x2 hr = *(const u32x2*)(lds + L_STG + nl * 528 + 32 * w + 8 * gq);
      float o[4];
#pragma unroll
      for (int r = 0; r < 4; r++) {
        const float z = rbf(acc[rt][r]);
        const float sg = rbf(z * __builtin_amdgcn_rcpf(1.0f + __builtin_amdgcn_exp2f(z * NLOG2E)));
        const u32 hb = (r & 1) ? (hr[r >> 1] >> 16) : (hr[r >> 1] & 0xffffu);
        o[r] = bf2f(hb) + sg;
      }
      if (nl < nn) *(u32x2*)(out + (size_t)(n0 + nl) * 128 + 16 * w + 4 * gq) = (u32x2){pk2(o[0], o[1]), pk2(o[2], o[3])};
    }
  }
}

extern "C" void kernel_launch(void* const* d_in, const int* in_sizes, int n_in, void* d_out, int out_size,
                              void* d_ws, size_t ws_size, hipStream_t stream) {
  const u16* h = (const u16*)d_in[0];
  const u16* pos = (const u16*)d_in[1];
  const u16* ea = (const u16*)d_in[2];
  const int* src = (const int*)d_in[3];
  const int* dst = (const int*)d_in[4];
  const u16* we = (const u16*)d_in[5];
  const u16* wx = (const u16*)d_in[6];
  const u16* wh = (const u16*)d_in[7];
  egnn_k1<<<2 * NCH, 320, 0, stream>>>(h, ea, src, dst, we, wh, (char*)d_ws);
  egnn_k2<<<NB, 512, 0, stream>>>(h, pos, we, wx, (const char*)d_ws, (u16*)d_out);
}
```

```cpp
#include <hip/hip_runtime.h>
#include <hip/hip_bf16.h>

typedef unsigned short u16;
typedef unsigned int u32;
typedef float f32x2 __attribute__((ext_vector_type(2)));
typedef float f32x4 __attribute__((ext_vector_type(4)));
typedef float f32x16 __attribute__((ext_vector_type(16)));
typedef __bf16 bf16x8 __attribute__((ext_vector_type(8)));
typedef __bf16 bf16x2 __attribute__((ext_vector_type(2)));
typedef u32 u32x2 __attribute__((ext_vector_type(2)));
typedef u32 u32x3 __attribute__((ext_vector_type(3)));
typedef u32 u32x4 __attribute__((ext_vector_type(4)));
typedef int i32x4 __attribute__((ext_vector_type(4)));
typedef short s16x4 __attribute__((ext_vector_type(4)));
typedef u16 u16x2 __attribute__((ext_vector_type(2)));
#define LAS __attribute__((address_space(3)))
typedef __amdgpu_buffer_rsrc_t rsrc_t;

constexpr int NN = 10000;
constexpr int NE = 320000;
constexpr int NCH = 250;
constexpr int CH = 1280;
constexpr int NB = 256;
constexpr int CAP = 1408;
constexpr float NLOG2E = -1.4426950408889634f;
constexpr float NLN2 = -0.6931471805599453f;
constexpr u32 OH = 0x3F81u;
constexpr u32 OHINV = 0x81u;

constexpr size_t WS_T = 0;
constexpr size_t WS_R = WS_T + (size_t)NB * 256 * 4;
constexpr size_t WS_B = WS_R + (size_t)NE * 12;
constexpr size_t WS_WH = WS_B + (size_t)NN * 256;
constexpr size_t WS_W1 = WS_WH + 65536;

__device__ __forceinline__ float bf2f(u32 v) { return __uint_as_float(v << 16); }
__device__ __forceinline__ float bfhi(u32 v) { return __uint_as_float(v & 0xffff0000u); }
__device__ __forceinline__ u32 pk2(float a, float b) {
  f32x2 v = {a, b};
  return __builtin_bit_cast(u32, __builtin_convertvector(v, bf16x2));
}
__device__ __forceinline__ u32 mul16x2(u32 a, u32 b) {
  return __builtin_bit_cast(u32, __builtin_bit_cast(u16x2, a) * __builtin_bit_cast(u16x2, b));
}
__device__ __forceinline__ int wave_incl_scan(int x) {
  x += __builtin_amdgcn_update_dpp(0, x, 0x111, 0xf, 0xf, false);
  x += __builtin_amdgcn_update_dpp(0, x, 0x112, 0xf, 0xf, false);
  x += __builtin_amdgcn_update_dpp(0, x, 0x114, 0xf, 0xf, false);
  x += __builtin_amdgcn_update_dpp(0, x, 0x118, 0xf, 0xf, false);
  x += __builtin_amdgcn_update_dpp(0, x, 0x142, 0xa, 0xf, false);
  x += __builtin_amdgcn_update_dpp(0, x, 0x143, 0xc, 0xf, false);
  return x;
}
__device__ __forceinline__ bf16x8 asbf(u32x4 v) { return __builtin_bit_cast(bf16x8, v); }
__device__ __forceinline__ u32x2 trread(const char* p) {
  return __builtin_bit_cast(u32x2, __builtin_amdgcn_ds_read_tr16_b64_v4i16((LAS s16x4*)p));
}
__device__ __forceinline__ rsrc_t mkrs(const void* p, u32 bytes) { return __builtin_amdgcn_make_buffer_rsrc((void*)p, 0, bytes, 0x00020000); }
__device__ __forceinline__ void dma16(rsrc_t rs, char* ldsp, u32 voff, u32 soff) {
  __builtin_amdgcn_raw_ptr_buffer_load_lds(rs, (LAS void*)ldsp, 16, voff, soff, 0, 0);
}
#define MFMA32(a, b, c) __builtin_amdgcn_mfma_f32_32x32x16_bf16(a, b, c, 0, 0, 0)
#define MFMA16(a, b, c) __builtin_amdgcn_mfma_f32_16x16x32_bf16(a, b, c, 0, 0, 0)
#define SB() __builtin_amdgcn_sched_barrier(0)
#define VMWAIT0() asm volatile("s_waitcnt vmcnt(0)" ::: "memory")
#define STAMPDECL
#define STAMP(buf, i)
#define STAMPV(buf, i)
#define RSTAMP(buf, i)
#define DRAIN()

constexpr int K1_H = 32768;
constexpr int K1_IMG = K1_H + 48 * 256;
constexpr int K1_HIST = K1_IMG + CH * 12;
constexpr int K1_LDS = K1_HIST + 2 * NB * 4 + 16;

__global__ __launch_bounds__(640) void egnn_k1(const u16* __restrict__ h, const u16* __restrict__ ea,
                                               const int* __restrict__ src, const int* __restrict__ dst,
                                               const u16* __restrict__ we, const u16* __restrict__ wh,
                                               char* __restrict__ ws) {
  __shared__ __attribute__((aligned(16))) char lds[K1_LDS];
  const int lane = threadIdx.x & 63;
  const int w = __builtin_amdgcn_readfirstlane(threadIdx.x >> 6);
  const int c = blockIdx.x;
  STAMPDECL; RSTAMP(0, 30); STAMP(0, 0);
  if (w < 5) {
    const int tid = threadIdx.x, e0 = c * CH + tid * 4;
    const i32x4 d4 = *(const i32x4*)(dst + e0);
    const i32x4 s4 = *(const i32x4*)(src + e0);
    const u32x4 a0 = *(const u32x4*)(ea + (size_t)e0 * 4);
    const u32x4 a1 = *(const u32x4*)(ea + (size_t)e0 * 4 + 8);
    u32* img = (u32*)(lds + K1_IMG);
    u32* hist = (u32*)(lds + K1_HIST);
    u32* base = hist + NB;
    if (tid < NB) hist[tid] = 0;
    if (tid == NB) hist[2 * NB] = 0;
    __syncthreads();
    STAMP(0, 1);
    u32 bk[4], rk[4];
#pragma unroll
    for (int i = 0; i < 4; i++) {
      bk[i] = __umul24((u32)d4[i], 214749u) >> 23;
      rk[i] = atomicAdd(&hist[bk[i]], 1u);
    }
    STAMP(0, 2);
    __syncthreads();
    STAMP(0, 3);
    {
      const u32x4 v = *(const u32x4*)(hist + 4 * lane);
      const int s = v[0] + v[1] + v[2] + v[3];
      const int ex = wave_incl_scan(s) - s;
      u32x4 bs = {(u32)ex, ex + v[0], ex + v[0] + v[1], ex + v[0] + v[1] + v[2]};
      *(u32x4*)(base + 4 * lane) = bs;
      if (w < 4) {
        const u32 bb = w == 0 ? bs[0] : w == 1 ? bs[1] : w == 2 ? bs[2] : bs[3];
        const u32 vv = w == 0 ? v[0] : w == 1 ? v[1] : w == 2 ? v[2] : v[3];
        ((u32*)(ws + WS_T))[(4 * lane + w) * 256 + c] = bb | (vv << 16);
      }
    }
#pragma unroll
    for (int i = 0; i < 4; i++) {
      u32* p = img + 3 * (base[bk[i]] + rk[i]);
      p[0] = i < 2 ? a0[2 * (i & 1)] : a1[2 * (i & 1)];
      p[1] = i < 2 ? a0[2 * (i & 1) + 1] : a1[2 * (i & 1) + 1];
      p[2] = (u32)s4[i] | ((u32)d4[i] << 16);
    }
    STAMP(0, 4);
    __builtin_amdgcn_fence(__ATOMIC_RELEASE, "workgroup");
    if (lane == 0) __hip_atomic_fetch_add(&hist[2 * NB], 1u, __ATOMIC_RELAXED, __HIP_MEMORY_SCOPE_WORKGROUP);
    while (__hip_atomic_load(&hist[2 * NB], __ATOMIC_RELAXED, __HIP_MEMORY_SCOPE_WORKGROUP) < 5u) __builtin_amdgcn_s_sleep(1);
    __builtin_amdgcn_fence(__ATOMIC_ACQUIRE, "workgroup");
    STAMP(0, 5);
    u32x4* out = (u32x4*)(ws + WS_R + (size_t)c * (CH * 12));
#pragma unroll
    for (int i = 0; i < 3; i++) out[tid + 320 * i] = ((const u32x4*)img)[tid + 320 * i];
    STAMP(0, 6); DRAIN(); STAMP(0, 7); RSTAMP(0, 31);
  } else {
    const int u = w - 5, n0 = 40 * c;
    const int gq = lane >> 4, cc = lane & 15;
    {
      const rsrc_t rs_w = mkrs(we + 128 * 128, 32768), rs_h = mkrs(h + (size_t)n0 * 128, 40 * 256);
      const u32 vw = gq * 256 + 16 * (cc ^ (4 * gq));
      for (int i = u; i < 32; i += 5) dma16(rs_w, lds + i * 1024, vw, i * 1024);
#pragma unroll
      for (int i = 0; i < 2; i++) {
        const int p = u + 5 * i;
        dma16(rs_h, lds + K1_H + p * 1024, gq * 256 + 16 * (cc ^ ((4 * p + gq) & 15)), p * 1024);
      }
    }
    u32 pe[8];
    const bool dopack = (u == 4) && (c < 96);
    if (dopack) {
      const u16* wsrc = c < 32 ? we : wh;
      const int f = c < 32 ? c : c - 32;
      const int ww = c < 32 ? (f >> 2) : (f >> 3), ss = c < 32 ? (f & 3) : (f & 7);
#pragma unroll
      for (int j = 0; j < 8; j++) pe[j] = wsrc[(32 * ss + 8 * gq + j) * 128 + 16 * ww + cc];
    }
    __builtin_amdgcn_s_barrier();
    STAMP(0, 1);
    VMWAIT0();
    STAMP(0, 2);
    __builtin_amdgcn_s_barrier();
    STAMP(0, 3);
    if (u < 4) {
      f32x4 acc[3][2];
#pragma unroll
      for (int rt = 0; rt < 3; rt++) acc[rt][0] = acc[rt][1] = (f32x4)(0.f);
      const char* wb = lds + (8 * gq + (cc >> 2)) * 256 + 16 * ((4 * u + (cc & 3)) ^ (cc & 12));
      const int hb = K1_H + cc * 256 + 16 * (gq ^ cc);
#pragma unroll
      for (int s = 0; s < 4; s++) {
        const u32x2 x0 = trread(wb + (32 * s) * 256), x1 = trread(wb + (32 * s + 4) * 256);
        const u32x2 y0 = trread(wb + (32 * s) * 256 + 8), y1 = trread(wb + (32 * s + 4) * 256 + 8);
        const u32x4 fa = {x0[0], x0[1], x1[0], x1[1]}, fb = {y0[0], y0[1], y1[0], y1[1]};
#pragma unroll
        for (int rt = 0; rt < 3; rt++) {
          const u32x4 hf = *(const u32x4*)(lds + (hb ^ (64 * s)) + rt * 4096);
          acc[rt][0] = MFMA16(asbf(fa), asbf(hf), acc[rt][0]);
          acc[rt][1] = MFMA16(asbf(fb), asbf(hf), acc[rt][1]);
        }
      }
      char* ob = ws + WS_B + (size_t)(n0 + cc) * 256 + 64 * u + 16 * gq;
#pragma unroll
      for (int rt = 0; rt < 3; rt++) {
        if (16 * rt + cc < 40) {
          const f32x4 a = acc[rt][0] * NLOG2E, b = acc[rt][1] * NLOG2E;
          const u32x4 o = {pk2(a[0], a[1]), pk2(a[2], a[3]), pk2(b[0], b[1]), pk2(b[2], b[3])};
          *(u32x4*)(ob + rt * 4096) = o;
        }
      }
    } else if (dopack) {
      u32x4 o = {pe[0] | (pe[1] << 16), pe[2] | (pe[3] << 16), pe[4] | (pe[5] << 16), pe[6] | (pe[7] << 16)};
      if (c < 32) {
        *(u32x4*)(ws + WS_W1 + (size_t)c * 1024 + lane * 16) = o;
      } else {
        const int f = c - 32, ss = f & 7;
        if (ss >= 4)
#pragma unroll
          for (int i = 0; i < 4; i++) o[i] = pk2(bf2f(pe[2 * i]) * NLN2, bf2f(pe[2 * i + 1]) * NLN2);
        const int row = 32 * (f >> 3) + 4 * (lane >> 3) + (ss & 3);
        *(u32x4*)(ws + WS_WH + row * 256 + (ss >> 2) * 128 + 16 * ((lane & 7) ^ (2 * (ss & 3) + ((lane >> 4) & 1)))) = o;
      }
    }
    STAMP(0, 6); DRAIN(); STAMP(0, 7); RSTAMP(0, 31);
  }
}

constexpr int OHS = 2 * CAP + 16;
constexpr int L_NODE = 0;
constexpr int L_WX = L_NODE + 320;
constexpr int L_WS = L_WX + 512;
constexpr int L_REC = L_WS + 16;
constexpr int L_CV = L_REC + 16 * (CAP + 1);
constexpr int L_SRC = L_CV + 8 * (CAP + 2);
constexpr int L_OH = L_SRC + 2 * (CAP + 8);
constexpr int L_CNT = L_OH + 8 * OHS;
constexpr int L_HM = L_CNT + 256;
constexpr int L_IMG = L_HM + 160;
constexpr int L_AT = L_IMG + 48 * 528;
constexpr int L_STG = (L_AT + 128 * 96 + 127) & ~127;
constexpr int K2_LDS = L_STG + 65536;
constexpr int L_MG = L_STG + 32768;
static_assert(K2_LDS <= 163840, "LDS");
static_assert(L_CV % 16 == 0 && L_OH % 16 == 0 && L_SRC % 16 == 0 && L_IMG % 16 == 0 && L_AT % 16 == 0, "align");
static_assert(L_IMG - L_OH > 2 * 8192 && L_IMG - L_OH <= 3 * 8192, "zero sweep");

#define NPF 0
#define PFSTR 64
#define STR2_(x) #x
#define STR_(x) STR2_(x)
#define NPF_S STR_(NPF)
template <int V> struct IC { static constexpr int value = V; };
__device__ __forceinline__ u32x4 mksrd(const void* p, u32 bytes) {
  const unsigned long long a = (unsigned long long)p;
  return (u32x4){(u32)a, (u32)(a >> 32) & 0xffffu, bytes, 0x00020000u};
}
#define DMA1(J, A, SRD, SOFF) asm volatile("buffer_load_dwordx4 %0, %1, %2 offen offset:" #J " lds" :: "v"(A), "s"(SRD), "s"(SOFF) : "memory")
#define LDB(T, DST, VO, SRD, SO) asm volatile("buffer_load_" T " %0, %1, %2, %3 offen" : "=v"(DST) : "v"(VO), "s"(SRD), "s"(SO) : "memory")
#define SETM0(V) asm volatile("s_mov_b32 m0, %0\n\ts_nop 0" :: "s"(V) : "memory")
#define VMWAIT(N) asm volatile("s_waitcnt vmcnt(" #N ")" ::: "memory")
#define LGKMWAIT0() asm volatile("s_waitcnt lgkmcnt(0)" ::: "memory")
#define PIN(x) do { asm volatile("" : "+v"(x)); SB(); } while (0)

__global__ __launch_bounds__(512, 2) void egnn_k2(const u16* __restrict__ h, const u16* __restrict__ pos,
                                                  const u16* __restrict__ we, const u16* __restrict__ wx,
                                                  const char* __restrict__ ws, u16* __restrict__ out) {
  __shared__ __attribute__((aligned(128))) char lds[K2_LDS];
  const int tid = threadIdx.x, lane = tid & 63;
  const int w = __builtin_amdgcn_readfirstlane(tid >> 6);
  const int b = (blockIdx.x & 7) * 32 + (blockIdx.x >> 3);
  const int n0 = (b * 625 + 15) >> 4, nn = (((b + 1) * 625 + 15) >> 4) - n0;
  const int gq = lane >> 4, c16 = lane & 15, hh = lane >> 5, n32 = lane & 31;
  const u32 ldsb = (u32)(size_t)(LAS char*)lds;
  const u32x4 srd_g = mksrd(ws + WS_B - 4096, 4096 + NN * 256 + 65536 + 32768), srd_pos = mksrd(pos, NN * 6);

  STAMPDECL; RSTAMP(1, 30); STAMP(1, 0);
  u32 cwv[6], tv;
  u32x4 hv[2], w1f[4];
  LDB("dword", tv, (tid & 255) * 4, mksrd(ws + WS_T + b * 1024, NCH * 4), 0);
  {
    const u32x4 srd_h = mksrd(h + (size_t)n0 * 128, nn * 256);
    LDB("dwordx4", hv[0], tid * 16, srd_h, 0);
    LDB("dwordx4", hv[1], tid * 16, srd_h, 8192);
#pragma unroll
    for (int s = 0; s < 4; s++) LDB("dwordx4", w1f[s], lane * 16, srd_g, 4096 + NN * 256 + 65536 + (w * 4 + s) * 1024);
    if (w >= 6) {
      const u32x4 srd_we = mksrd(we, 261 * 256);
#pragma unroll
      for (int k = 0; k < 5; k++) LDB("ushort", cwv[k], tid * 2, srd_we, (256 + k) * 256 - 768);
      LDB("ushort", cwv[5], tid * 2 - 768, mksrd(wx, 256), 0);
    } else {
      LDB("ushort", cwv[0], tid * 2 - 512, srd_pos, n0 * 6);
    }
  }
  SB();
#pragma unroll
  for (int i = 0; i < 2; i++) *(u32x4*)(lds + L_OH + tid * 16 + i * 8192) = (u32x4)(0u);
  if (tid < (L_IMG - L_OH - 2 * 8192) / 16) *(u32x4*)(lds + L_OH + tid * 16 + 2 * 8192) = (u32x4)(0u);
  {
    char* ip = lds + L_IMG + (tid >> 4) * 528 + 256 + c16 * 16;
    *(u32x4*)ip = (u32x4)(0u);
    if (w < 4) *(u32x4*)(ip + 32 * 528) = (u32x4)(0u);
    else if (tid < 304) *(u32x4*)(lds + L_IMG + (tid - 256) * 528 + 512) = (u32x4)(0u);
  }
  u32x4 id0, id1;
  {
    const int k0 = n32 - 8 * hh;
    const u32 one = 0x3F80u << ((k0 & 1) * 16);
#pragma unroll
    for (int i = 0; i < 4; i++) {
      id0[i] = (k0 >> 1) == i ? one : 0u;
      id1[i] = (k0 >> 1) == 8 + i ? one : 0u;
    }
  }
  const int prow = (n32 & 19) | ((n32 & 4) << 1) | ((n32 & 8) >> 1);
  int ga[4];
  {
    const int sa = prow & 3, sb = prow >> 2;
#pragma unroll
    for (int s = 0; s < 4; s++) ga[s] = (L_STG + w * 8192 + (8 * sa + sb) * 128 + 16 * (hh ^ (2 * sa + ((sb >> 1) & 1)))) ^ (32 * s);
  }
  u32 go[4];
#pragma unroll
  for (int j = 0; j < 4; j++) go[j] = 4096 - 1024 * j + 16 * ((lane & 7) ^ (2 * j + ((lane >> 4) & 1)));
  int recoff = L_REC + prow * 16;
  int ohoff = L_OH + (n32 >> 2) * OHS + 16 * hh;
  int cvoff = L_CV + (8 * hh + (c16 >> 2)) * 8;
  int axoff = L_OH + (c16 >> 2) * OHS + 32 * (gq & 1) + 16 * (c16 & 1) + 8 * ((c16 >> 1) & 1);
  int srcoff = L_SRC + (lane >> 3) * 8;
  u32 vsrc = (NN + 32 * w + 4 * (lane >> 3)) * 0x10001u + 0x10000u;
  const u32 m0a = ldsb + L_STG + w * 8192, m0b = m0a + 4096;
  f32x16 Y[4];
#pragma unroll
  for (int t = 0; t < 4; t++) { Y[t] = (f32x16)(0.f); asm volatile("" : "+v"(Y[t])); }
  asm volatile("" : "+v"(id0), "+v"(id1), "+v"(ga[0]), "+v"(ga[1]), "+v"(ga[2]), "+v"(ga[3]), "+v"(go[0]), "+v"(go[1]), "+v"(go[2]), "+v"(go[3]));
  asm volatile("" : "+v"(recoff), "+v"(ohoff), "+v"(cvoff), "+v"(axoff), "+v"(srcoff), "+v"(vsrc));
  SB();
  int scnt, sinc;
  if (w < 4) {
    asm volatile("s_waitcnt vmcnt(7+" NPF_S ")" ::: "memory"); SB();
    scnt = tv >> 16;
    sinc = wave_incl_scan(scnt);
    if (lane == 63) ((u32*)(lds + L_WS))[w] = sinc;
  }
  STAMP(1, 1);
  __syncthreads();
  STAMP(1, 2);
  const u32x4 wsum = *(const u32x4*)(lds + L_WS);
  if (w >= 4) {
    asm volatile("s_waitcnt vmcnt(7+" NPF_S ")" ::: "memory"); SB();
    scnt = tv >> 16;
    sinc = wave_incl_scan(scnt);
  }
  int mtot, sexc;
  {
    const int w0 = __builtin_amdgcn_readfirstlane(wsum[0]), w1 = __builtin_amdgcn_readfirstlane(wsum[1]);
    const int w2 = __builtin_amdgcn_readfirstlane(wsum[2]), w3 = __builtin_amdgcn_readfirstlane(wsum[3]);
    mtot = w0 + w1 + w2 + w3;
    const int wq = w & 3;
    sexc = sinc - scnt + (wq > 0 ? w0 : 0) + (wq > 1 ? w1 : 0) + (wq > 2 ? w2 : 0);
  }
  const u32 sbase = ((tid & 255) * CH + (tv & 0xffffu)) * 12;
  bool general = mtot > CAP;
  if (!general) {
    u32* p = (u32*)(lds + L_CV) + sexc + (w >> 2);
    u32 v = sbase + (w >> 2) * 12;
#pragma clang loop vectorize(disable) unroll(disable)
    for (int i = w >> 2; i < scnt; i += 2, p += 2, v += 24) *p = v;
  }
  asm volatile("s_waitcnt vmcnt(" NPF_S ")" ::: "memory"); SB();
  asm volatile("" : "+v"(cwv[0]), "+v"(cwv[1]), "+v"(cwv[2]), "+v"(cwv[3]), "+v"(cwv[4]), "+v"(cwv[5]));
  {
    char* hp = lds + L_IMG + (tid >> 4) * 528 + c16 * 16;
    *(u32x4*)hp = hv[0];
    if (w < 4) *(u32x4*)(hp + 32 * 528) = hv[1];
  }
  if (w == 4 || w == 5) {
    const int t3 = tid - 256, nl = __umul24(t3, 21846) >> 16;
    if (t3 < 120) *(u16*)(lds + L_NODE + nl * 2 + t3 * 2) = (u16)cwv[0];
  }
  STAMP(1, 3);
  __syncthreads();
  STAMP(1, 4);
  u32x3 rec[3];
  {
    const u32x4 srd_rec = mksrd(ws + WS_R, NE * 12);
    u32 ro[3];
#pragma unroll
    for (int k = 0; k < 3; k++) ro[k] = ((const u32*)(lds + L_CV))[tid + 512 * k];
    const int lim = general ? 0 : mtot;
#pragma unroll
    for (int k = 0; k < 3; k++) LDB("dwordx3", rec[k], tid + 512 * k < lim ? ro[k] : 0x7fffff00u, srd_rec, 0);
  }
  SB();
  if (w >= 6) {
    const float c0 = bf2f(cwv[0]) * NLOG2E;
    const u32 d_hi = pk2(c0, 0.f);
    const u32x4 o = {pk2(bf2f(cwv[1]) * NLOG2E, bf2f(cwv[2]) * NLOG2E), pk2(bf2f(cwv[3]) * NLOG2E, bf2f(cwv[4]) * NLOG2E),
                     pk2(c0, c0 - bf2f(d_hi)), 0u};
    *(u32x4*)(lds + L_AT + (tid - 384) * 96 + 80) = o;
    ((float*)(lds + L_WX))[tid - 384] = bf2f(cwv[5]) * NLN2;
  }
  const char* const xb = lds + L_IMG + c16 * 528 + 16 * gq;
  {
    f32x4 acc[3];
#pragma unroll
    for (int rt = 0; rt < 3; rt++) acc[rt] = (f32x4)(0.f);
#pragma unroll
    for (int s = 0; s < 4; s++)
#pragma unroll
      for (int rt = 0; rt < 3; rt++) acc[rt] = MFMA16(asbf(*(const u32x4*)(xb + rt * 8448 + 64 * s)), asbf(w1f[s]), acc[rt]);
#pragma unroll
    for (int rt = 0; rt < 3; rt++)
      if (16 * rt + 4 * gq < 40) {
        const f32x4 a = acc[rt] * (NLOG2E / 1.0078125f);
        const u32x2 o = {pk2(a[0], a[1]), pk2(a[2], a[3])};
        *(u32x2*)(lds + L_AT + (16 * w + c16) * 96 + (16 * rt + 4 * gq) * 2) = o;
      }
  }
  SB();
  STAMP(1, 28);
  u32x4 wex[4];
  u32 ad[4];
  int ninc;
  auto prep = [&](u32x2 sv, bool real) {
    const u32 s0 = real ? sv[0] : vsrc, s1 = real ? sv[1] : vsrc + 0x20002u;
    asm("v_mad_u32_u16 %0, %1, %2, %3" : "=v"(ad[0]) : "v"(s0), "s"(256), "v"(go[0]));
    asm("v_mad_u32_u16 %0, %1, %2, %3 op_sel:[1,0,0,0]" : "=v"(ad[1]) : "v"(s0), "s"(256), "v"(go[1]));
    asm("v_mad_u32_u16 %0, %1, %2, %3" : "=v"(ad[2]) : "v"(s1), "s"(256), "v"(go[2]));
    asm("v_mad_u32_u16 %0, %1, %2, %3 op_sel:[1,0,0,0]" : "=v"(ad[3]) : "v"(s1), "s"(256), "v"(go[3]));
  };
  auto issue_a = [&]() { SETM0(m0a); DMA1(0, ad[0], srd_g, 0); DMA1(1024, ad[1], srd_g, 0); DMA1(2048, ad[2], srd_g, 0); DMA1(3072, ad[3], srd_g, 0); };
  auto issue_b = [&]() { SETM0(m0b); DMA1(0, ad[0], srd_g, 128); DMA1(1024, ad[1], srd_g, 128); DMA1(2048, ad[2], srd_g, 128); DMA1(3072, ad[3], srd_g, 128); };
  auto node_at = [&](int x) { return (int)__builtin_popcountll(__builtin_amdgcn_ballot_w64(ninc <= x)); };
  auto loadw = [&](int base) {
    const int g0 = base >> 3, bm = base & 7;
    const char* pa = lds + L_AT + n32 * 96 + (hh ? g0 * 16 : 80);
    const char* pb = lds + L_AT + n32 * 96 + (hh ? g0 * 16 + 16 : 80);
    const unsigned long long lo = ~0ull << (16 * min(bm, 3)) << (bm > 3 ? 16 : 0), hi = bm > 4 ? ~0ull << (16 * (bm - 4)) : ~0ull;
    const u32 mk[4] = {(u32)lo, (u32)(lo >> 32), (u32)hi, (u32)(hi >> 32)};
#pragma unroll
    for (int t = 0; t < 4; t++) {
      const u32x4 a = *(const u32x4*)(pa + t * 3072), bb = *(const u32x4*)(pb + t * 3072);
#pragma unroll
      for (int i = 0; i < 4; i++) wex[t][i] = (a[i] & mk[i]) | (bb[i] & ~mk[i]);
    }
  };
  auto pdot = [&](auto nbc) {
    constexpr int nb = decltype(nbc)::value;
    f32x4 p4[2] = {(f32x4)(0.f), (f32x4)(0.f)};
#pragma unroll
    for (int q0 = 0; q0 < 16; q0 += nb) {
      f32x4 wv[16];
#pragma unroll
      for (int q = q0; q < q0 + nb; q++) wv[q] = *(const f32x4*)(lds + L_WX + (8 * q + 4 * hh) * 4);
      SB();
#pragma unroll
      for (int q = q0; q < q0 + nb; q++)
        p4[q & 1] += (f32x4){Y[q >> 2][4 * (q & 3)], Y[q >> 2][4 * (q & 3) + 1], Y[q >> 2][4 * (q & 3) + 2], Y[q >> 2][4 * (q & 3) + 3]} * wv[q];
      SB();
    }
    p4[0] += p4[1];
    const f32x2 pa = (f32x2){p4[0][0], p4[0][1]} + (f32x2){p4[0][2], p4[0][3]};
    const u32 ph = __float_as_uint(pa[0] + pa[1]);
    const auto sw = __builtin_amdgcn_permlane32_swap(ph, ph, false, false);
    return __uint_as_float(sw[0]) + __uint_as_float(sw[1]);
  };
  auto place = [&](u32x3 rec, u32x2 ps, u32x2 pd, u32 dl, u32 p) {
    const u32 sh = (rec[2] & 1) * 16;
    const u32 sxy = __builtin_amdgcn_alignbit(ps[1], ps[0], sh), sz = ps[1] >> sh;
    const u32 dxy = pk2(bf2f(pd[0]) - bf2f(sxy), bfhi(pd[0]) - bfhi(sxy));
    const u32 dz1 = pk2(bf2f(pd[1]) - bf2f(sz), 1.0f);
    const float fx = bf2f(dxy), fy = bfhi(dxy), fz = bf2f(dz1);
    const float d2 = fx * fx + fy * fy + fz * fz;
    *(u32x4*)(lds + L_REC + p * 16) = (u32x4){rec[0], rec[1], pk2(d2, d2), dl};
    *(u32x2*)(lds + L_CV + p * 8) = (u32x2){mul16x2(dxy, OHINV * 0x10001u), mul16x2(dz1, OHINV * 0x10001u)};
    *(u16*)(lds + L_OH + (dl & 7) * OHS + 2 * p) = (u16)OH;
  };
  auto count_nodes = [&](int m) {
    const int cn = ((const u32*)(lds + L_CNT))[lane];
    ninc = wave_incl_scan(cn);
    if (w == 7) {
      const int p = m + n32 < ((m + 31) & ~31) ? m + n32 : CAP;
      *(u32x4*)(lds + L_REC + p * 16) = (u32x4)(0u);
      ((u16*)(lds + L_SRC))[p] = 0;
    }
    return ninc - cn;
  };
#define RDG(s, half) (*(const u32x4*)(lds + ga[s] + (half) * 4096))
#define EXP_(lo, hi) _Pragma("unroll") for (int r = lo; r < hi; r++) e[r] = __builtin_amdgcn_exp2f(xc[r]); SB()
#define RCP_(lo, hi) _Pragma("unroll") for (int r = lo; r < hi; r++) e[r] = __builtin_amdgcn_rcpf(e[r]); SB()
#define PACK_() _Pragma("unroll") for (int i = 0; i < 4; i++) { pa[i] = pk2(e[2 * i], e[2 * i + 1]); pb[i] = pk2(e[8 + 2 * i], e[8 + 2 * i + 1]); } \
  asm volatile("" : "+v"(pa), "+v"(pb)); SB()
#define SPROD(o, ca, cb) (u32x4){mul16x2(o[0], ca[0]), mul16x2(o[1], ca[1]), mul16x2(o[2], cb[0]), mul16x2(o[3], cb[1])}

  if (!general) {
    const int m = mtot;
    const bool third = w * 64 + 1024 < m;
    u32 rk[3], dl[3];
    u32x2 ps[3], pd[3];
    VMWAIT0(); SB();
    STAMP(1, 27);
#pragma unroll
    for (int k = 0; k < 3; k++)
      if (k < 2 || third) {
        LDB("dwordx2", ps[k], ((rec[k][2] & 0xffffu) * 6) & ~3u, srd_pos, 0);
        dl[k] = tid + 512 * k < m ? (rec[k][2] >> 16) - n0 : 48u + c16;
        rk[k] = atomicAdd((u32*)(lds + L_CNT) + dl[k], 1u);
        pd[k] = *(const u32x2*)(lds + L_NODE + dl[k] * 8);
      }
    STAMP(1, 5);
    __syncthreads();
    STAMP(1, 6);
    const int nst = count_nodes(m);
    const int nprev = __builtin_amdgcn_ds_bpermute(lane * 4 - 28, ninc);
#pragma unroll
    for (int k = 0; k < 3; k++)
      if (k < 2 || third) {
        rk[k] += __builtin_amdgcn_ds_bpermute(dl[k] * 4, nst);
        rk[k] = dl[k] < 48 ? rk[k] : CAP;
        *(u16*)(lds + L_SRC + 2 * rk[k]) = (u16)rec[k][2];
      }
    const int nt = (m + 31) >> 5, tq = nt >> 3, trm = nt & 7;
    const int tbeg = w * tq + min(w, trm);
    const int tend = tbeg + tq + (w < trm ? 1 : 0);
    general = m < 256 || __builtin_amdgcn_ballot_w64(lane >= 7 && lane < nn && ninc - nprev <= 30) != 0;
    if (!general) {
      srcoff += tbeg * 64;
      __syncthreads();
      STAMP(1, 29);
      const u32x2 sv0 = *(const u32x2*)(lds + srcoff), sv1 = *(const u32x2*)(lds + srcoff + 64);
      prep(sv0, true);
      if (w < 4) {
        issue_a();
        issue_b();
        VMWAIT(8);
      } else {
        VMWAIT(0);
      }
      SB();
      STAMP(1, 26);
      int base = node_at(tbeg * 32);
      loadw(base);
      const int lnode = node_at(min(tend * 32, m) - 1);
      const int hnode = (tbeg > 0 && node_at(tbeg * 32 - 1) == base) ? base : -1;
      if (hnode >= 0 && lane == 0) __hip_atomic_fetch_or((u32*)(lds + L_HM) + hnode, 1u << w, __ATOMIC_RELAXED, __HIP_MEMORY_SCOPE_WORKGROUP);
      recoff += tbeg * 512; cvoff += tbeg * 256; ohoff += tbeg * 64; axoff += tbeg * 64;
#pragma unroll
      for (int k = 0; k < 3; k++)
        if (k < 2 || third) place(rec[k], ps[k], pd[k], dl[k], rk[k]);
      if (w >= 4) {
        issue_a();
        issue_b();
      }
      prep(sv1, tbeg + 1 < tend);
#pragma unroll
      for (int t = 0; t < 4; t++) asm volatile("" : "+v"(wex[t]));
      auto flush = [&](int base, int lim, auto clearc) {
        const int node = base + (((n32 >> 2) - base) & 7), comp = n32 & 3;
        constexpr bool clear = decltype(clearc)::value != 0;
        const float pt = pdot(IC<(clear ? 2 : 16)>{});
        if (node < lim) {
          char* mp = lds + L_IMG + (node == hnode ? 39 + w : node) * 528;
          if (comp == 3) {
#pragma unroll
            for (int q = 0; q < 16; q++)
              *(u32x2*)(mp + 256 + 8 * hh + 16 * q) = (u32x2){pk2(Y[q >> 2][4 * (q & 3)], Y[q >> 2][4 * (q & 3) + 1]),
                                                              pk2(Y[q >> 2][4 * (q & 3) + 2], Y[q >> 2][4 * (q & 3) + 3])};
          } else {
            *(float*)(mp + 512 + 4 * comp) = pt;
          }
          if (clear) {
#pragma unroll
            for (int t = 0; t < 4; t++) Y[t] = (f32x16)(0.f);
          }
        }
      };
      STAMP(1, 7);
      __syncthreads();
      STAMP(1, 8);
      u32x4 aex, sp0 = (u32x4)(0u), sp1 = (u32x4)(0u), pa = (u32x4)(0u), pb = (u32x4)(0u);
      {
        const u32x2 t0 = trread(lds + axoff), t1 = trread(lds + axoff + 4 * OHS);
        const u32x4 ra = *(const u32x4*)(lds + recoff);
        aex = hh ? (u32x4){t0[0], t0[1], t1[0], t1[1]} : ra;
      }
      u32x2 c0 = trread(lds + cvoff), c1 = trread(lds + cvoff + 32), c2 = trread(lds + cvoff + 128), c3 = trread(lds + cvoff + 160);
      u32x4 o0 = *(const u32x4*)(lds + ohoff), o1 = *(const u32x4*)(lds + ohoff + 32);
      f32x16 XA, XB, e;
      XA = MFMA32(asbf(aex), asbf(wex[0]), (f32x16)(0.f)); PIN(XA);
      VMWAIT(4);
      STAMP(1, 23);
      u32x4 g0 = RDG(0, 0), g1 = RDG(1, 0), g2 = RDG(2, 0), g3 = RDG(3, 0);
      LGKMWAIT0();
      SB();
      XA = MFMA32(asbf(g0), asbf(id0), XA); PIN(XA);
      issue_a();
      XA = MFMA32(asbf(g1), asbf(id1), XA); PIN(XA);
      int fbase = base;
      __builtin_amdgcn_s_setprio(3);
      STAMP(1, 9);
      int tcount_ = 0;
      for (int left = tend - tbeg, x = tbeg * 32 + 32; left > 0; left--, x += 32) {
        STAMPV(1, 16 + min(tcount_, 6)); tcount_++;
        if (left == 3) __builtin_amdgcn_s_setprio(2);
        if (left == 2) __builtin_amdgcn_s_setprio(1);
        if (left == 1) __builtin_amdgcn_s_setprio(0);
        SB();
        {
          const f32x16 xc = XA;
          XB = MFMA32(asbf(aex), asbf(wex[1]), (f32x16)(0.f)); PIN(XB);
          EXP_(0, 8);
          XB = MFMA32(asbf(g2), asbf(id0), XB); PIN(XB);
          VMWAIT(4);
          g0 = RDG(0, 1); g1 = RDG(1, 1); g2 = RDG(2, 1); SB();
          EXP_(8, 16);
          e = e + 1.0f; SB();
          XB = MFMA32(asbf(g3), asbf(id1), XB); PIN(XB);
          g3 = RDG(3, 1); SB();
          RCP_(0, 8);
          Y[3] = MFMA32(asbf(pa), asbf(sp0), Y[3]); PIN(Y[3]);
          RCP_(8, 16);
          e = e * xc; SB();
          Y[3] = MFMA32(asbf(pb), asbf(sp1), Y[3]); PIN(Y[3]);
          PACK_();
          sp0 = SPROD(o0, c0, c1);
          sp1 = SPROD(o1, c2, c3);
          asm volatile("" : "+v"(sp0), "+v"(sp1)); SB();
        }
        if (fbase != base) {
          flush(fbase, base, IC<1>{});
          fbase = base;
        }
        SB();
        u32x2 sv;
        {
          const f32x16 xc = XB;
          LGKMWAIT0();
          XA = MFMA32(asbf(aex), asbf(wex[2]), (f32x16)(0.f)); PIN(XA);
          sv = *(const u32x2*)(lds + srcoff + 128); SB();
          EXP_(0, 4);
          SETM0(m0b); DMA1(0, ad[0], srd_g, 128); SB();
          EXP_(4, 8);
          XA = MFMA32(asbf(g0), asbf(id0), XA); PIN(XA);
          EXP_(8, 12);
          DMA1(1024, ad[1], srd_g, 128); SB();
          EXP_(12, 16);
          e = e + 1.0f; SB();
          XA = MFMA32(asbf(g1), asbf(id1), XA); PIN(XA);
          RCP_(0, 4);
          DMA1(2048, ad[2], srd_g, 128); SB();
          RCP_(4, 8);
          Y[0] = MFMA32(asbf(pa), asbf(sp0), Y[0]); PIN(Y[0]);
          RCP_(8, 12);
          DMA1(3072, ad[3], srd_g, 128); SB();
          RCP_(12, 16);
          e = e * xc; SB();
          Y[0] = MFMA32(asbf(pb), asbf(sp1), Y[0]); PIN(Y[0]);
          PACK_();
        }
        {
          const f32x16 xc = XA;
          XB = MFMA32(asbf(aex), asbf(wex[3]), (f32x16)(0.f)); PIN(XB);
          EXP_(0, 8);
          XB = MFMA32(asbf(g2), asbf(id0), XB); PIN(XB);
          EXP_(8, 16);
          e = e + 1.0f; SB();
          XB = MFMA32(asbf(g3), asbf(id1), XB); PIN(XB);
          RCP_(0, 8);
          Y[1] = MFMA32(asbf(pa), asbf(sp0), Y[1]); PIN(Y[1]);
          RCP_(8, 16);
          e = e * xc; SB();
          Y[1] = MFMA32(asbf(pb), asbf(sp1), Y[1]); PIN(Y[1]);
          PACK_();
          prep(sv, left > 2);
          asm volatile("" : "+v"(ad[0]), "+v"(ad[1]), "+v"(ad[2]), "+v"(ad[3])); SB();
        }
        if (left > 1 && node_at(min(x + 31, m - 1)) >= base + 8) {
          base = node_at(x);
          loadw(base);
        }
        SB();
        {
          const f32x16 xc = XB;
          VMWAIT(4);
          const u32x2 t0 = trread(lds + axoff + 64), t1 = trread(lds + axoff + 64 + 4 * OHS);
          const u32x4 ra = *(const u32x4*)(lds + recoff + 512);
          g0 = RDG(0, 0); g1 = RDG(1, 0); g2 = RDG(2, 0); g3 = RDG(3, 0);
          SB();
          EXP_(0, 8);
          Y[2] = MFMA32(asbf(pa), asbf(sp0), Y[2]); PIN(Y[2]);
          EXP_(8, 16);
          LGKMWAIT0();
          c0 = trread(lds + cvoff + 256); c1 = trread(lds + cvoff + 288);
          c2 = trread(lds + cvoff + 384); c3 = trread(lds + cvoff + 416);
          o0 = *(const u32x4*)(lds + ohoff + 64); o1 = *(const u32x4*)(lds + ohoff + 96);
          SB();
          aex = hh ? (u32x4){t0[0], t0[1], t1[0], t1[1]} : ra;
          PIN(aex);
          e = e + 1.0f; SB();
          XA = MFMA32(asbf(aex), asbf(wex[0]), (f32x16)(0.f)); PIN(XA);
          SETM0(m0a); DMA1(0, ad[0], srd_g, 0); SB();
          RCP_(0, 4);
          XA = MFMA32(asbf(g0), asbf(id0), XA); PIN(XA);
          DMA1(1024, ad[1], srd_g, 0); SB();
          RCP_(4, 8);
          XA = MFMA32(asbf(g1), asbf(id1), XA); PIN(XA);
          DMA1(2048, ad[2], srd_g, 0); SB();
          RCP_(8, 12);
          DMA1(3072, ad[3], srd_g, 0); SB();
          RCP_(12, 16);
          e = e * xc; SB();
          Y[2] = MFMA32(asbf(pb), asbf(sp1), Y[2]); PIN(Y[2]);
          PACK_();
          srcoff += 64; recoff += 512; cvoff += 256; ohoff += 64; axoff += 64;
        }
      }
      Y[3] = MFMA32(asbf(pa), asbf(sp0), Y[3]);
      Y[3] = MFMA32(asbf(pb), asbf(sp1), Y[3]);
      STAMP(1, 10);
      flush(base, lnode + 1, IC<0>{});
      STAMP(1, 11);
    } else {
      asm volatile("s_waitcnt vmcnt(0)" :: "v"(ps[0]), "v"(ps[1]), "v"(ps[2]) : "memory");
    }
  }
  if (general) {
    asm volatile("s_waitcnt vmcnt(0)" :: "v"(rec[0]), "v"(rec[1]), "v"(rec[2]) : "memory");
    const rsrc_t rs_rec = mkrs(ws + WS_R, NE * 12), rs_pos = mkrs(pos, NN * 6);
    for (int q = tid; q < 40 * 33; q += 512) *(u32x4*)(lds + L_MG + q * 16) = (u32x4)(0u);
    for (int r0 = 0; r0 < mtot; r0 += CAP) {
      const int m = min(mtot - r0, CAP);
      __syncthreads();
      for (int q = tid; q < (L_HM - L_OH) / 16; q += 512) *(u32x4*)(lds + L_OH + q * 16) = (u32x4)(0u);
      const int lim = min(scnt, r0 + CAP - sexc);
      for (int i = max(r0 - sexc, 0) + (w >> 2); i < lim; i += 2)
        *(u32x3*)(lds + L_STG + (sexc + i - r0) * 16) = __builtin_amdgcn_raw_buffer_load_b96(rs_rec, sbase + i * 12, 0, 0);
      __syncthreads();
      for (int f = tid; f < m; f += 512) {
        u32* rp = (u32*)(lds + L_STG + f * 16);
        rp[3] = atomicAdd((u32*)(lds + L_CNT) + ((rp[2] >> 16) - n0), 1u);
      }
      __syncthreads();
      const int nst = count_nodes(m);
      for (int f0 = w * 64; f0 < m; f0 += 512) {
        const bool ok = f0 + lane < m;
        const u32x4 rc = *(const u32x4*)(lds + L_STG + (f0 + lane) * 16);
        const u32 dl = ok ? (rc[2] >> 16) - n0 : 0u;
        const u32 pp = rc[3] + __builtin_amdgcn_ds_bpermute(dl * 4, nst), p = ok ? pp : CAP;
        const u32x2 ps = __builtin_amdgcn_raw_buffer_load_b64(rs_pos, ((rc[2] & 0xffffu) * 6) & ~3u, 0, 0);
        *(u16*)(lds + L_SRC + 2 * p) = (u16)rc[2];
        place((u32x3){rc[0], rc[1], rc[2]}, ps, *(const u32x2*)(lds + L_NODE + dl * 8), dl, p);
      }
      __syncthreads();
      const int nt = w < 4 ? (m + 31) >> 5 : 0;
      for (int tile = w; tile < nt; tile += 4) {
        const int last = node_at(min(tile * 32 + 31, m - 1));
        prep(*(const u32x2*)(lds + srcoff + tile * 64), true);
        issue_a();
        issue_b();
        const u32x2 t0 = trread(lds + axoff + tile * 64), t1 = trread(lds + axoff + tile * 64 + 4 * OHS);
        const u32x4 ra = *(const u32x4*)(lds + recoff + tile * 512);
        const u32x4 aex = hh ? (u32x4){t0[0], t0[1], t1[0], t1[1]} : ra;
        for (int base = node_at(tile * 32); base <= last; base += 8) {
          loadw(base);
          u32x4 sp[2];
#pragma unroll
          for (int s = 0; s < 2; s++) {
            const u32x2 c0 = trread(lds + cvoff + tile * 256 + s * 128), c1 = trread(lds + cvoff + tile * 256 + s * 128 + 32);
            const u32x4 oh = *(const u32x4*)(lds + ohoff + tile * 64 + 32 * s);
            sp[s] = SPROD(oh, c0, c1);
          }
#pragma unroll 1
          for (int j = 0; j < 16; j++) {
            const int dn = *(const u32*)(lds + L_REC + (tile * 32 + 16 * (j >> 3) + 8 * hh + (j & 7)) * 16 + 12);
            const u32 mk = (dn < base || dn >= base + 8) ? ((j & 1) ? 0x0000ffffu : 0xffff0000u) : 0xffffffffu;
#pragma unroll
            for (int i = 0; i < 8; i++) sp[i >> 2][i & 3] &= (i == (j >> 1)) ? mk : 0xffffffffu;
          }
          VMWAIT0();
#pragma unroll
          for (int t = 0; t < 4; t++) {
            const u32x4 ga_ = RDG((2 * t) & 3, t >> 1), gb_ = RDG((2 * t + 1) & 3, t >> 1);
            f32x16 xc = MFMA32(asbf(aex), asbf(wex[t]), (f32x16)(0.f)), e;
            xc = MFMA32(asbf(ga_), asbf(id0), xc);
            xc = MFMA32(asbf(gb_), asbf(id1), xc);
            u32x4 pa, pb;
            EXP_(0, 16);
            e = e + 1.0f;
            RCP_(0, 16);
            e = e * xc;
            PACK_();
            Y[t] = MFMA32(asbf(pa), asbf(sp[0]), (f32x16)(0.f));
            Y[t] = MFMA32(asbf(pb), asbf(sp[1]), Y[t]);
          }
          const int node = base + (((n32 >> 2) - base) & 7), comp = n32 & 3;
          const float pt = pdot(IC<4>{});
          if (node <= last) {
            float* mp = (float*)(lds + L_MG) + node * 132;
            if (comp == 3) {
#pragma unroll
              for (int t = 0; t < 4; t++)
#pragma unroll
                for (int r = 0; r < 16; r++) atomicAdd(mp + 4 * hh + 32 * t + 8 * (r >> 2) + (r & 3), Y[t][r]);
            } else if (hh == 0) {
              atomicAdd(mp + 128 + comp, pt);
            }
          }
        }
        LGKMWAIT0();
      }
    }
    __syncthreads();
    for (int q = tid; q < 40 * 66; q += 512) {
      const int row = q / 66, c = q - row * 66;
      const f32x2 v = *(const f32x2*)(lds + L_MG + row * 528 + c * 8);
      if (c < 64) *(u32*)(lds + L_IMG + row * 528 + 256 + c * 4) = pk2(v[0], v[1]);
      else *(f32x2*)(lds + L_IMG + row * 528 + c * 8) = v;
    }
    __syncthreads();
    prep((u32x2){0u, 0u}, false);
    issue_a();
    issue_b();
  }

  f32x4 acc[3];
  const char* const wfp = lds + L_STG + w * 8192 + lane * 16;
  VMWAIT0();
#pragma unroll
  for (int rt = 0; rt < 3; rt++) acc[rt] = (f32x4)(0.f);
#pragma unroll
  for (int s = 0; s < 4; s++) {
    const u32x4 wf = *(const u32x4*)(wfp + s * 1024);
#pragma unroll
    for (int rt = 0; rt < 3; rt++) acc[rt] = MFMA16(asbf(wf), asbf(*(const u32x4*)(xb + rt * 8448 + 64 * s)), acc[rt]);
  }
  u32x4 wf2[4];
  u32x2 hr[3];
  u32 hm[3];
  const char* xp[3];
#pragma unroll
  for (int s = 0; s < 4; s++) wf2[s] = *(const u32x4*)(wfp + 4096 + s * 1024);
#pragma unroll
  for (int rt = 0; rt < 3; rt++) {
    hr[rt] = *(const u32x2*)(lds + L_IMG + c16 * 528 + 8 * gq + rt * 8448 + 32 * w);
    hm[rt] = 16 * rt + c16 < 40 ? ((const u32*)(lds + L_HM))[16 * rt + c16] : 0u;
    xp[rt] = lds + L_IMG + 256 + 16 * gq + (39 + __builtin_ctz(hm[rt] | 256u)) * 528;
    hm[rt] &= hm[rt] - 1;
  }
  STAMP(1, 12);
  __syncthreads();
  STAMP(1, 13);
  {
    u32x4 xf[2][3][4];
#pragma unroll
    for (int s = 0; s < 4; s++)
#pragma unroll
      for (int rt = 0; rt < 3; rt++) {
        xf[0][rt][s] = *(const u32x4*)(xb + 256 + rt * 8448 + 64 * s);
        xf[1][rt][s] = *(const u32x4*)(xp[rt] + 64 * s);
      }
    SB();
#pragma unroll
    for (int s = 0; s < 4; s++)
#pragma unroll
      for (int j = 0; j < 2; j++)
#pragma unroll
        for (int rt = 0; rt < 3; rt++) acc[rt] = MFMA16(asbf(wf2[s]), asbf(xf[j][rt][s]), acc[rt]);
  }
  while (__builtin_amdgcn_ballot_w64((hm[0] | hm[1] | hm[2]) != 0) != 0) {
#pragma unroll
    for (int rt = 0; rt < 3; rt++) {
      const char* p = lds + L_IMG + 256 + 16 * gq + (39 + __builtin_ctz(hm[rt] | 256u)) * 528;
      hm[rt] &= hm[rt] - 1;
#pragma unroll
      for (int s = 0; s < 4; s++) acc[rt] = MFMA16(asbf(wf2[s]), asbf(*(const u32x4*)(p + 64 * s)), acc[rt]);
    }
  }
  STAMP(1, 14);
  {
    const rsrc_t rs_o = mkrs(out + (size_t)n0 * 128, nn * 256);
#pragma unroll
    for (int rt = 0; rt < 3; rt++) {
      float o[4];
#pragma unroll
      for (int r = 0; r < 4; r++) {
        const float z = acc[rt][r];
        o[r] = ((r & 1) ? bfhi(hr[rt][r >> 1]) : bf2f(hr[rt][r >> 1])) + z * __builtin_amdgcn_rcpf(1.0f + __builtin_amdgcn_exp2f(z * NLOG2E));
      }
      __builtin_amdgcn_raw_buffer_store_b64((u32x2){pk2(o[0], o[1]), pk2(o[2], o[3])}, rs_o, c16 * 256 + 8 * gq, rt * 4096 + 32 * w, 0);
    }
  }
  if (w < 2) {
    const int nl = __umul24(tid, 21846) >> 16, k = tid - 3 * nl;
    if (tid < 3 * nn) {
      const char* mp = lds + L_IMG + 512 + 4 * k;
      u32 hq = ((const u32*)(lds + L_HM))[nl];
      float pv = *(const float*)(mp + nl * 528) + bf2f(*(const u16*)(lds + L_NODE + nl * 2 + tid * 2));
      while (hq) {
        pv += *(const float*)(mp + (39 + __builtin_ctz(hq)) * 528);
        hq &= hq - 1;
      }
      out[(size_t)NN * 128 + n0 * 3 + tid] = (u16)pk2(pv, 0.f);
    }
  }
  DRAIN();
  STAMP(1, 15); RSTAMP(1, 31);
}

extern "C" void kernel_launch(void* const* d_in, const int* in_sizes, int n_in, void* d_out, int out_size,
                              void* d_ws, size_t ws_size, hipStream_t stream) {
  const u16* h = (const u16*)d_in[0];
  const u16* pos = (const u16*)d_in[1];
  const u16* ea = (const u16*)d_in[2];
  const int* src = (const int*)d_in[3];
  const int* dst = (const int*)d_in[4];
  const u16* we = (const u16*)d_in[5];
  const u16* wx = (const u16*)d_in[6];
  const u16* wh = (const u16*)d_in[7];
  egnn_k1<<<NCH, 640, 0, stream>>>(h, ea, src, dst, we, wh, (char*)d_ws);
  egnn_k2<<<NB, 512, 0, stream>>>(h, pos, we, wx, (const char*)d_ws, (u16*)d_out);
}
```

```cpp
#include <hip/hip_runtime.h>
#include <hip/hip_bf16.h>

typedef unsigned short u16;
typedef unsigned int u32;
typedef float f32x2 __attribute__((ext_vector_type(2)));
typedef float f32x4 __attribute__((ext_vector_type(4)));
typedef float f32x16 __attribute__((ext_vector_type(16)));
typedef __bf16 bf16x8 __attribute__((ext_vector_type(8)));
typedef __bf16 bf16x2 __attribute__((ext_vector_type(2)));
typedef u32 u32x2 __attribute__((ext_vector_type(2)));
typedef u32 u32x3 __attribute__((ext_vector_type(3)));
typedef u32 u32x4 __attribute__((ext_vector_type(4)));
typedef int i32x4 __attribute__((ext_vector_type(4)));
typedef short s16x4 __attribute__((ext_vector_type(4)));
typedef u16 u16x2 __attribute__((ext_vector_type(2)));
#define LAS __attribute__((address_space(3)))
typedef __amdgpu_buffer_rsrc_t rsrc_t;

constexpr int NN = 10000;
constexpr int NE = 320000;
constexpr int NCH = 250;
constexpr int CH = 1280;
constexpr int NB = 256;
constexpr int CAP = 1408;
constexpr float NLOG2E = -1.4426950408889634f;
constexpr float NLN2 = -0.6931471805599453f;
constexpr u32 OH = 0x3F81u;
constexpr u32 OHINV = 0x81u;

constexpr size_t WS_T = 0;
constexpr size_t WS_R = WS_T + (size_t)NB * 256 * 4;
constexpr size_t WS_B = WS_R + (size_t)NE * 12;

__device__ __forceinline__ float bf2f(u32 v) { return __uint_as_float(v << 16); }
__device__ __forceinline__ float bfhi(u32 v) { return __uint_as_float(v & 0xffff0000u); }
__device__ __forceinline__ u32 pk2(float a, float b) {
  f32x2 v = {a, b};
  return __builtin_bit_cast(u32, __builtin_convertvector(v, bf16x2));
}
__device__ __forceinline__ u32 mul16x2(u32 a, u32 b) {
  return __builtin_bit_cast(u32, __builtin_bit_cast(u16x2, a) * __builtin_bit_cast(u16x2, b));
}
__device__ __forceinline__ int wave_incl_scan(int x) {
  x += __builtin_amdgcn_update_dpp(0, x, 0x111, 0xf, 0xf, false);
  x += __builtin_amdgcn_update_dpp(0, x, 0x112, 0xf, 0xf, false);
  x += __builtin_amdgcn_update_dpp(0, x, 0x114, 0xf, 0xf, false);
  x += __builtin_amdgcn_update_dpp(0, x, 0x118, 0xf, 0xf, false);
  x += __builtin_amdgcn_update_dpp(0, x, 0x142, 0xa, 0xf, false);
  x += __builtin_amdgcn_update_dpp(0, x, 0x143, 0xc, 0xf, false);
  return x;
}
__device__ __forceinline__ bf16x8 asbf(u32x4 v) { return __builtin_bit_cast(bf16x8, v); }
__device__ __forceinline__ u32x2 trread(const char* p) {
  return __builtin_bit_cast(u32x2, __builtin_amdgcn_ds_read_tr16_b64_v4i16((LAS s16x4*)p));
}
__device__ __forceinline__ rsrc_t mkrs(const void* p, u32 bytes) { return __builtin_amdgcn_make_buffer_rsrc((void*)p, 0, bytes, 0x00020000); }
__device__ __forceinline__ void dma16(rsrc_t rs, char* ldsp, u32 voff, u32 soff) {
  __builtin_amdgcn_raw_ptr_buffer_load_lds(rs, (LAS void*)ldsp, 16, voff, soff, 0, 0);
}
#define MFMA32(a, b, c) __builtin_amdgcn_mfma_f32_32x32x16_bf16(a, b, c, 0, 0, 0)
#define MFMA16(a, b, c) __builtin_amdgcn_mfma_f32_16x16x32_bf16(a, b, c, 0, 0, 0)
#define SB() __builtin_amdgcn_sched_barrier(0)
#define VMWAIT0() asm volatile("s_waitcnt vmcnt(0)" ::: "memory")
#define STAMPDECL
#define STAMP(buf, i)
#define STAMPV(buf, i)
#define RSTAMP(buf, i)
#define DRAIN()

template <int V> struct IC { static constexpr int value = V; };
__device__ __forceinline__ u32x4 mksrd(const void* p, u32 bytes) {
  const unsigned long long a = (unsigned long long)p;
  return (u32x4){(u32)a, (u32)(a >> 32) & 0xffffu, bytes, 0x00020000u};
}
#define DMA1(J, A, SRD, SOFF) asm volatile("buffer_load_dwordx4 %0, %1, %2 offen offset:" #J " lds" :: "v"(A), "s"(SRD), "s"(SOFF) : "memory")
#define LDB(T, DST, VO, SRD, SO) asm volatile("buffer_load_" T " %0, %1, %2, %3 offen" : "=v"(DST) : "v"(VO), "s"(SRD), "s"(SO) : "memory")
#define SETM0(V) asm volatile("s_mov_b32 m0, %0\n\ts_nop 0" :: "s"(V) : "memory")
#define VMWAIT(N) asm volatile("s_waitcnt vmcnt(" #N ")" ::: "memory")
#define LGKMWAIT0() asm volatile("s_waitcnt lgkmcnt(0)" ::: "memory")
#define PIN(x) do { asm volatile("" : "+v"(x)); SB(); } while (0)

constexpr int K1_H = 32768;
constexpr int K1_IMG = K1_H + 48 * 256;
constexpr int K1_HIST = K1_IMG + 16384;
constexpr int K1_LDS = K1_HIST + 2 * NB * 4 + 32;

__global__ __launch_bounds__(512) void egnn_k1(const u16* __restrict__ h, const u16* __restrict__ ea,
                                               const int* __restrict__ src, const int* __restrict__ dst,
                                               const u16* __restrict__ we, char* __restrict__ ws) {
  __shared__ __attribute__((aligned(128))) char lds[K1_LDS];
  const int tid = threadIdx.x, lane = tid & 63;
  const int w = __builtin_amdgcn_readfirstlane(tid >> 6);
  const int c = (blockIdx.x & 7) * 32 + (blockIdx.x >> 3);
  const int gq = lane >> 4, c16 = lane & 15;
  u32* const hist = (u32*)(lds + K1_HIST);
  asm volatile("" :: "s"(h), "s"(ea), "s"(src), "s"(dst), "s"(we), "s"(ws));
  STAMPDECL; RSTAMP(0, 30); STAMP(0, 0);
  if (tid < NB) hist[tid] = 0;
  else if (tid < NB + 8) hist[NB + tid] = 0;
  __syncthreads();
  auto meet = [&](int k) {
    LGKMWAIT0();
    if (lane == 0) __hip_atomic_fetch_add(&hist[2 * NB + k], 1u, __ATOMIC_RELAXED, __HIP_MEMORY_SCOPE_WORKGROUP);
    while (__hip_atomic_load(&hist[2 * NB + k], __ATOMIC_RELAXED, __HIP_MEMORY_SCOPE_WORKGROUP) < 4u) __builtin_amdgcn_s_sleep(1);
    asm volatile("" ::: "memory");
  };
  if (w < 4) {
    const u32 eo = tid * 20, nb = c < NCH ? CH * 4 : 0;
    const rsrc_t rs_d = mkrs(dst + c * CH, nb), rs_s = mkrs(src + c * CH, nb), rs_a = mkrs(ea + (size_t)c * CH * 4, 2 * nb);
    const u32x4 d4 = __builtin_amdgcn_raw_buffer_load_b128(rs_d, eo, 0, 0);
    const u32 d1 = __builtin_amdgcn_raw_buffer_load_b32(rs_d, eo + 16, 0, 0);
    const u32x4 s4 = __builtin_amdgcn_raw_buffer_load_b128(rs_s, eo, 0, 0);
    const u32 s1 = __builtin_amdgcn_raw_buffer_load_b32(rs_s, eo + 16, 0, 0);
    const u32x4 a0 = __builtin_amdgcn_raw_buffer_load_b128(rs_a, 2 * eo, 0, 0);
    const u32x4 a1 = __builtin_amdgcn_raw_buffer_load_b128(rs_a, 2 * eo + 16, 0, 0);
    const u32x2 a2 = __builtin_amdgcn_raw_buffer_load_b64(rs_a, 2 * eo + 32, 0, 0);
    __builtin_amdgcn_s_setprio(1);
    STAMP(0, 1);
    if (c < NCH) {
      u32* const img = (u32*)(lds + K1_IMG);
      u32* const base = hist + NB;
      u32 bk[5], rk[5];
#pragma unroll
      for (int i = 0; i < 5; i++) {
        bk[i] = __umul24(i < 4 ? d4[i & 3] : d1, 214749u) >> 23;
        rk[i] = atomicAdd(&hist[bk[i]], 1u);
      }
      STAMP(0, 2);
      meet(1);
      STAMP(0, 3);
      {
        const u32x4 v = *(const u32x4*)(hist + 4 * lane);
        const int s = v[0] + v[1] + v[2] + v[3];
        const int ex = wave_incl_scan(s) - s;
        const u32x4 bs = {(u32)ex, ex + v[0], ex + v[0] + v[1], ex + v[0] + v[1] + v[2]};
        *(u32x4*)(base + 4 * lane) = bs;
        const u32 bb = w == 0 ? bs[0] : w == 1 ? bs[1] : w == 2 ? bs[2] : bs[3];
        const u32 vv = w == 0 ? v[0] : w == 1 ? v[1] : w == 2 ? v[2] : v[3];
        ((u32*)(ws + WS_T))[(4 * lane + w) * 256 + c] = bb | (vv << 16);
      }
#pragma unroll
      for (int i = 0; i < 5; i++) {
        u32* p = img + 3 * (base[bk[i]] + rk[i]);
        p[0] = i < 2 ? a0[2 * (i & 1)] : i < 4 ? a1[2 * (i & 1)] : a2[0];
        p[1] = i < 2 ? a0[2 * (i & 1) + 1] : i < 4 ? a1[2 * (i & 1) + 1] : a2[1];
        p[2] = (i < 4 ? s4[i & 3] : s1) | ((i < 4 ? d4[i & 3] : d1) << 16);
      }
      STAMP(0, 4);
      meet(2);
      STAMP(0, 5);
      const rsrc_t rs_r = mkrs(ws + WS_R + (size_t)c * (CH * 12), CH * 12);
#pragma unroll
      for (int i = 0; i < 4; i++)
        __builtin_amdgcn_raw_buffer_store_b128(((const u32x4*)img)[tid + 256 * i], rs_r, tid * 16 + i * 4096, 0, 0);
    }
    STAMP(0, 6); DRAIN(); STAMP(0, 7); RSTAMP(0, 31);
  } else {
    const int u = w - 4, n0 = (c * 625 + 15) >> 4, nn = (((c + 1) * 625 + 15) >> 4) - n0;
    const u32 ldsb = (u32)(size_t)(LAS char*)lds;
    {
      const u32x4 srd_h = mksrd(h + (size_t)n0 * 128, nn * 256), srd_w = mksrd(we + 128 * 128, 32768);
      const u32 vw = gq * 256 + 16 * (c16 ^ (4 * gq));
      SETM0(ldsb + u * 8192);
      DMA1(0, vw, srd_w, u * 8192); DMA1(1024, vw, srd_w, u * 8192); DMA1(2048, vw, srd_w, u * 8192); DMA1(3072, vw, srd_w, u * 8192);
      SETM0(ldsb + u * 8192 + 4096);
      DMA1(0, vw, srd_w, u * 8192 + 4096); DMA1(1024, vw, srd_w, u * 8192 + 4096); DMA1(2048, vw, srd_w, u * 8192 + 4096); DMA1(3072, vw, srd_w, u * 8192 + 4096);
      SETM0(ldsb + K1_H + u * 3072);
      DMA1(0, gq * 256 + 16 * (c16 ^ ((12 * u + gq) & 15)), srd_h, u * 3072);
      DMA1(1024, gq * 256 + 16 * (c16 ^ ((12 * u + 4 + gq) & 15)), srd_h, u * 3072);
      DMA1(2048, gq * 256 + 16 * (c16 ^ ((12 * u + 8 + gq) & 15)), srd_h, u * 3072);
    }
    STAMP(0, 1);
    VMWAIT0();
    STAMP(0, 2);
    meet(0);
    STAMP(0, 3);
    f32x4 acc[3][2];
#pragma unroll
    for (int rt = 0; rt < 3; rt++) acc[rt][0] = acc[rt][1] = (f32x4)(0.f);
    const char* wb = lds + (8 * gq + (c16 >> 2)) * 256 + 16 * ((4 * u + (c16 & 3)) ^ (c16 & 12));
    const int hb = K1_H + c16 * 256 + 16 * (gq ^ c16);
    u32x4 fa[4], fb[4], hf[4][3];
#pragma unroll
    for (int s = 0; s < 4; s++) {
      const u32x2 x0 = trread(wb + (32 * s) * 256), x1 = trread(wb + (32 * s + 4) * 256);
      const u32x2 y0 = trread(wb + (32 * s) * 256 + 8), y1 = trread(wb + (32 * s + 4) * 256 + 8);
      fa[s] = (u32x4){x0[0], x0[1], x1[0], x1[1]};
      fb[s] = (u32x4){y0[0], y0[1], y1[0], y1[1]};
#pragma unroll
      for (int rt = 0; rt < 3; rt++) hf[s][rt] = *(const u32x4*)(lds + (hb ^ (64 * s)) + rt * 4096);
    }
    SB();
#pragma unroll
    for (int s = 0; s < 4; s++)
#pragma unroll
      for (int rt = 0; rt < 3; rt++) {
        acc[rt][0] = MFMA16(asbf(fa[s]), asbf(hf[s][rt]), acc[rt][0]);
        acc[rt][1] = MFMA16(asbf(fb[s]), asbf(hf[s][rt]), acc[rt][1]);
      }
    const rsrc_t rs_b = mkrs(ws + WS_B + (size_t)n0 * 256, nn * 256);
#pragma unroll
    for (int rt = 0; rt < 3; rt++) {
      const f32x4 a = acc[rt][0] * NLOG2E, b = acc[rt][1] * NLOG2E;
      const u32x4 o = {pk2(a[0], a[1]), pk2(a[2], a[3]), pk2(b[0], b[1]), pk2(b[2], b[3])};
      __builtin_amdgcn_raw_buffer_store_b128(o, rs_b, c16 * 256 + 16 * gq + 64 * u + rt * 4096, 0, 0);
    }
    STAMP(0, 6); DRAIN(); STAMP(0, 7); RSTAMP(0, 31);
  }
}

constexpr int OHS = 2 * CAP + 16;
constexpr int L_NODE = 0;
constexpr int L_WX = L_NODE + 320;
constexpr int L_WS = L_WX + 512;
constexpr int L_REC = L_WS + 16;
constexpr int L_CV = L_REC + 16 * (CAP + 1);
constexpr int L_SRC = L_CV + 8 * (CAP + 2);
constexpr int L_OH = L_SRC + 2 * (CAP + 8);
constexpr int L_CNT = L_OH + 8 * OHS;
constexpr int L_HM = L_CNT + 256;
constexpr int L_IMG = L_HM + 160;
constexpr int L_AT = L_IMG + 48 * 528;
constexpr int L_STG = (L_AT + 128 * 96 + 127) & ~127;
constexpr int K2_LDS = L_STG + 65536;
constexpr int L_MG = L_STG + 32768;
static_assert(K2_LDS <= 163840, "LDS");
static_assert(L_CV % 16 == 0 && L_OH % 16 == 0 && L_SRC % 16 == 0 && L_IMG % 16 == 0 && L_AT % 16 == 0, "align");
static_assert(L_IMG - L_OH > 2 * 8192 && L_IMG - L_OH <= 3 * 8192, "zero sweep");

__global__ __launch_bounds__(512, 2) void egnn_k2(const u16* __restrict__ h, const u16* __restrict__ pos,
                                                  const u16* __restrict__ we, const u16* __restrict__ wx,
                                                  const u16* __restrict__ wh, const char* __restrict__ ws,
                                                  u16* __restrict__ out) {
  __shared__ __attribute__((aligned(128))) char lds[K2_LDS];
  const int tid = threadIdx.x, lane = tid & 63;
  const int w = __builtin_amdgcn_readfirstlane(tid >> 6);
  const int b = (blockIdx.x & 7) * 32 + (blockIdx.x >> 3);
  const int n0 = (b * 625 + 15) >> 4, nn = (((b + 1) * 625 + 15) >> 4) - n0;
  const int gq = lane >> 4, c16 = lane & 15, hh = lane >> 5, n32 = lane & 31;
  const u32 ldsb = (u32)(size_t)(LAS char*)lds;
  const u32x4 srd_g = mksrd(ws + WS_B - 4096, 4096 + NN * 256), srd_pos = mksrd(pos, NN * 6);

  asm volatile("" :: "s"(h), "s"(pos), "s"(we), "s"(wx), "s"(wh), "s"(ws), "s"(out));
  STAMPDECL; RSTAMP(1, 30); STAMP(1, 0);
  u32 cwv[6], tv;
  u32x4 hv[2];
  LDB("dword", tv, (tid & 255) * 4, mksrd(ws + WS_T + b * 1024, NCH * 4), 0);
  {
    const u32x4 srd_h = mksrd(h + (size_t)n0 * 128, nn * 256);
    LDB("dwordx4", hv[0], tid * 16, srd_h, 0);
    LDB("dwordx4", hv[1], tid * 16, srd_h, 8192);
    const u32x4 srd_we = mksrd(we, 261 * 256);
    const u32 vw = gq * 256 + 16 * (c16 ^ (4 * gq));
    SETM0(ldsb + L_STG + w * 4096);
    DMA1(0, vw, srd_we, w * 4096); DMA1(1024, vw, srd_we, w * 4096); DMA1(2048, vw, srd_we, w * 4096); DMA1(3072, vw, srd_we, w * 4096);
    if (w >= 6) {
#pragma unroll
      for (int k = 0; k < 5; k++) LDB("ushort", cwv[k], tid * 2, srd_we, (256 + k) * 256 - 768);
      LDB("ushort", cwv[5], tid * 2 - 768, mksrd(wx, 256), 0);
    } else {
      LDB("ushort", cwv[0], tid * 2 - 512, srd_pos, n0 * 6);
    }
  }
  SB();
#pragma unroll
  for (int i = 0; i < 2; i++) *(u32x4*)(lds + L_OH + tid * 16 + i * 8192) = (u32x4)(0u);
  if (tid < (L_IMG - L_OH - 2 * 8192) / 16) *(u32x4*)(lds + L_OH + tid * 16 + 2 * 8192) = (u32x4)(0u);
  {
    char* ip = lds + L_IMG + (tid >> 4) * 528 + 256 + c16 * 16;
    *(u32x4*)ip = (u32x4)(0u);
    if (w < 4) *(u32x4*)(ip + 32 * 528) = (u32x4)(0u);
    else if (tid < 304) *(u32x4*)(lds + L_IMG + (tid - 256) * 528 + 512) = (u32x4)(0u);
  }
  u32x4 id0, id1, one4 = {hh ? 0u : 0x3F80u, 0u, 0u, 0u};
  {
    const int k0 = n32 - 8 * hh;
    const u32 one = 0x3F80u << ((k0 & 1) * 16);
#pragma unroll
    for (int i = 0; i < 4; i++) {
      id0[i] = (k0 >> 1) == i ? one : 0u;
      id1[i] = (k0 >> 1) == 8 + i ? one : 0u;
    }
  }
  const int prow = (n32 & 19) | ((n32 & 4) << 1) | ((n32 & 8) >> 1);
  int ga[4];
  {
    const int sa = prow & 3, sb = prow >> 2;
#pragma unroll
    for (int s = 0; s < 4; s++) ga[s] = (L_STG + w * 8192 + (8 * sa + sb) * 128 + 16 * (hh ^ (2 * sa + ((sb >> 1) & 1)))) ^ (32 * s);
  }
  u32 go[4];
#pragma unroll
  for (int j = 0; j < 4; j++) go[j] = 4096 - 1024 * j + 16 * ((lane & 7) ^ (2 * j + ((lane >> 4) & 1)));
  int recoff = L_REC + prow * 16;
  int ohoff = L_OH + (n32 >> 2) * OHS + 16 * hh;
  int cvoff = L_CV + (8 * hh + (c16 >> 2)) * 8;
  int axoff = L_OH + (c16 >> 2) * OHS + 32 * (gq & 1) + 16 * (c16 & 1) + 8 * ((c16 >> 1) & 1);
  int srcoff = L_SRC + (lane >> 3) * 8;
  u32 vsrc = (lane >> 1) * 256 + 32 * w + 16 * (lane & 1);
  const u32 m0a = ldsb + L_STG + w * 8192, m0b = m0a + 4096;
  asm volatile("" : "+v"(one4), "+v"(id0), "+v"(id1), "+v"(ga[0]), "+v"(ga[1]), "+v"(ga[2]), "+v"(ga[3]), "+v"(go[0]), "+v"(go[1]), "+v"(go[2]), "+v"(go[3]));
  asm volatile("" : "+v"(recoff), "+v"(ohoff), "+v"(cvoff), "+v"(axoff), "+v"(srcoff), "+v"(vsrc));
  SB();
  int scnt, sinc;
  if (w < 4) {
    asm volatile("s_waitcnt vmcnt(7)" ::: "memory"); SB();
    scnt = tv >> 16;
    sinc = wave_incl_scan(scnt);
    if (lane == 63) ((u32*)(lds + L_WS))[w] = sinc;
  }
  STAMP(1, 1);
  __syncthreads();
  STAMP(1, 2);
  const u32x4 wsum = *(const u32x4*)(lds + L_WS);
  if (w >= 4) {
    asm volatile("s_waitcnt vmcnt(7)" ::: "memory"); SB();
    scnt = tv >> 16;
    sinc = wave_incl_scan(scnt);
  }
  int mtot, sexc;
  {
    const int w0 = __builtin_amdgcn_readfirstlane(wsum[0]), w1 = __builtin_amdgcn_readfirstlane(wsum[1]);
    const int w2 = __builtin_amdgcn_readfirstlane(wsum[2]), w3 = __builtin_amdgcn_readfirstlane(wsum[3]);
    mtot = w0 + w1 + w2 + w3;
    const int wq = w & 3;
    sexc = sinc - scnt + (wq > 0 ? w0 : 0) + (wq > 1 ? w1 : 0) + (wq > 2 ? w2 : 0);
  }
  const u32 sbase = ((tid & 255) * CH + (tv & 0xffffu)) * 12;
  bool general = mtot > CAP;
  if (!general) {
    u32* p = (u32*)(lds + L_CV) + sexc + (w >> 2);
    u32 v = sbase + (w >> 2) * 12;
#pragma clang loop vectorize(disable) unroll(disable)
    for (int i = w >> 2; i < scnt; i += 2, p += 2, v += 24) *p = v;
  }
  asm volatile("s_waitcnt vmcnt(0)" ::: "memory"); SB();
  asm volatile("" : "+v"(cwv[0]), "+v"(cwv[1]), "+v"(cwv[2]), "+v"(cwv[3]), "+v"(cwv[4]), "+v"(cwv[5]));
  {
    char* hp = lds + L_IMG + (tid >> 4) * 528 + c16 * 16;
    *(u32x4*)hp = hv[0];
    if (w < 4) *(u32x4*)(hp + 32 * 528) = hv[1];
  }
  if (w == 4 || w == 5) {
    const int t3 = tid - 256, nl = __umul24(t3, 21846) >> 16;
    if (t3 < 120) *(u16*)(lds + L_NODE + nl * 2 + t3 * 2) = (u16)cwv[0];
  }
  STAMP(1, 3);
  __syncthreads();
  STAMP(1, 4);
  u32x3 rec[3];
  {
    const u32x4 srd_rec = mksrd(ws + WS_R, NE * 12);
    u32 ro[3];
#pragma unroll
    for (int k = 0; k < 3; k++) ro[k] = ((const u32*)(lds + L_CV))[tid + 512 * k];
    const int lim = general ? 0 : mtot;
#pragma unroll
    for (int k = 0; k < 3; k++) LDB("dwordx3", rec[k], tid + 512 * k < lim ? ro[k] : 0x7fffff00u, srd_rec, 0);
  }
  SB();
  if (w >= 6) {
    const float c0 = bf2f(cwv[0]) * NLOG2E;
    const u32 d_hi = pk2(c0, 0.f);
    const u32x4 o = {pk2(bf2f(cwv[1]) * NLOG2E, bf2f(cwv[2]) * NLOG2E), pk2(bf2f(cwv[3]) * NLOG2E, bf2f(cwv[4]) * NLOG2E),
                     pk2(c0, c0 - bf2f(d_hi)), 0u};
    *(u32x4*)(lds + L_AT + (tid - 384) * 96 + 80) = o;
    ((float*)(lds + L_WX))[tid - 384] = bf2f(cwv[5]) * NLN2;
  }
  const char* const xb = lds + L_IMG + c16 * 528 + 16 * gq;
  {
    f32x4 acc[3];
#pragma unroll
    for (int rt = 0; rt < 3; rt++) acc[rt] = (f32x4)(0.f);
    const char* wb = lds + L_STG + (8 * gq + (c16 >> 2)) * 256 + 16 * ((2 * w + ((c16 >> 1) & 1)) ^ (c16 & 12)) + 8 * (c16 & 1);
    u32x4 wf[4], hf[4][3];
#pragma unroll
    for (int s = 0; s < 4; s++) {
      const u32x2 x0 = trread(wb + (32 * s) * 256), x1 = trread(wb + (32 * s + 4) * 256);
      wf[s] = (u32x4){x0[0], x0[1], x1[0], x1[1]};
#pragma unroll
      for (int rt = 0; rt < 3; rt++) hf[s][rt] = *(const u32x4*)(xb + rt * 8448 + 64 * s);
    }
    SB();
#pragma unroll
    for (int s = 0; s < 4; s++)
#pragma unroll
      for (int rt = 0; rt < 3; rt++) acc[rt] = MFMA16(asbf(hf[s][rt]), asbf(wf[s]), acc[rt]);
#pragma unroll
    for (int rt = 0; rt < 3; rt++)
      if (16 * rt + 4 * gq < 40) {
        const f32x4 a = acc[rt] * (NLOG2E / 1.0078125f);
        const u32x2 o = {pk2(a[0], a[1]), pk2(a[2], a[3])};
        *(u32x2*)(lds + L_AT + (16 * w + c16) * 96 + (16 * rt + 4 * gq) * 2) = o;
      }
  }
  f32x16 Y[4], XA, XB, e;
  u32x4 sp0 = (u32x4)(0u), sp1 = (u32x4)(0u), pa = (u32x4)(0u), pb = (u32x4)(0u);
  asm volatile("" : "+v"(sp0), "+v"(sp1), "+v"(pa), "+v"(pb));
  Y[0] = MFMA32(asbf(pa), asbf(pa), (f32x16)(0.f));
  Y[1] = MFMA32(asbf(pb), asbf(pb), (f32x16)(0.f));
  Y[2] = MFMA32(asbf(sp0), asbf(sp0), (f32x16)(0.f));
  Y[3] = MFMA32(asbf(sp1), asbf(sp1), (f32x16)(0.f));
  XB = MFMA32(asbf(pa), asbf(pb), (f32x16)(0.f));
  asm volatile("" : "+v"(Y[0]), "+v"(Y[1]), "+v"(Y[2]), "+v"(Y[3]), "+v"(XB));
  SB();
  STAMP(1, 28);
  u32x4 wex[4];
  u32 ad[4];
  int ninc;
  u32x4 srd = srd_g;
  u32 sfb = 128;
  auto prep = [&](u32x2 sv, bool real) {
    if (real) {
      asm("v_mad_u32_u16 %0, %1, %2, %3" : "=v"(ad[0]) : "v"(sv[0]), "s"(256), "v"(go[0]));
      asm("v_mad_u32_u16 %0, %1, %2, %3 op_sel:[1,0,0,0]" : "=v"(ad[1]) : "v"(sv[0]), "s"(256), "v"(go[1]));
      asm("v_mad_u32_u16 %0, %1, %2, %3" : "=v"(ad[2]) : "v"(sv[1]), "s"(256), "v"(go[2]));
      asm("v_mad_u32_u16 %0, %1, %2, %3 op_sel:[1,0,0,0]" : "=v"(ad[3]) : "v"(sv[1]), "s"(256), "v"(go[3]));
    } else {
#pragma unroll
      for (int j = 0; j < 4; j++) ad[j] = vsrc + 7168 * j;
      srd = mksrd(wh, 65536);
      sfb = 32768;
    }
  };
  auto issue_a = [&]() { SETM0(m0a); DMA1(0, ad[0], srd, 0); DMA1(1024, ad[1], srd, 0); DMA1(2048, ad[2], srd, 0); DMA1(3072, ad[3], srd, 0); };
  auto issue_b = [&]() { SETM0(m0b); DMA1(0, ad[0], srd, sfb); DMA1(1024, ad[1], srd, sfb); DMA1(2048, ad[2], srd, sfb); DMA1(3072, ad[3], srd, sfb); };
  auto node_at = [&](int x) { return (int)__builtin_popcountll(__builtin_amdgcn_ballot_w64(ninc <= x)); };
  auto loadw = [&](int base) {
    const int g0 = base >> 3, bm = base & 7;
    const char* pa = lds + L_AT + n32 * 96 + (hh ? g0 * 16 : 80);
    const char* pb = lds + L_AT + n32 * 96 + (hh ? g0 * 16 + 16 : 80);
    const unsigned long long lo = ~0ull << (16 * min(bm, 3)) << (bm > 3 ? 16 : 0), hi = bm > 4 ? ~0ull << (16 * (bm - 4)) : ~0ull;
    const u32 mk[4] = {(u32)lo, (u32)(lo >> 32), (u32)hi, (u32)(hi >> 32)};
#pragma unroll
    for (int t = 0; t < 4; t++) {
      const u32x4 a = *(const u32x4*)(pa + t * 3072), bb = *(const u32x4*)(pb + t * 3072);
#pragma unroll
      for (int i = 0; i < 4; i++) wex[t][i] = (a[i] & mk[i]) | (bb[i] & ~mk[i]);
    }
  };
  auto pdot = [&](auto nbc) {
    constexpr int nb = decltype(nbc)::value;
    f32x4 p4[2] = {(f32x4)(0.f), (f32x4)(0.f)};
#pragma unroll
    for (int q0 = 0; q0 < 16; q0 += nb) {
      f32x4 wv[16];
#pragma unroll
      for (int q = q0; q < q0 + nb; q++) wv[q] = *(const f32x4*)(lds + L_WX + (8 * q + 4 * hh) * 4);
      SB();
#pragma unroll
      for (int q = q0; q < q0 + nb; q++)
        p4[q & 1] += (f32x4){Y[q >> 2][4 * (q & 3)], Y[q >> 2][4 * (q & 3) + 1], Y[q >> 2][4 * (q & 3) + 2], Y[q >> 2][4 * (q & 3) + 3]} * wv[q];
      SB();
    }
    p4[0] += p4[1];
    const f32x2 pa = (f32x2){p4[0][0], p4[0][1]} + (f32x2){p4[0][2], p4[0][3]};
    const u32 ph = __float_as_uint(pa[0] + pa[1]);
    const auto sw = __builtin_amdgcn_permlane32_swap(ph, ph, false, false);
    return __uint_as_float(sw[0]) + __uint_as_float(sw[1]);
  };
  auto place = [&](u32x3 rec, u32x2 ps, u32x2 pd, u32 dl, u32 p) {
    const u32 sh = (rec[2] & 1) * 16;
    const u32 sxy = __builtin_amdgcn_alignbit(ps[1], ps[0], sh), sz = ps[1] >> sh;
    const u32 dxy = pk2(bf2f(pd[0]) - bf2f(sxy), bfhi(pd[0]) - bfhi(sxy));
    const u32 dz1 = pk2(bf2f(pd[1]) - bf2f(sz), 1.0f);
    const float fx = bf2f(dxy), fy = bfhi(dxy), fz = bf2f(dz1);
    const float d2 = fx * fx + fy * fy + fz * fz;
    *(u32x4*)(lds + L_REC + p * 16) = (u32x4){rec[0], rec[1], pk2(d2, d2), dl};
    *(u32x2*)(lds + L_CV + p * 8) = (u32x2){mul16x2(dxy, OHINV * 0x10001u), mul16x2(dz1, OHINV * 0x10001u)};
    *(u16*)(lds + L_OH + (dl & 7) * OHS + 2 * p) = (u16)OH;
  };
  auto count_nodes = [&](int m) {
    const int cn = ((const u32*)(lds + L_CNT))[lane];
    ninc = wave_incl_scan(cn);
    if (w == 7) {
      const int p = m + n32 < ((m + 31) & ~31) ? m + n32 : CAP;
      *(u32x4*)(lds + L_REC + p * 16) = (u32x4)(0u);
      ((u16*)(lds + L_SRC))[p] = 0;
    }
    return ninc - cn;
  };
#define RDG(s, half) (*(const u32x4*)(lds + ga[s] + (half) * 4096))
#define EXP_(lo, hi) _Pragma("unroll") for (int r = lo; r < hi; r++) e[r] = __builtin_amdgcn_exp2f(xc[r]); SB()
#define RCP_(lo, hi) _Pragma("unroll") for (int r = lo; r < hi; r++) e[r] = __builtin_amdgcn_rcpf(e[r]); SB()
#define PACK_() _Pragma("unroll") for (int i = 0; i < 4; i++) { pa[i] = pk2(e[2 * i], e[2 * i + 1]); pb[i] = pk2(e[8 + 2 * i], e[8 + 2 * i + 1]); } \
  asm volatile("" : "+v"(pa), "+v"(pb)); SB()
#define SPROD(o, ca, cb) (u32x4){mul16x2(o[0], ca[0]), mul16x2(o[1], ca[1]), mul16x2(o[2], cb[0]), mul16x2(o[3], cb[1])}

  if (!general) {
    const int m = mtot;
    const bool third = w * 64 + 1024 < m;
    u32 rk[3], dl[3];
    u32x2 ps[3], pd[3];
    VMWAIT0(); SB();
    STAMP(1, 27);
#pragma unroll
    for (int k = 0; k < 3; k++)
      if (k < 2 || third) {
        LDB("dwordx2", ps[k], ((rec[k][2] & 0xffffu) * 6) & ~3u, srd_pos, 0);
        dl[k] = tid + 512 * k < m ? (rec[k][2] >> 16) - n0 : 48u + c16;
        rk[k] = atomicAdd((u32*)(lds + L_CNT) + dl[k], 1u);
        pd[k] = *(const u32x2*)(lds + L_NODE + dl[k] * 8);
      }
    STAMP(1, 5);
    __syncthreads();
    STAMP(1, 6);
    const int nst = count_nodes(m);
    const int nprev = __builtin_amdgcn_ds_bpermute(lane * 4 - 28, ninc);
#pragma unroll
    for (int k = 0; k < 3; k++)
      if (k < 2 || third) {
        rk[k] += __builtin_amdgcn_ds_bpermute(dl[k] * 4, nst);
        rk[k] = dl[k] < 48 ? rk[k] : CAP;
        *(u16*)(lds + L_SRC + 2 * rk[k]) = (u16)rec[k][2];
      }
    const int nt = (m + 31) >> 5, tq = nt >> 3, trm = nt & 7;
    const int tbeg = w * tq + min(w, trm);
    const int tend = tbeg + tq + (w < trm ? 1 : 0);
    general = m < 256 || __builtin_amdgcn_ballot_w64(lane >= 7 && lane < nn && ninc - nprev <= 30) != 0;
    if (!general) {
      srcoff += tbeg * 64;
      __syncthreads();
      STAMP(1, 29);
      const u32x2 sv0 = *(const u32x2*)(lds + srcoff), sv1 = *(const u32x2*)(lds + srcoff + 64);
      prep(sv0, true);
      if (w < 4) {
        issue_a();
        issue_b();
        VMWAIT(8);
      } else {
        VMWAIT(0);
      }
      SB();
      STAMP(1, 26);
      int base = node_at(tbeg * 32);
      loadw(base);
      const int lnode = node_at(min(tend * 32, m) - 1);
      const int hnode = (tbeg > 0 && node_at(tbeg * 32 - 1) == base) ? base : -1;
      if (hnode >= 0 && lane == 0) __hip_atomic_fetch_or((u32*)(lds + L_HM) + hnode, 1u << w, __ATOMIC_RELAXED, __HIP_MEMORY_SCOPE_WORKGROUP);
      recoff += tbeg * 512; cvoff += tbeg * 256 - 256; ohoff += tbeg * 64 - 64; axoff += tbeg * 64;
#pragma unroll
      for (int k = 0; k < 3; k++)
        if (k < 2 || third) place(rec[k], ps[k], pd[k], dl[k], rk[k]);
      if (w >= 4) {
        issue_a();
        issue_b();
      }
      prep(sv1, tbeg + 1 < tend);
#pragma unroll
      for (int t = 0; t < 4; t++) asm volatile("" : "+v"(wex[t]));
      auto flush = [&](int base, int lim, auto clearc) {
        const int node = base + (((n32 >> 2) - base) & 7), comp = n32 & 3;
        constexpr bool clear = decltype(clearc)::value != 0;
        const float pt = pdot(IC<(clear ? 2 : 16)>{});
        if (node < lim) {
          char* mp = lds + L_IMG + (node == hnode ? 39 + w : node) * 528;
          if (comp == 3) {
#pragma unroll
            for (int q = 0; q < 16; q++)
              *(u32x2*)(mp + 256 + 8 * hh + 16 * q) = (u32x2){pk2(Y[q >> 2][4 * (q & 3)], Y[q >> 2][4 * (q & 3) + 1]),
                                                              pk2(Y[q >> 2][4 * (q & 3) + 2], Y[q >> 2][4 * (q & 3) + 3])};
          } else {
            *(float*)(mp + 512 + 4 * comp) = pt;
          }
          if (clear) {
#pragma unroll
            for (int t = 0; t < 4; t++) Y[t] = (f32x16)(0.f);
          }
        }
      };
      STAMP(1, 7);
      __syncthreads();
      STAMP(1, 8);
      u32x4 aex;
      {
        const u32x2 t0 = trread(lds + axoff), t1 = trread(lds + axoff + 4 * OHS);
        const u32x4 ra = *(const u32x4*)(lds + recoff);
        aex = hh ? (u32x4){t0[0], t0[1], t1[0], t1[1]} : ra;
      }
      XA = MFMA32(asbf(aex), asbf(wex[0]), (f32x16)(0.f)); PIN(XA);
      VMWAIT(4);
      STAMP(1, 23);
      u32x4 g0 = RDG(0, 0), g1 = RDG(1, 0), g2 = RDG(2, 0), g3 = RDG(3, 0);
      LGKMWAIT0();
      SB();
      XA = MFMA32(asbf(g0), asbf(id0), XA); PIN(XA);
      issue_a();
      XA = MFMA32(asbf(g1), asbf(id1), XA); PIN(XA);
      int fbase = base;
      __builtin_amdgcn_s_setprio(3);
      STAMP(1, 9);
      int tcount_ = 0;
#define EXPX(X, lo, hi) _Pragma("unroll") for (int r = lo; r < hi; r++) e[r] = __builtin_amdgcn_exp2f(X[r]); SB()
#define PACKX(X) _Pragma("unroll") for (int i = 0; i < 4; i++) { pa[i] = pk2(X[2 * i], X[2 * i + 1]); pb[i] = pk2(X[8 + 2 * i], X[8 + 2 * i + 1]); } \
  asm volatile("" : "+v"(pa), "+v"(pb)); SB()
#define SEG(t, p, sp) Y[t] = MFMA32(asbf(p), asbf(sp), Y[t]); PIN(Y[t])
#define PLUS1() e = MFMA32(asbf(one4), asbf(one4), e); PIN(e)
      for (int left = tend - tbeg, x = tbeg * 32 + 32; left > 0; left--, x += 32) {
        STAMPV(1, 16 + min(tcount_, 4)); tcount_++;
        if (__builtin_expect(left == 1, 0)) __builtin_amdgcn_s_setprio(1);
        SB();
        SEG(2, pa, sp0);
        EXPX(XA, 0, 8);
        SEG(2, pb, sp1);
        EXPX(XA, 8, 16);
        PLUS1();
        PACKX(XB);
        srcoff += 64; recoff += 512; cvoff += 256; ohoff += 64; axoff += 64;
        asm volatile("" : "+v"(srcoff), "+v"(recoff), "+v"(cvoff), "+v"(ohoff), "+v"(axoff)); SB();
        RCP_(0, 4);
        XB = MFMA32(asbf(aex), asbf(wex[1]), (f32x16)(0.f)); PIN(XB);
        RCP_(4, 8);
        XB = MFMA32(asbf(g2), asbf(id0), XB); PIN(XB);
        RCP_(8, 12);
        XB = MFMA32(asbf(g3), asbf(id1), XB); PIN(XB);
        RCP_(12, 16);
        XA = XA * e; PIN(XA);
        SEG(3, pa, sp0);
        VMWAIT(4);
        g0 = RDG(0, 1); g1 = RDG(1, 1); g2 = RDG(2, 1); g3 = RDG(3, 1);
        const u32x2 sv = *(const u32x2*)(lds + srcoff + 64);
        SB();
        EXPX(XB, 0, 8);
        SEG(3, pb, sp1);
        const u32x2 c0 = trread(lds + cvoff), c1 = trread(lds + cvoff + 32), c2 = trread(lds + cvoff + 128), c3 = trread(lds + cvoff + 160);
        const u32x4 o0 = *(const u32x4*)(lds + ohoff), o1 = *(const u32x4*)(lds + ohoff + 32);
        SB();
        EXPX(XB, 8, 16);
        PLUS1();
        PACKX(XA);
        sp0 = SPROD(o0, c0, c1);
        sp1 = SPROD(o1, c2, c3);
        asm volatile("" : "+v"(sp0), "+v"(sp1));
        LGKMWAIT0();
        SB();
        SETM0(m0b); DMA1(0, ad[0], srd, sfb); SB();
        RCP_(0, 4);
        XA = MFMA32(asbf(aex), asbf(wex[2]), (f32x16)(0.f)); PIN(XA);
        DMA1(1024, ad[1], srd, sfb); SB();
        RCP_(4, 8);
        XA = MFMA32(asbf(g0), asbf(id0), XA); PIN(XA);
        DMA1(2048, ad[2], srd, sfb); SB();
        RCP_(8, 12);
        XA = MFMA32(asbf(g1), asbf(id1), XA); PIN(XA);
        DMA1(3072, ad[3], srd, sfb); SB();
        RCP_(12, 16);
        XB = XB * e; PIN(XB);
        if (__builtin_expect(fbase != base, 0)) {
          flush(fbase, base, IC<1>{});
          fbase = base;
        }
        if (__builtin_expect(left <= 2, 0)) {
          if (left == 2) __builtin_amdgcn_s_setprio(2);
          else __builtin_amdgcn_s_setprio(0);
          prep(sv, false);
        } else {
          prep(sv, true);
        }
        asm volatile("" : "+v"(ad[0]), "+v"(ad[1]), "+v"(ad[2]), "+v"(ad[3]));
        SB();
        SEG(0, pa, sp0);
        const int nlast = node_at(min(x + 31, m - 1));
        SB();
        EXPX(XA, 0, 8);
        SEG(0, pb, sp1);
        EXPX(XA, 8, 16);
        PLUS1();
        PACKX(XB);
        RCP_(0, 4);
        XB = MFMA32(asbf(aex), asbf(wex[3]), (f32x16)(0.f)); PIN(XB);
        RCP_(4, 8);
        XB = MFMA32(asbf(g2), asbf(id0), XB); PIN(XB);
        RCP_(8, 12);
        XB = MFMA32(asbf(g3), asbf(id1), XB); PIN(XB);
        RCP_(12, 16);
        XA = XA * e; PIN(XA);
        if (__builtin_expect(left > 1 && nlast >= base + 8, 0)) {
          base = node_at(x);
          loadw(base);
        }
        SB();
        SEG(1, pa, sp0);
        VMWAIT(4);
        const u32x2 t0 = trread(lds + axoff), t1 = trread(lds + axoff + 4 * OHS);
        const u32x4 ra = *(const u32x4*)(lds + recoff);
        g0 = RDG(0, 0); g1 = RDG(1, 0); g2 = RDG(2, 0); g3 = RDG(3, 0);
        SB();
        EXPX(XB, 0, 8);
        SEG(1, pb, sp1);
        EXPX(XB, 8, 16);
        PLUS1();
        PACKX(XA);
        LGKMWAIT0();
        SB();
        aex = hh ? (u32x4){t0[0], t0[1], t1[0], t1[1]} : ra;
        PIN(aex);
        SETM0(m0a); DMA1(0, ad[0], srd, 0); SB();
        RCP_(0, 4);
        XA = MFMA32(asbf(aex), asbf(wex[0]), (f32x16)(0.f)); PIN(XA);
        DMA1(1024, ad[1], srd, 0); SB();
        RCP_(4, 8);
        XA = MFMA32(asbf(g0), asbf(id0), XA); PIN(XA);
        DMA1(2048, ad[2], srd, 0); SB();
        RCP_(8, 12);
        XA = MFMA32(asbf(g1), asbf(id1), XA); PIN(XA);
        DMA1(3072, ad[3], srd, 0); SB();
        RCP_(12, 16);
        XB = XB * e; PIN(XB);
      }
      SEG(2, pa, sp0);
      SEG(2, pb, sp1);
      PACKX(XB);
      SEG(3, pa, sp0);
      SEG(3, pb, sp1);
      STAMP(1, 10);
      flush(base, lnode + 1, IC<0>{});
      STAMP(1, 11);
    } else {
      asm volatile("s_waitcnt vmcnt(0)" :: "v"(ps[0]), "v"(ps[1]), "v"(ps[2]) : "memory");
    }
  } else {
    asm volatile("s_waitcnt vmcnt(0)" :: "v"(rec[0]), "v"(rec[1]), "v"(rec[2]) : "memory");
  }
  if (general) {
    const rsrc_t rs_rec = mkrs(ws + WS_R, NE * 12), rs_pos = mkrs(pos, NN * 6);
    for (int q = tid; q < 40 * 33; q += 512) *(u32x4*)(lds + L_MG + q * 16) = (u32x4)(0u);
    for (int r0 = 0; r0 < mtot; r0 += CAP) {
      const int m = min(mtot - r0, CAP);
      __syncthreads();
      for (int q = tid; q < (L_HM - L_OH) / 16; q += 512) *(u32x4*)(lds + L_OH + q * 16) = (u32x4)(0u);
      const int lim = min(scnt, r0 + CAP - sexc);
      for (int i = max(r0 - sexc, 0) + (w >> 2); i < lim; i += 2)
        *(u32x3*)(lds + L_STG + (sexc + i - r0) * 16) = __builtin_amdgcn_raw_buffer_load_b96(rs_rec, sbase + i * 12, 0, 0);
      __syncthreads();
      for (int f = tid; f < m; f += 512) {
        u32* rp = (u32*)(lds + L_STG + f * 16);
        rp[3] = atomicAdd((u32*)(lds + L_CNT) + ((rp[2] >> 16) - n0), 1u);
      }
      __syncthreads();
      const int nst = count_nodes(m);
      for (int f0 = w * 64; f0 < m; f0 += 512) {
        const bool ok = f0 + lane < m;
        const u32x4 rc = *(const u32x4*)(lds + L_STG + (f0 + lane) * 16);
        const u32 dl = ok ? (rc[2] >> 16) - n0 : 0u;
        const u32 pp = rc[3] + __builtin_amdgcn_ds_bpermute(dl * 4, nst), p = ok ? pp : CAP;
        const u32x2 ps = __builtin_amdgcn_raw_buffer_load_b64(rs_pos, ((rc[2] & 0xffffu) * 6) & ~3u, 0, 0);
        *(u16*)(lds + L_SRC + 2 * p) = (u16)rc[2];
        place((u32x3){rc[0], rc[1], rc[2]}, ps, *(const u32x2*)(lds + L_NODE + dl * 8), dl, p);
      }
      __syncthreads();
      const int nt = w < 4 ? (m + 31) >> 5 : 0;
      for (int tile = w; tile < nt; tile += 4) {
        const int last = node_at(min(tile * 32 + 31, m - 1));
        prep(*(const u32x2*)(lds + srcoff + tile * 64), true);
        SETM0(m0a); DMA1(0, ad[0], srd_g, 0); DMA1(1024, ad[1], srd_g, 0); DMA1(2048, ad[2], srd_g, 0); DMA1(3072, ad[3], srd_g, 0);
        SETM0(m0b); DMA1(0, ad[0], srd_g, 128); DMA1(1024, ad[1], srd_g, 128); DMA1(2048, ad[2], srd_g, 128); DMA1(3072, ad[3], srd_g, 128);
        const u32x2 t0 = trread(lds + axoff + tile * 64), t1 = trread(lds + axoff + tile * 64 + 4 * OHS);
        const u32x4 ra = *(const u32x4*)(lds + recoff + tile * 512);
        const u32x4 aex = hh ? (u32x4){t0[0], t0[1], t1[0], t1[1]} : ra;
        for (int base = node_at(tile * 32); base <= last; base += 8) {
          loadw(base);
          u32x4 sp[2];
#pragma unroll
          for (int s = 0; s < 2; s++) {
            const u32x2 c0 = trread(lds + cvoff + tile * 256 + s * 128), c1 = trread(lds + cvoff + tile * 256 + s * 128 + 32);
            const u32x4 oh = *(const u32x4*)(lds + ohoff + tile * 64 + 32 * s);
            sp[s] = SPROD(oh, c0, c1);
          }
#pragma unroll 1
          for (int j = 0; j < 16; j++) {
            const int dn = *(const u32*)(lds + L_REC + (tile * 32 + 16 * (j >> 3) + 8 * hh + (j & 7)) * 16 + 12);
            const u32 mk = (dn < base || dn >= base + 8) ? ((j & 1) ? 0x0000ffffu : 0xffff0000u) : 0xffffffffu;
#pragma unroll
            for (int i = 0; i < 8; i++) sp[i >> 2][i & 3] &= (i == (j >> 1)) ? mk : 0xffffffffu;
          }
          VMWAIT0();
#pragma unroll
          for (int t = 0; t < 4; t++) {
            const u32x4 ga_ = RDG((2 * t) & 3, t >> 1), gb_ = RDG((2 * t + 1) & 3, t >> 1);
            f32x16 xc = MFMA32(asbf(aex), asbf(wex[t]), (f32x16)(0.f)), e;
            xc = MFMA32(asbf(ga_), asbf(id0), xc);
            xc = MFMA32(asbf(gb_), asbf(id1), xc);
            u32x4 pa, pb;
            EXP_(0, 16);
            e = e + 1.0f;
            RCP_(0, 16);
            e = e * xc;
            PACK_();
            Y[t] = MFMA32(asbf(pa), asbf(sp[0]), (f32x16)(0.f));
            Y[t] = MFMA32(asbf(pb), asbf(sp[1]), Y[t]);
          }
          const int node = base + (((n32 >> 2) - base) & 7), comp = n32 & 3;
          const float pt = pdot(IC<4>{});
          if (node <= last) {
            float* mp = (float*)(lds + L_MG) + node * 132;
            if (comp == 3) {
#pragma unroll
              for (int t = 0; t < 4; t++)
#pragma unroll
                for (int r = 0; r < 16; r++) atomicAdd(mp + 4 * hh + 32 * t + 8 * (r >> 2) + (r & 3), Y[t][r]);
            } else if (hh == 0) {
              atomicAdd(mp + 128 + comp, pt);
            }
          }
        }
        LGKMWAIT0();
      }
    }
    __syncthreads();
    for (int q = tid; q < 40 * 66; q += 512) {
      const int row = q / 66, c = q - row * 66;
      const f32x2 v = *(const f32x2*)(lds + L_MG + row * 528 + c * 8);
      if (c < 64) *(u32*)(lds + L_IMG + row * 528 + 256 + c * 4) = pk2(v[0], v[1]);
      else *(f32x2*)(lds + L_IMG + row * 528 + c * 8) = v;
    }
    __syncthreads();
    const u32x4 srd_wh = mksrd(wh, 65536);
    SETM0(m0a); DMA1(0, vsrc, srd_wh, 0); DMA1(1024, vsrc, srd_wh, 7168); DMA1(2048, vsrc, srd_wh, 14336); DMA1(3072, vsrc, srd_wh, 21504);
    SETM0(m0b); DMA1(0, vsrc, srd_wh, 32768); DMA1(1024, vsrc, srd_wh, 39936); DMA1(2048, vsrc, srd_wh, 47104); DMA1(3072, vsrc, srd_wh, 54272);
    VMWAIT0();
  }

  int tid2 = threadIdx.x;
  asm volatile("" : "+v"(tid2));
  const int c16e = tid2 & 15, gqe = (tid2 >> 4) & 3;
  const char* const xbe = lds + L_IMG + c16e * 528 + 16 * gqe;
  f32x4 acc[3], acm[3];
  const char* const wfp = lds + L_STG + w * 8192 + (8 * gqe + (c16e >> 2)) * 32 + 8 * (c16e & 3);
  VMWAIT(4);
  u32x4 wf2[4];
#pragma unroll
  for (int rt = 0; rt < 3; rt++) acc[rt] = acm[rt] = (f32x4)(0.f);
  {
    u32x4 wf1[4], xf[4][3];
#pragma unroll
    for (int s = 0; s < 4; s++) {
      const u32x2 x0 = trread(wfp + s * 1024), x1 = trread(wfp + s * 1024 + 128);
      const u32x2 y0 = trread(wfp + 4096 + s * 1024), y1 = trread(wfp + 4096 + s * 1024 + 128);
      wf1[s] = (u32x4){x0[0], x0[1], x1[0], x1[1]};
      wf2[s] = (u32x4){y0[0], y0[1], y1[0], y1[1]};
#pragma unroll
      for (int rt = 0; rt < 3; rt++) xf[s][rt] = *(const u32x4*)(xbe + rt * 8448 + 64 * s);
    }
    SB();
#pragma unroll
    for (int s = 0; s < 4; s++)
#pragma unroll
      for (int rt = 0; rt < 3; rt++) acc[rt] = MFMA16(asbf(wf1[s]), asbf(xf[s][rt]), acc[rt]);
  }
  u32x2 hr[3];
  u32 hm[3];
  const char* xp[3];
#pragma unroll
  for (int rt = 0; rt < 3; rt++) {
    hr[rt] = *(const u32x2*)(lds + L_IMG + c16e * 528 + 8 * gqe + rt * 8448 + 32 * w);
    hm[rt] = 16 * rt + c16e < 40 ? ((const u32*)(lds + L_HM))[16 * rt + c16e] : 0u;
    xp[rt] = lds + L_IMG + 256 + 16 * gqe + (39 + __builtin_ctz(hm[rt] | 256u)) * 528;
    hm[rt] &= hm[rt] - 1;
  }
  STAMP(1, 12);
  __syncthreads();
  STAMP(1, 13);
  {
    u32x4 xf[2][3][4];
#pragma unroll
    for (int s = 0; s < 4; s++)
#pragma unroll
      for (int rt = 0; rt < 3; rt++) {
        xf[0][rt][s] = *(const u32x4*)(xbe + 256 + rt * 8448 + 64 * s);
        xf[1][rt][s] = *(const u32x4*)(xp[rt] + 64 * s);
      }
    SB();
#pragma unroll
    for (int s = 0; s < 4; s++)
#pragma unroll
      for (int j = 0; j < 2; j++)
#pragma unroll
        for (int rt = 0; rt < 3; rt++) acm[rt] = MFMA16(asbf(wf2[s]), asbf(xf[j][rt][s]), acm[rt]);
  }
  while (__builtin_amdgcn_ballot_w64((hm[0] | hm[1] | hm[2]) != 0) != 0) {
#pragma unroll
    for (int rt = 0; rt < 3; rt++) {
      const char* p = lds + L_IMG + 256 + 16 * gqe + (39 + __builtin_ctz(hm[rt] | 256u)) * 528;
      hm[rt] &= hm[rt] - 1;
#pragma unroll
      for (int s = 0; s < 4; s++) acm[rt] = MFMA16(asbf(wf2[s]), asbf(*(const u32x4*)(p + 64 * s)), acm[rt]);
    }
  }
  STAMP(1, 14);
  {
    const rsrc_t rs_o = mkrs(out + (size_t)n0 * 128, nn * 256);
#pragma unroll
    for (int rt = 0; rt < 3; rt++) {
      u32 o[2];
#pragma unroll
      for (int r = 0; r < 2; r++) {
        const f32x2 z = (f32x2){acc[rt][2 * r], acc[rt][2 * r + 1]} + (f32x2){acm[rt][2 * r], acm[rt][2 * r + 1]} * NLN2;
        const f32x2 t = z * NLOG2E;
        const f32x2 d = (f32x2){__builtin_amdgcn_exp2f(t[0]), __builtin_amdgcn_exp2f(t[1])} + 1.0f;
        const f32x2 v = z * (f32x2){__builtin_amdgcn_rcpf(d[0]), __builtin_amdgcn_rcpf(d[1])} + (f32x2){bf2f(hr[rt][r]), bfhi(hr[rt][r])};
        o[r] = pk2(v[0], v[1]);
      }
      __builtin_amdgcn_raw_buffer_store_b64((u32x2){o[0], o[1]}, rs_o, c16e * 256 + 8 * gqe, rt * 4096 + 32 * w, 0);
    }
  }
  if (w < 2) {
    const int nl = __umul24(tid2, 21846) >> 16, k = tid2 - 3 * nl;
    if (tid2 < 3 * nn) {
      const char* mp = lds + L_IMG + 512 + 4 * k;
      u32 hq = ((const u32*)(lds + L_HM))[nl];
      float pv = *(const float*)(mp + nl * 528) + bf2f(*(const u16*)(lds + L_NODE + nl * 2 + tid2 * 2));
      while (hq) {
        pv += *(const float*)(mp + (39 + __builtin_ctz(hq)) * 528);
        hq &= hq - 1;
      }
      out[(size_t)NN * 128 + n0 * 3 + tid2] = (u16)pk2(pv, 0.f);
    }
  }
  VMWAIT0();
  STAMP(1, 15); RSTAMP(1, 31);
}

extern "C" void kernel_launch(void* const* d_in, const int* in_sizes, int n_in, void* d_out, int out_size,
                              void* d_ws, size_t ws_size, hipStream_t stream) {
  const u16* h = (const u16*)d_in[0];
  const u16* pos = (const u16*)d_in[1];
  const u16* ea = (const u16*)d_in[2];
  const int* src = (const int*)d_in[3];
  const int* dst = (const int*)d_in[4];
  const u16* we = (const u16*)d_in[5];
  const u16* wx = (const u16*)d_in[6];
  const u16* wh = (const u16*)d_in[7];
  egnn_k1<<<NB, 512, 0, stream>>>(h, ea, src, dst, we, (char*)d_ws);
  egnn_k2<<<NB, 512, 0, stream>>>(h, pos, we, wx, wh, (const char*)d_ws, (u16*)d_out);
}
```

```cpp
#include <hip/hip_runtime.h>
#include <hip/hip_bf16.h>

typedef unsigned short u16;
typedef unsigned int u32;
typedef float f32x2 __attribute__((ext_vector_type(2)));
typedef float f32x4 __attribute__((ext_vector_type(4)));
typedef float f32x16 __attribute__((ext_vector_type(16)));
typedef __bf16 bf16x8 __attribute__((ext_vector_type(8)));
typedef __bf16 bf16x2 __attribute__((ext_vector_type(2)));
typedef u32 u32x2 __attribute__((ext_vector_type(2)));
typedef u32 u32x3 __attribute__((ext_vector_type(3)));
typedef u32 u32x4 __attribute__((ext_vector_type(4)));
typedef int i32x4 __attribute__((ext_vector_type(4)));
typedef short s16x4 __attribute__((ext_vector_type(4)));
typedef u16 u16x2 __attribute__((ext_vector_type(2)));
#define LAS __attribute__((address_space(3)))
typedef __amdgpu_buffer_rsrc_t rsrc_t;

constexpr int NN = 10000;
constexpr int NE = 320000;
constexpr int NCH = 250;
constexpr int CH = 1280;
constexpr int NB = 256;
constexpr int CAP = 1408;
constexpr float NLOG2E = -1.4426950408889634f;
constexpr float NLN2 = -0.6931471805599453f;
constexpr u32 OH = 0x3F81u;
constexpr u32 OHINV = 0x81u;

constexpr size_t WS_T = 0;
constexpr size_t WS_R = WS_T + (size_t)NB * 256 * 4;
constexpr size_t WS_B = WS_R + (size_t)NE * 12;

__device__ __forceinline__ float bf2f(u32 v) { return __uint_as_float(v << 16); }
__device__ __forceinline__ float bfhi(u32 v) { return __uint_as_float(v & 0xffff0000u); }
__device__ __forceinline__ u32 pk2(float a, float b) {
  f32x2 v = {a, b};
  return __builtin_bit_cast(u32, __builtin_convertvector(v, bf16x2));
}
__device__ __forceinline__ u32 mul16x2(u32 a, u32 b) {
  return __builtin_bit_cast(u32, __builtin_bit_cast(u16x2, a) * __builtin_bit_cast(u16x2, b));
}
__device__ __forceinline__ int wave_incl_scan(int x) {
  x += __builtin_amdgcn_update_dpp(0, x, 0x111, 0xf, 0xf, false);
  x += __builtin_amdgcn_update_dpp(0, x, 0x112, 0xf, 0xf, false);
  x += __builtin_amdgcn_update_dpp(0, x, 0x114, 0xf, 0xf, false);
  x += __builtin_amdgcn_update_dpp(0, x, 0x118, 0xf, 0xf, false);
  x += __builtin_amdgcn_update_dpp(0, x, 0x142, 0xa, 0xf, false);
  x += __builtin_amdgcn_update_dpp(0, x, 0x143, 0xc, 0xf, false);
  return x;
}
__device__ __forceinline__ bf16x8 asbf(u32x4 v) { return __builtin_bit_cast(bf16x8, v); }
__device__ __forceinline__ u32x2 trread(const char* p) {
  return __builtin_bit_cast(u32x2, __builtin_amdgcn_ds_read_tr16_b64_v4i16((LAS s16x4*)p));
}
__device__ __forceinline__ rsrc_t mkrs(const void* p, u32 bytes) { return __builtin_amdgcn_make_buffer_rsrc((void*)p, 0, bytes, 0x00020000); }
__device__ __forceinline__ void dma16(rsrc_t rs, char* ldsp, u32 voff, u32 soff) {
  __builtin_amdgcn_raw_ptr_buffer_load_lds(rs, (LAS void*)ldsp, 16, voff, soff, 0, 0);
}
#define MFMA32(a, b, c) __builtin_amdgcn_mfma_f32_32x32x16_bf16(a, b, c, 0, 0, 0)
#define MFMA16(a, b, c) __builtin_amdgcn_mfma_f32_16x16x32_bf16(a, b, c, 0, 0, 0)
#define SB() __builtin_amdgcn_sched_barrier(0)
#define VMWAIT0() asm volatile("s_waitcnt vmcnt(0)" ::: "memory")
#define STAMPDECL
#define STAMP(buf, i)
#define STAMPV(buf, i)
#define RSTAMP(buf, i)
#define DRAIN()

template <int V> struct IC { static constexpr int value = V; };
__device__ __forceinline__ u32x4 mksrd(const void* p, u32 bytes) {
  const unsigned long long a = (unsigned long long)p;
  return (u32x4){(u32)a, (u32)(a >> 32) & 0xffffu, bytes, 0x00020000u};
}
#define DMA1(J, A, SRD, SOFF) asm volatile("buffer_load_dwordx4 %0, %1, %2 offen offset:" #J " lds" :: "v"(A), "s"(SRD), "s"(SOFF) : "memory")
#define LDB(T, DST, VO, SRD, SO) asm volatile("buffer_load_" T " %0, %1, %2, %3 offen" : "=v"(DST) : "v"(VO), "s"(SRD), "s"(SO) : "memory")
#define SETM0(V) asm volatile("s_mov_b32 m0, %0\n\ts_nop 0" :: "s"(V) : "memory")
#define VMWAIT(N) asm volatile("s_waitcnt vmcnt(" #N ")" ::: "memory")
#define LGKMWAIT0() asm volatile("s_waitcnt lgkmcnt(0)" ::: "memory")
#define PIN(x) do { asm volatile("" : "+v"(x)); SB(); } while (0)

constexpr int K1_H = 32768;
constexpr int K1_IMG = K1_H + 48 * 256;
constexpr int K1_HIST = K1_IMG + 16384;
constexpr int K1_LDS = K1_HIST + 2 * NB * 4 + 32;

__global__ __launch_bounds__(512) void egnn_k1(const u16* __restrict__ h, const u16* __restrict__ ea,
                                               const int* __restrict__ src, const int* __restrict__ dst,
                                               const u16* __restrict__ we, char* __restrict__ ws) {
  __shared__ __attribute__((aligned(128))) char lds[K1_LDS];
  const int tid = threadIdx.x, lane = tid & 63;
  const int w = __builtin_amdgcn_readfirstlane(tid >> 6);
  const int c = (blockIdx.x & 7) * 32 + (blockIdx.x >> 3);
  const int gq = lane >> 4, c16 = lane & 15;
  u32* const hist = (u32*)(lds + K1_HIST);
  asm volatile("" :: "s"(h), "s"(ea), "s"(src), "s"(dst), "s"(we), "s"(ws));
  STAMPDECL; RSTAMP(0, 30); STAMP(0, 0);
  if (tid < NB) hist[tid] = 0;
  else if (tid < NB + 8) hist[NB + tid] = 0;
  __syncthreads();
  auto meet = [&](int k) {
    LGKMWAIT0();
    if (lane == 0) __hip_atomic_fetch_add(&hist[2 * NB + k], 1u, __ATOMIC_RELAXED, __HIP_MEMORY_SCOPE_WORKGROUP);
    while (__hip_atomic_load(&hist[2 * NB + k], __ATOMIC_RELAXED, __HIP_MEMORY_SCOPE_WORKGROUP) < 4u) __builtin_amdgcn_s_sleep(1);
    asm volatile("" ::: "memory");
  };
  if (w < 4) {
    const u32 eo = tid * 20, nb = c < NCH ? CH * 4 : 0;
    const rsrc_t rs_d = mkrs(dst + c * CH, nb), rs_s = mkrs(src + c * CH, nb), rs_a = mkrs(ea + (size_t)c * CH * 4, 2 * nb);
    const u32x4 d4 = __builtin_amdgcn_raw_buffer_load_b128(rs_d, eo, 0, 0);
    const u32 d1 = __builtin_amdgcn_raw_buffer_load_b32(rs_d, eo + 16, 0, 0);
    const u32x4 s4 = __builtin_amdgcn_raw_buffer_load_b128(rs_s, eo, 0, 0);
    const u32 s1 = __builtin_amdgcn_raw_buffer_load_b32(rs_s, eo + 16, 0, 0);
    const u32x4 a0 = __builtin_amdgcn_raw_buffer_load_b128(rs_a, 2 * eo, 0, 0);
    const u32x4 a1 = __builtin_amdgcn_raw_buffer_load_b128(rs_a, 2 * eo + 16, 0, 0);
    const u32x2 a2 = __builtin_amdgcn_raw_buffer_load_b64(rs_a, 2 * eo + 32, 0, 0);
    __builtin_amdgcn_s_setprio(1);
    STAMP(0, 1);
    if (c < NCH) {
      u32* const img = (u32*)(lds + K1_IMG);
      u32* const base = hist + NB;
      u32 bk[5], rk[5];
#pragma unroll
      for (int i = 0; i < 5; i++) {
        bk[i] = __umul24(i < 4 ? d4[i & 3] : d1, 214749u) >> 23;
        rk[i] = atomicAdd(&hist[bk[i]], 1u);
      }
      STAMP(0, 2);
      meet(1);
      STAMP(0, 3);
      {
        const u32x4 v = *(const u32x4*)(hist + 4 * lane);
        const int s = v[0] + v[1] + v[2] + v[3];
        const int ex = wave_incl_scan(s) - s;
        const u32x4 bs = {(u32)ex, ex + v[0], ex + v[0] + v[1], ex + v[0] + v[1] + v[2]};
        *(u32x4*)(base + 4 * lane) = bs;
        const u32 bb = w == 0 ? bs[0] : w == 1 ? bs[1] : w == 2 ? bs[2] : bs[3];
        const u32 vv = w == 0 ? v[0] : w == 1 ? v[1] : w == 2 ? v[2] : v[3];
        ((u32*)(ws + WS_T))[(4 * lane + w) * 256 + c] = bb | (vv << 16);
      }
#pragma unroll
      for (int i = 0; i < 5; i++) {
        u32* p = img + 3 * (base[bk[i]] + rk[i]);
        p[0] = i < 2 ? a0[2 * (i & 1)] : i < 4 ? a1[2 * (i & 1)] : a2[0];
        p[1] = i < 2 ? a0[2 * (i & 1) + 1] : i < 4 ? a1[2 * (i & 1) + 1] : a2[1];
        p[2] = (i < 4 ? s4[i & 3] : s1) | ((i < 4 ? d4[i & 3] : d1) << 16);
      }
      STAMP(0, 4);
      meet(2);
      STAMP(0, 5);
      const rsrc_t rs_r = mkrs(ws + WS_R + (size_t)c * (CH * 12), CH * 12);
#pragma unroll
      for (int i = 0; i < 4; i++)
        __builtin_amdgcn_raw_buffer_store_b128(((const u32x4*)img)[tid + 256 * i], rs_r, tid * 16 + i * 4096, 0, 0);
    }
    STAMP(0, 6); DRAIN(); STAMP(0, 7); RSTAMP(0, 31);
  } else {
    const int u = w - 4, n0 = (c * 625 + 15) >> 4, nn = (((c + 1) * 625 + 15) >> 4) - n0;
    const u32 ldsb = (u32)(size_t)(LAS char*)lds;
    {
      const u32x4 srd_h = mksrd(h + (size_t)n0 * 128, nn * 256), srd_w = mksrd(we + 128 * 128, 32768);
      const u32 vw = gq * 256 + 16 * (c16 ^ (4 * gq));
      SETM0(ldsb + u * 8192);
      DMA1(0, vw, srd_w, u * 8192); DMA1(1024, vw, srd_w, u * 8192); DMA1(2048, vw, srd_w, u * 8192); DMA1(3072, vw, srd_w, u * 8192);
      SETM0(ldsb + u * 8192 + 4096);
      DMA1(0, vw, srd_w, u * 8192 + 4096); DMA1(1024, vw, srd_w, u * 8192 + 4096); DMA1(2048, vw, srd_w, u * 8192 + 4096); DMA1(3072, vw, srd_w, u * 8192 + 4096);
      SETM0(ldsb + K1_H + u * 3072);
      DMA1(0, gq * 256 + 16 * (c16 ^ ((12 * u + gq) & 15)), srd_h, u * 3072);
      DMA1(1024, gq * 256 + 16 * (c16 ^ ((12 * u + 4 + gq) & 15)), srd_h, u * 3072);
      DMA1(2048, gq * 256 + 16 * (c16 ^ ((12 * u + 8 + gq) & 15)), srd_h, u * 3072);
    }
    STAMP(0, 1);
    VMWAIT0();
    STAMP(0, 2);
    meet(0);
    STAMP(0, 3);
    f32x4 acc[3][2];
#pragma unroll
    for (int rt = 0; rt < 3; rt++) acc[rt][0] = acc[rt][1] = (f32x4)(0.f);
    const char* wb = lds + (8 * gq + (c16 >> 2)) * 256 + 16 * ((4 * u + (c16 & 3)) ^ (c16 & 12));
    const int hb = K1_H + c16 * 256 + 16 * (gq ^ c16);
    u32x4 fa[4], fb[4], hf[4][3];
#pragma unroll
    for (int s = 0; s < 4; s++) {
      const u32x2 x0 = trread(wb + (32 * s) * 256), x1 = trread(wb + (32 * s + 4) * 256);
      const u32x2 y0 = trread(wb + (32 * s) * 256 + 8), y1 = trread(wb + (32 * s + 4) * 256 + 8);
      fa[s] = (u32x4){x0[0], x0[1], x1[0], x1[1]};
      fb[s] = (u32x4){y0[0], y0[1], y1[0], y1[1]};
#pragma unroll
      for (int rt = 0; rt < 3; rt++) hf[s][rt] = *(const u32x4*)(lds + (hb ^ (64 * s)) + rt * 4096);
    }
    SB();
#pragma unroll
    for (int s = 0; s < 4; s++)
#pragma unroll
      for (int rt = 0; rt < 3; rt++) {
        acc[rt][0] = MFMA16(asbf(fa[s]), asbf(hf[s][rt]), acc[rt][0]);
        acc[rt][1] = MFMA16(asbf(fb[s]), asbf(hf[s][rt]), acc[rt][1]);
      }
    const rsrc_t rs_b = mkrs(ws + WS_B + (size_t)n0 * 256, nn * 256);
#pragma unroll
    for (int rt = 0; rt < 3; rt++) {
      const f32x4 a = acc[rt][0] * NLOG2E, b = acc[rt][1] * NLOG2E;
      const u32x4 o = {pk2(a[0], a[1]), pk2(a[2], a[3]), pk2(b[0], b[1]), pk2(b[2], b[3])};
      __builtin_amdgcn_raw_buffer_store_b128(o, rs_b, c16 * 256 + 16 * gq + 64 * u + rt * 4096, 0, 0);
    }
    STAMP(0, 6); DRAIN(); STAMP(0, 7); RSTAMP(0, 31);
  }
}

constexpr int OHS = 2 * CAP + 16;
constexpr int L_NODE = 0;
constexpr int L_WX = L_NODE + 320;
constexpr int L_WS = L_WX + 512;
constexpr int L_REC = L_WS + 16;
constexpr int L_CV = L_REC + 16 * (CAP + 1);
constexpr int L_SRC = L_CV + 8 * (CAP + 2);
constexpr int L_OH = L_SRC + 2 * (CAP + 8);
constexpr int L_CNT = L_OH + 8 * OHS;
constexpr int L_HM = L_CNT + 256;
constexpr int L_IMG = L_HM + 160;
constexpr int L_AT = L_IMG + 48 * 528;
constexpr int L_STG = (L_AT + 128 * 96 + 127) & ~127;
constexpr int K2_LDS = L_STG + 65536;
constexpr int L_MG = L_STG + 32768;
static_assert(K2_LDS <= 163840, "LDS");
static_assert(L_CV % 16 == 0 && L_OH % 16 == 0 && L_SRC % 16 == 0 && L_IMG % 16 == 0 && L_AT % 16 == 0, "align");
static_assert(L_IMG - L_OH > 2 * 8192 && L_IMG - L_OH <= 3 * 8192, "zero sweep");

__global__ __launch_bounds__(512, 2) void egnn_k2(const u16* __restrict__ h, const u16* __restrict__ pos,
                                                  const u16* __restrict__ we, const u16* __restrict__ wx,
                                                  const u16* __restrict__ wh, const char* __restrict__ ws,
                                                  u16* __restrict__ out) {
  __shared__ __attribute__((aligned(128))) char lds[K2_LDS];
  int tid = threadIdx.x, lane = tid & 63;
  const int w = __builtin_amdgcn_readfirstlane(tid >> 6);
  const int b = (blockIdx.x & 7) * 32 + (blockIdx.x >> 3);
  const int n0 = (b * 625 + 15) >> 4, nn = (((b + 1) * 625 + 15) >> 4) - n0;
  int gq = lane >> 4, c16 = lane & 15, hh = lane >> 5, n32 = lane & 31;
  const u32 ldsb = (u32)(size_t)(LAS char*)lds;
  const u32x4 srd_g = mksrd(ws + WS_B - 4096, 4096 + NN * 256), srd_pos = mksrd(pos, NN * 6);

  asm volatile("" :: "s"(h), "s"(pos), "s"(we), "s"(wx), "s"(wh), "s"(ws), "s"(out));
  STAMPDECL; RSTAMP(1, 30); STAMP(1, 0);
  u32 cwv[6];
  u32x4 hv[2], t4;
  if (w < 4) LDB("dwordx4", t4, lane * 16, mksrd(ws + WS_T + b * 1024, 1024), 0);
  {
    const u32x4 srd_h = mksrd(h + (size_t)n0 * 128, nn * 256);
    LDB("dwordx4", hv[0], tid * 16, srd_h, 0);
    LDB("dwordx4", hv[1], tid * 16, srd_h, 8192);
    const u32x4 srd_we = mksrd(we, 261 * 256);
    const u32 vw = gq * 256 + 16 * (c16 ^ (4 * gq));
    SETM0(ldsb + L_STG + w * 4096);
    DMA1(0, vw, srd_we, w * 4096); DMA1(1024, vw, srd_we, w * 4096); DMA1(2048, vw, srd_we, w * 4096); DMA1(3072, vw, srd_we, w * 4096);
    if (w >= 6) {
#pragma unroll
      for (int k = 0; k < 5; k++) LDB("ushort", cwv[k], tid * 2, srd_we, (256 + k) * 256 - 768);
      LDB("ushort", cwv[5], tid * 2 - 768, mksrd(wx, 256), 0);
    } else {
      LDB("ushort", cwv[0], tid * 2 - 512, srd_pos, n0 * 6);
    }
  }
  SB();
#pragma unroll
  for (int i = 0; i < 2; i++) *(u32x4*)(lds + L_OH + tid * 16 + i * 8192) = (u32x4)(0u);
  if (tid < (L_IMG - L_OH - 2 * 8192) / 16) *(u32x4*)(lds + L_OH + tid * 16 + 2 * 8192) = (u32x4)(0u);
  {
    char* ip = lds + L_IMG + (tid >> 4) * 528 + 256 + c16 * 16;
    *(u32x4*)ip = (u32x4)(0u);
    if (w < 4) *(u32x4*)(ip + 32 * 528) = (u32x4)(0u);
    else if (tid < 304) *(u32x4*)(lds + L_IMG + (tid - 256) * 528 + 512) = (u32x4)(0u);
  }
  u32x4 id0, id1, one4 = {hh ? 0u : 0x3F80u, 0u, 0u, 0u};
  int ga[4], recoff, ohoff, cvoff, axoff, srcoff;
  u32 go[4], vsrc;
  auto lane_consts = [&]() {
    const int k0 = n32 - 8 * hh;
    const u32 one = 0x3F80u << ((k0 & 1) * 16);
#pragma unroll
    for (int i = 0; i < 4; i++) {
      id0[i] = (k0 >> 1) == i ? one : 0u;
      id1[i] = (k0 >> 1) == 8 + i ? one : 0u;
    }
    const int prow = (n32 & 19) | ((n32 & 4) << 1) | ((n32 & 8) >> 1);
    const int sa = prow & 3, sb = prow >> 2;
#pragma unroll
    for (int s = 0; s < 4; s++) ga[s] = (L_STG + w * 8192 + (8 * sa + sb) * 128 + 16 * (hh ^ (2 * sa + ((sb >> 1) & 1)))) ^ (32 * s);
#pragma unroll
    for (int j = 0; j < 4; j++) go[j] = 4096 - 1024 * j + 16 * ((lane & 7) ^ (2 * j + ((lane >> 4) & 1)));
    recoff = L_REC + prow * 16;
    ohoff = L_OH + (n32 >> 2) * OHS + 16 * hh;
    cvoff = L_CV + (8 * hh + (c16 >> 2)) * 8;
    axoff = L_OH + (c16 >> 2) * OHS + 32 * (gq & 1) + 16 * (c16 & 1) + 8 * ((c16 >> 1) & 1);
    srcoff = L_SRC + (lane >> 3) * 8;
    vsrc = (lane >> 1) * 256 + 32 * w + 16 * (lane & 1);
  };
  lane_consts();
  const u32 m0a = ldsb + L_STG + w * 8192, m0b = m0a + 4096;
  asm volatile("" : "+v"(one4), "+v"(id0), "+v"(id1), "+v"(ga[0]), "+v"(ga[1]), "+v"(ga[2]), "+v"(ga[3]), "+v"(go[0]), "+v"(go[1]), "+v"(go[2]), "+v"(go[3]));
  asm volatile("" : "+v"(recoff), "+v"(ohoff), "+v"(cvoff), "+v"(axoff), "+v"(srcoff), "+v"(vsrc));
  SB();
  int mtot, scnt, sexc;
  u32 sbase;
  auto decode = [&]() {
    if (lane == 62) t4[2] = t4[3] = 0;
    if (lane == 63) t4 = (u32x4)(0u);
    const int c0 = t4[0] >> 16, c1 = c0 + (t4[1] >> 16), c2 = c1 + (t4[2] >> 16), c3 = c2 + (t4[3] >> 16);
    const int inc = wave_incl_scan(c3);
    mtot = __builtin_amdgcn_readlane(inc, 63);
    const int wq = w & 3;
    const u32 tv = wq == 0 ? t4[0] : wq == 1 ? t4[1] : wq == 2 ? t4[2] : t4[3];
    scnt = tv >> 16;
    sexc = inc - c3 + (wq == 0 ? 0 : wq == 1 ? c0 : wq == 2 ? c1 : c2);
    sbase = ((4 * lane + wq) * CH + (tv & 0xffffu)) * 12;
  };
  if (w < 4) {
    asm volatile("s_waitcnt vmcnt(7)" ::: "memory"); SB();
    STAMP(1, 1);
    decode();
    if (tid == 0) *(int*)(lds + L_WS) = mtot;
    if (mtot <= CAP) {
      u32* p = (u32*)(lds + L_CV) + sexc;
      u32 v = sbase;
#pragma clang loop vectorize(disable) unroll(disable)
      for (int i = 0; i < scnt; i++, p++, v += 12) *p = v;
    }
  }
  asm volatile("s_waitcnt vmcnt(0)" ::: "memory"); SB();
  asm volatile("" : "+v"(cwv[0]), "+v"(cwv[1]), "+v"(cwv[2]), "+v"(cwv[3]), "+v"(cwv[4]), "+v"(cwv[5]));
  {
    char* hp = lds + L_IMG + (tid >> 4) * 528 + c16 * 16;
    *(u32x4*)hp = hv[0];
    if (w < 4) *(u32x4*)(hp + 32 * 528) = hv[1];
  }
  if (w == 4 || w == 5) {
    const int t3 = tid - 256, nl = __umul24(t3, 21846) >> 16;
    if (t3 < 120) *(u16*)(lds + L_NODE + nl * 2 + t3 * 2) = (u16)cwv[0];
  }
  STAMP(1, 3);
  __syncthreads();
  STAMP(1, 4);
  u32x3 rec[3];
  bool general;
  {
    const u32x4 srd_rec = mksrd(ws + WS_R, NE * 12);
    u32 ro[3];
#pragma unroll
    for (int k = 0; k < 3; k++) ro[k] = ((const u32*)(lds + L_CV))[tid + 512 * k];
    mtot = __builtin_amdgcn_readfirstlane(*(const int*)(lds + L_WS));
    general = mtot > CAP;
    const int lim = general ? 0 : mtot;
#pragma unroll
    for (int k = 0; k < 3; k++) LDB("dwordx3", rec[k], tid + 512 * k < lim ? ro[k] : 0x7fffff00u, srd_rec, 0);
  }
  if (general) asm volatile("s_waitcnt vmcnt(0)" :: "v"(rec[0]), "v"(rec[1]), "v"(rec[2]) : "memory");
  SB();
  if (w >= 6) {
    const float c0 = bf2f(cwv[0]) * NLOG2E;
    const u32 d_hi = pk2(c0, 0.f);
    const u32x4 o = {pk2(bf2f(cwv[1]) * NLOG2E, bf2f(cwv[2]) * NLOG2E), pk2(bf2f(cwv[3]) * NLOG2E, bf2f(cwv[4]) * NLOG2E),
                     pk2(c0, c0 - bf2f(d_hi)), 0u};
    *(u32x4*)(lds + L_AT + (tid - 384) * 96 + 80) = o;
    ((float*)(lds + L_WX))[tid - 384] = bf2f(cwv[5]) * NLN2;
  }
  const char* const xb = lds + L_IMG + c16 * 528 + 16 * gq;
  if (w < 4) {
    f32x4 acc[3][2];
#pragma unroll
    for (int rt = 0; rt < 3; rt++) acc[rt][0] = acc[rt][1] = (f32x4)(0.f);
    const char* wb = lds + L_STG + (8 * gq + (c16 >> 2)) * 256 + 16 * ((4 * w + (c16 & 3)) ^ (c16 & 12));
    u32x4 fa[4], fb[4], hf[4][3];
#pragma unroll
    for (int s = 0; s < 4; s++) {
      const u32x2 x0 = trread(wb + (32 * s) * 256), x1 = trread(wb + (32 * s + 4) * 256);
      const u32x2 y0 = trread(wb + (32 * s) * 256 + 8), y1 = trread(wb + (32 * s + 4) * 256 + 8);
      fa[s] = (u32x4){x0[0], x0[1], x1[0], x1[1]};
      fb[s] = (u32x4){y0[0], y0[1], y1[0], y1[1]};
#pragma unroll
      for (int rt = 0; rt < 3; rt++) hf[s][rt] = *(const u32x4*)(xb + rt * 8448 + 64 * s);
    }
    SB();
#pragma unroll
    for (int s = 0; s < 4; s++)
#pragma unroll
      for (int rt = 0; rt < 3; rt++) {
        acc[rt][0] = MFMA16(asbf(hf[s][rt]), asbf(fa[s]), acc[rt][0]);
        acc[rt][1] = MFMA16(asbf(hf[s][rt]), asbf(fb[s]), acc[rt][1]);
      }
    char* ap = lds + L_AT + (32 * w + 8 * (c16 >> 2) + (c16 & 3)) * 96 + 8 * gq;
#pragma unroll
    for (int rt = 0; rt < 3; rt++)
      if (16 * rt + 4 * gq < 40)
#pragma unroll
        for (int j = 0; j < 2; j++) {
          const f32x4 a = acc[rt][j] * (NLOG2E / 1.0078125f);
          *(u32x2*)(ap + j * 384 + rt * 32) = (u32x2){pk2(a[0], a[1]), pk2(a[2], a[3])};
        }
  }
  f32x16 Y[4], XA, XB, e;
  u32x4 sp0 = (u32x4)(0u), sp1 = (u32x4)(0u), pa = (u32x4)(0u), pb = (u32x4)(0u);
  asm volatile("" : "+v"(sp0), "+v"(sp1), "+v"(pa), "+v"(pb));
  Y[0] = MFMA32(asbf(pa), asbf(pa), (f32x16)(0.f));
  Y[1] = MFMA32(asbf(pb), asbf(pb), (f32x16)(0.f));
  Y[2] = MFMA32(asbf(sp0), asbf(sp0), (f32x16)(0.f));
  Y[3] = MFMA32(asbf(sp1), asbf(sp1), (f32x16)(0.f));
  XB = MFMA32(asbf(pa), asbf(pb), (f32x16)(0.f));
  asm volatile("" : "+v"(Y[0]), "+v"(Y[1]), "+v"(Y[2]), "+v"(Y[3]), "+v"(XB));
  SB();
  STAMP(1, 28);
  u32x4 wex[4];
  u32 ad[4];
  int ninc;
  u32x4 srd = srd_g;
  u32 sfb = 128;
  auto prep = [&](u32x2 sv, bool real) {
    if (real) {
      asm("v_mad_u32_u16 %0, %1, %2, %3" : "=v"(ad[0]) : "v"(sv[0]), "s"(256), "v"(go[0]));
      asm("v_mad_u32_u16 %0, %1, %2, %3 op_sel:[1,0,0,0]" : "=v"(ad[1]) : "v"(sv[0]), "s"(256), "v"(go[1]));
      asm("v_mad_u32_u16 %0, %1, %2, %3" : "=v"(ad[2]) : "v"(sv[1]), "s"(256), "v"(go[2]));
      asm("v_mad_u32_u16 %0, %1, %2, %3 op_sel:[1,0,0,0]" : "=v"(ad[3]) : "v"(sv[1]), "s"(256), "v"(go[3]));
    } else {
#pragma unroll
      for (int j = 0; j < 4; j++) ad[j] = vsrc + 7168 * j;
      srd = mksrd(wh, 65536);
      sfb = 32768;
    }
  };
  auto issue_a = [&]() { SETM0(m0a); DMA1(0, ad[0], srd, 0); DMA1(1024, ad[1], srd, 0); DMA1(2048, ad[2], srd, 0); DMA1(3072, ad[3], srd, 0); };
  auto issue_b = [&]() { SETM0(m0b); DMA1(0, ad[0], srd, sfb); DMA1(1024, ad[1], srd, sfb); DMA1(2048, ad[2], srd, sfb); DMA1(3072, ad[3], srd, sfb); };
  auto node_at = [&](int x) { return (int)__builtin_popcountll(__builtin_amdgcn_ballot_w64(ninc <= x)); };
  auto loadw = [&](int base) {
    const int g0 = base >> 3, bm = base & 7;
    const char* pa = lds + L_AT + n32 * 96 + (hh ? g0 * 16 : 80);
    const char* pb = lds + L_AT + n32 * 96 + (hh ? g0 * 16 + 16 : 80);
    const unsigned long long lo = ~0ull << (16 * min(bm, 3)) << (bm > 3 ? 16 : 0), hi = bm > 4 ? ~0ull << (16 * (bm - 4)) : ~0ull;
    const u32 mk[4] = {(u32)lo, (u32)(lo >> 32), (u32)hi, (u32)(hi >> 32)};
#pragma unroll
    for (int t = 0; t < 4; t++) {
      const u32x4 a = *(const u32x4*)(pa + t * 3072), bb = *(const u32x4*)(pb + t * 3072);
#pragma unroll
      for (int i = 0; i < 4; i++) wex[t][i] = (a[i] & mk[i]) | (bb[i] & ~mk[i]);
    }
  };
  auto pdot = [&](auto nbc) {
    constexpr int nb = decltype(nbc)::value;
    f32x4 p4[2] = {(f32x4)(0.f), (f32x4)(0.f)};
#pragma unroll
    for (int q0 = 0; q0 < 16; q0 += nb) {
      f32x4 wv[16];
#pragma unroll
      for (int q = q0; q < q0 + nb; q++) wv[q] = *(const f32x4*)(lds + L_WX + (8 * q + 4 * hh) * 4);
      SB();
#pragma unroll
      for (int q = q0; q < q0 + nb; q++)
        p4[q & 1] += (f32x4){Y[q >> 2][4 * (q & 3)], Y[q >> 2][4 * (q & 3) + 1], Y[q >> 2][4 * (q & 3) + 2], Y[q >> 2][4 * (q & 3) + 3]} * wv[q];
      SB();
    }
    p4[0] += p4[1];
    const f32x2 pa = (f32x2){p4[0][0], p4[0][1]} + (f32x2){p4[0][2], p4[0][3]};
    const u32 ph = __float_as_uint(pa[0] + pa[1]);
    const auto sw = __builtin_amdgcn_permlane32_swap(ph, ph, false, false);
    return __uint_as_float(sw[0]) + __uint_as_float(sw[1]);
  };
  auto place = [&](u32x3 rec, u32x2 ps, u32x2 pd, u32 dl, u32 p) {
    const u32 sh = (rec[2] & 1) * 16;
    const u32 sxy = __builtin_amdgcn_alignbit(ps[1], ps[0], sh), sz = ps[1] >> sh;
    const u32 dxy = pk2(bf2f(pd[0]) - bf2f(sxy), bfhi(pd[0]) - bfhi(sxy));
    const u32 dz1 = pk2(bf2f(pd[1]) - bf2f(sz), 1.0f);
    const float fx = bf2f(dxy), fy = bfhi(dxy), fz = bf2f(dz1);
    const float d2 = fx * fx + fy * fy + fz * fz;
    *(u32x4*)(lds + L_REC + p * 16) = (u32x4){rec[0], rec[1], pk2(d2, d2), dl};
    *(u32x2*)(lds + L_CV + p * 8) = (u32x2){mul16x2(dxy, OHINV * 0x10001u), mul16x2(dz1, OHINV * 0x10001u)};
    *(u16*)(lds + L_OH + (dl & 7) * OHS + 2 * p) = (u16)OH;
  };
  auto count_nodes = [&](int m) {
    const int cn = ((const u32*)(lds + L_CNT))[lane];
    ninc = wave_incl_scan(cn);
    if (w == 7) {
      const int p = m + n32 < ((m + 31) & ~31) ? m + n32 : CAP;
      *(u32x4*)(lds + L_REC + p * 16) = (u32x4)(0u);
      ((u16*)(lds + L_SRC))[p] = 0;
    }
    return ninc - cn;
  };
#define RDG(s, half) (*(const u32x4*)(lds + ga[s] + (half) * 4096))
#define EXP_(lo, hi) _Pragma("unroll") for (int r = lo; r < hi; r++) e[r] = __builtin_amdgcn_exp2f(xc[r]); SB()
#define RCP_(lo, hi) _Pragma("unroll") for (int r = lo; r < hi; r++) e[r] = __builtin_amdgcn_rcpf(e[r]); SB()
#define PACK_() _Pragma("unroll") for (int i = 0; i < 4; i++) { pa[i] = pk2(e[2 * i], e[2 * i + 1]); pb[i] = pk2(e[8 + 2 * i], e[8 + 2 * i + 1]); } \
  asm volatile("" : "+v"(pa), "+v"(pb)); SB()
#define SPROD(o, ca, cb) (u32x4){mul16x2(o[0], ca[0]), mul16x2(o[1], ca[1]), mul16x2(o[2], cb[0]), mul16x2(o[3], cb[1])}

  const int m = mtot;
  const bool third = w * 64 + 1024 < m;
  u32 rk[3], dl[3];
  u32x2 ps[3], pd[3];
  if (!general) {
    VMWAIT0(); SB();
    STAMP(1, 27);
#pragma unroll
    for (int k = 0; k < 3; k++)
      if (k < 2 || third) {
        LDB("dwordx2", ps[k], ((rec[k][2] & 0xffffu) * 6) & ~3u, srd_pos, 0);
        dl[k] = tid + 512 * k < m ? (rec[k][2] >> 16) - n0 : 48u + c16;
        rk[k] = atomicAdd((u32*)(lds + L_CNT) + dl[k], 1u);
        pd[k] = *(const u32x2*)(lds + L_NODE + dl[k] * 8);
      }
    STAMP(1, 5);
    __syncthreads();
    STAMP(1, 6);
    const int nst = count_nodes(m);
    const int nprev = __builtin_amdgcn_ds_bpermute(lane * 4 - 28, ninc);
#pragma unroll
    for (int k = 0; k < 3; k++)
      if (k < 2 || third) {
        rk[k] += __builtin_amdgcn_ds_bpermute(dl[k] * 4, nst);
        rk[k] = dl[k] < 48 ? rk[k] : CAP;
        *(u16*)(lds + L_SRC + 2 * rk[k]) = (u16)rec[k][2];
      }
    general = m < 256 || __builtin_amdgcn_ballot_w64(lane >= 7 && lane < nn && ninc - nprev <= 30) != 0;
    if (general) asm volatile("s_waitcnt vmcnt(0)" :: "v"(ps[0]), "v"(ps[1]), "v"(ps[2]) : "memory");
  }
  if (!general) {
    {
      const int nt = (m + 31) >> 5, tq = nt >> 3, trm = nt & 7;
      const int tbeg = w * tq + min(w, trm);
      const int tend = tbeg + tq + (w < trm ? 1 : 0);
      srcoff += tbeg * 64;
      __syncthreads();
      STAMP(1, 29);
      const u32x2 sv0 = *(const u32x2*)(lds + srcoff), sv1 = *(const u32x2*)(lds + srcoff + 64);
      prep(sv0, true);
      if (w < 4) {
        issue_a();
        issue_b();
        VMWAIT(8);
      } else {
        VMWAIT(0);
      }
      SB();
      STAMP(1, 26);
      int base = node_at(tbeg * 32);
      loadw(base);
      const int lnode = node_at(min(tend * 32, m) - 1);
      const int hnode = (tbeg > 0 && node_at(tbeg * 32 - 1) == base) ? base : -1;
      if (hnode >= 0 && lane == 0) __hip_atomic_fetch_or((u32*)(lds + L_HM) + hnode, 1u << w, __ATOMIC_RELAXED, __HIP_MEMORY_SCOPE_WORKGROUP);
      recoff += tbeg * 512; cvoff += tbeg * 256 - 256; ohoff += tbeg * 64 - 64; axoff += tbeg * 64;
#pragma unroll
      for (int k = 0; k < 3; k++)
        if (k < 2 || third) place(rec[k], ps[k], pd[k], dl[k], rk[k]);
      if (w >= 4) {
        issue_a();
        issue_b();
      }
      prep(sv1, tbeg + 1 < tend);
#pragma unroll
      for (int t = 0; t < 4; t++) asm volatile("" : "+v"(wex[t]));
      auto flush = [&](int base, int lim, auto clearc) {
        const int node = base + (((n32 >> 2) - base) & 7), comp = n32 & 3;
        constexpr bool clear = decltype(clearc)::value != 0;
        const float pt = pdot(IC<(clear ? 2 : 16)>{});
        if (node < lim) {
          char* mp = lds + L_IMG + (node == hnode ? 39 + w : node) * 528;
          if (comp == 3) {
#pragma unroll
            for (int q = 0; q < 16; q++)
              *(u32x2*)(mp + 256 + 8 * hh + 16 * q) = (u32x2){pk2(Y[q >> 2][4 * (q & 3)], Y[q >> 2][4 * (q & 3) + 1]),
                                                              pk2(Y[q >> 2][4 * (q & 3) + 2], Y[q >> 2][4 * (q & 3) + 3])};
          } else {
            *(float*)(mp + 512 + 4 * comp) = pt;
          }
        }
        if (clear) {
          const float keep = node < lim ? 0.f : 1.f;
#pragma unroll
          for (int t = 0; t < 4; t++) Y[t] *= keep;
        }
      };
      STAMP(1, 7);
      __syncthreads();
      STAMP(1, 8);
      u32x4 aex;
      {
        const u32x2 t0 = trread(lds + axoff), t1 = trread(lds + axoff + 4 * OHS);
        const u32x4 ra = *(const u32x4*)(lds + recoff);
        aex = hh ? (u32x4){t0[0], t0[1], t1[0], t1[1]} : ra;
      }
      XA = MFMA32(asbf(aex), asbf(wex[0]), (f32x16)(0.f)); PIN(XA);
      VMWAIT(4);
      STAMP(1, 23);
      u32x4 g0 = RDG(0, 0), g1 = RDG(1, 0), g2 = RDG(2, 0), g3 = RDG(3, 0);
      LGKMWAIT0();
      SB();
      XA = MFMA32(asbf(g0), asbf(id0), XA); PIN(XA);
      issue_a();
      XA = MFMA32(asbf(g1), asbf(id1), XA); PIN(XA);
      int fbase = base;
      __builtin_amdgcn_s_setprio(3);
      STAMP(1, 9);
      int tcount_ = 0;
#define EXPX(X, lo, hi) _Pragma("unroll") for (int r = lo; r < hi; r++) e[r] = __builtin_amdgcn_exp2f(X[r]); SB()
#define PACKX(X) _Pragma("unroll") for (int i = 0; i < 4; i++) { pa[i] = pk2(X[2 * i], X[2 * i + 1]); pb[i] = pk2(X[8 + 2 * i], X[8 + 2 * i + 1]); } \
  asm volatile("" : "+v"(pa), "+v"(pb)); SB()
#define SEG(t, p, sp) Y[t] = MFMA32(asbf(p), asbf(sp), Y[t]); PIN(Y[t])
#define PLUS1() e = MFMA32(asbf(one4), asbf(one4), e); PIN(e)
      for (int left = tend - tbeg, x = tbeg * 32 + 32; left > 0; left--, x += 32) {
        STAMPV(1, 16 + min(tcount_, 4)); tcount_++;
        if (__builtin_expect(left == 1, 0)) __builtin_amdgcn_s_setprio(1);
        SB();
        SEG(2, pa, sp0);
        EXPX(XA, 0, 8);
        SEG(2, pb, sp1);
        EXPX(XA, 8, 16);
        PLUS1();
        PACKX(XB);
        srcoff += 64; recoff += 512; cvoff += 256; ohoff += 64; axoff += 64;
        asm volatile("" : "+v"(srcoff), "+v"(recoff), "+v"(cvoff), "+v"(ohoff), "+v"(axoff)); SB();
        RCP_(0, 4);
        XB = MFMA32(asbf(aex), asbf(wex[1]), (f32x16)(0.f)); PIN(XB);
        RCP_(4, 8);
        XB = MFMA32(asbf(g2), asbf(id0), XB); PIN(XB);
        RCP_(8, 12);
        XB = MFMA32(asbf(g3), asbf(id1), XB); PIN(XB);
        RCP_(12, 16);
        XA = XA * e; PIN(XA);
        SEG(3, pa, sp0);
        VMWAIT(4);
        g0 = RDG(0, 1); g1 = RDG(1, 1); g2 = RDG(2, 1); g3 = RDG(3, 1);
        const u32x2 sv = *(const u32x2*)(lds + srcoff + 64);
        SB();
        EXPX(XB, 0, 8);
        SEG(3, pb, sp1);
        const u32x2 c0 = trread(lds + cvoff), c1 = trread(lds + cvoff + 32), c2 = trread(lds + cvoff + 128), c3 = trread(lds + cvoff + 160);
        const u32x4 o0 = *(const u32x4*)(lds + ohoff), o1 = *(const u32x4*)(lds + ohoff + 32);
        SB();
        EXPX(XB, 8, 16);
        PLUS1();
        PACKX(XA);
        sp0 = SPROD(o0, c0, c1);
        sp1 = SPROD(o1, c2, c3);
        asm volatile("" : "+v"(sp0), "+v"(sp1));
        LGKMWAIT0();
        SB();
        SETM0(m0b); DMA1(0, ad[0], srd, sfb); SB();
        RCP_(0, 4);
        XA = MFMA32(asbf(aex), asbf(wex[2]), (f32x16)(0.f)); PIN(XA);
        DMA1(1024, ad[1], srd, sfb); SB();
        RCP_(4, 8);
        XA = MFMA32(asbf(g0), asbf(id0), XA); PIN(XA);
        DMA1(2048, ad[2], srd, sfb); SB();
        RCP_(8, 12);
        XA = MFMA32(asbf(g1), asbf(id1), XA); PIN(XA);
        DMA1(3072, ad[3], srd, sfb); SB();
        RCP_(12, 16);
        XB = XB * e; PIN(XB);
        if (__builtin_expect(fbase != base, 0)) {
          flush(fbase, base, IC<1>{});
          fbase = base;
        }
        if (__builtin_expect(left <= 2, 0)) {
          if (left == 2) __builtin_amdgcn_s_setprio(2);
          else __builtin_amdgcn_s_setprio(0);
          prep(sv, false);
        } else {
          prep(sv, true);
        }
        asm volatile("" : "+v"(ad[0]), "+v"(ad[1]), "+v"(ad[2]), "+v"(ad[3]));
        SB();
        SEG(0, pa, sp0);
        const int nlast = node_at(min(x + 31, m - 1));
        SB();
        EXPX(XA, 0, 8);
        SEG(0, pb, sp1);
        EXPX(XA, 8, 16);
        PLUS1();
        PACKX(XB);
        RCP_(0, 4);
        XB = MFMA32(asbf(aex), asbf(wex[3]), (f32x16)(0.f)); PIN(XB);
        RCP_(4, 8);
        XB = MFMA32(asbf(g2), asbf(id0), XB); PIN(XB);
        RCP_(8, 12);
        XB = MFMA32(asbf(g3), asbf(id1), XB); PIN(XB);
        RCP_(12, 16);
        XA = XA * e; PIN(XA);
        if (__builtin_expect(left > 1 && nlast >= base + 8, 0)) {
          base = node_at(x);
          loadw(base);
        }
        SB();
        SEG(1, pa, sp0);
        VMWAIT(4);
        const u32x2 t0 = trread(lds + axoff), t1 = trread(lds + axoff + 4 * OHS);
        const u32x4 ra = *(const u32x4*)(lds + recoff);
        g0 = RDG(0, 0); g1 = RDG(1, 0); g2 = RDG(2, 0); g3 = RDG(3, 0);
        SB();
        EXPX(XB, 0, 8);
        SEG(1, pb, sp1);
        EXPX(XB, 8, 16);
        PLUS1();
        PACKX(XA);
        LGKMWAIT0();
        SB();
        aex = hh ? (u32x4){t0[0], t0[1], t1[0], t1[1]} : ra;
        PIN(aex);
        SETM0(m0a); DMA1(0, ad[0], srd, 0); SB();
        RCP_(0, 4);
        XA = MFMA32(asbf(aex), asbf(wex[0]), (f32x16)(0.f)); PIN(XA);
        DMA1(1024, ad[1], srd, 0); SB();
        RCP_(4, 8);
        XA = MFMA32(asbf(g0), asbf(id0), XA); PIN(XA);
        DMA1(2048, ad[2], srd, 0); SB();
        RCP_(8, 12);
        XA = MFMA32(asbf(g1), asbf(id1), XA); PIN(XA);
        DMA1(3072, ad[3], srd, 0); SB();
        RCP_(12, 16);
        XB = XB * e; PIN(XB);
      }
      SEG(2, pa, sp0);
      SEG(2, pb, sp1);
      PACKX(XB);
      SEG(3, pa, sp0);
      SEG(3, pb, sp1);
      STAMP(1, 10);
      flush(base, lnode + 1, IC<0>{});
      STAMP(1, 11);
    }
  } else {
    tid = threadIdx.x;
    asm volatile("" : "+v"(tid));
    lane = tid & 63; gq = lane >> 4; c16 = lane & 15; hh = lane >> 5; n32 = lane & 31;
    lane_consts();
    t4 = *(const u32x4*)(ws + WS_T + b * 1024 + lane * 16);
    decode();
    const rsrc_t rs_rec = mkrs(ws + WS_R, NE * 12), rs_pos = mkrs(pos, NN * 6);
    for (int q = tid; q < 40 * 33; q += 512) *(u32x4*)(lds + L_MG + q * 16) = (u32x4)(0u);
    for (int r0 = 0; r0 < mtot; r0 += CAP) {
      const int m = min(mtot - r0, CAP);
      __syncthreads();
      for (int q = tid; q < (L_HM - L_OH) / 16; q += 512) *(u32x4*)(lds + L_OH + q * 16) = (u32x4)(0u);
      const int lim = min(scnt, r0 + CAP - sexc);
      for (int i = max(r0 - sexc, 0) + (w >> 2); i < lim; i += 2)
        *(u32x3*)(lds + L_STG + (sexc + i - r0) * 16) = __builtin_amdgcn_raw_buffer_load_b96(rs_rec, sbase + i * 12, 0, 0);
      __syncthreads();
      for (int f = tid; f < m; f += 512) {
        u32* rp = (u32*)(lds + L_STG + f * 16);
        rp[3] = atomicAdd((u32*)(lds + L_CNT) + ((rp[2] >> 16) - n0), 1u);
      }
      __syncthreads();
      const int nst = count_nodes(m);
      for (int f0 = w * 64; f0 < m; f0 += 512) {
        const bool ok = f0 + lane < m;
        const u32x4 rc = *(const u32x4*)(lds + L_STG + (f0 + lane) * 16);
        const u32 dl = ok ? (rc[2] >> 16) - n0 : 0u;
        const u32 pp = rc[3] + __builtin_amdgcn_ds_bpermute(dl * 4, nst), p = ok ? pp : CAP;
        const u32x2 ps = __builtin_amdgcn_raw_buffer_load_b64(rs_pos, ((rc[2] & 0xffffu) * 6) & ~3u, 0, 0);
        *(u16*)(lds + L_SRC + 2 * p) = (u16)rc[2];
        place((u32x3){rc[0], rc[1], rc[2]}, ps, *(const u32x2*)(lds + L_NODE + dl * 8), dl, p);
      }
      __syncthreads();
      const int nt = w < 4 ? (m + 31) >> 5 : 0;
      for (int tile = w; tile < nt; tile += 4) {
        const int last = node_at(min(tile * 32 + 31, m - 1));
        prep(*(const u32x2*)(lds + srcoff + tile * 64), true);
        SETM0(m0a); DMA1(0, ad[0], srd_g, 0); DMA1(1024, ad[1], srd_g, 0); DMA1(2048, ad[2], srd_g, 0); DMA1(3072, ad[3], srd_g, 0);
        SETM0(m0b); DMA1(0, ad[0], srd_g, 128); DMA1(1024, ad[1], srd_g, 128); DMA1(2048, ad[2], srd_g, 128); DMA1(3072, ad[3], srd_g, 128);
        const u32x2 t0 = trread(lds + axoff + tile * 64), t1 = trread(lds + axoff + tile * 64 + 4 * OHS);
        const u32x4 ra = *(const u32x4*)(lds + recoff + tile * 512);
        const u32x4 aex = hh ? (u32x4){t0[0], t0[1], t1[0], t1[1]} : ra;
        for (int base = node_at(tile * 32); base <= last; base += 8) {
          loadw(base);
          u32x4 sp[2];
#pragma unroll
          for (int s = 0; s < 2; s++) {
            const u32x2 c0 = trread(lds + cvoff + tile * 256 + s * 128), c1 = trread(lds + cvoff + tile * 256 + s * 128 + 32);
            const u32x4 oh = *(const u32x4*)(lds + ohoff + tile * 64 + 32 * s);
            sp[s] = SPROD(oh, c0, c1);
          }
#pragma unroll 1
          for (int j = 0; j < 16; j++) {
            const int dn = *(const u32*)(lds + L_REC + (tile * 32 + 16 * (j >> 3) + 8 * hh + (j & 7)) * 16 + 12);
            const u32 mk = (dn < base || dn >= base + 8) ? ((j & 1) ? 0x0000ffffu : 0xffff0000u) : 0xffffffffu;
#pragma unroll
            for (int i = 0; i < 8; i++) sp[i >> 2][i & 3] &= (i == (j >> 1)) ? mk : 0xffffffffu;
          }
          VMWAIT0();
#pragma unroll
          for (int t = 0; t < 4; t++) {
            const u32x4 ga_ = RDG((2 * t) & 3, t >> 1), gb_ = RDG((2 * t + 1) & 3, t >> 1);
            f32x16 xc = MFMA32(asbf(aex), asbf(wex[t]), (f32x16)(0.f)), e;
            xc = MFMA32(asbf(ga_), asbf(id0), xc);
            xc = MFMA32(asbf(gb_), asbf(id1), xc);
            u32x4 pa, pb;
            EXP_(0, 16);
            e = e + 1.0f;
            RCP_(0, 16);
            e = e * xc;
            PACK_();
            Y[t] = MFMA32(asbf(pa), asbf(sp[0]), (f32x16)(0.f));
            Y[t] = MFMA32(asbf(pb), asbf(sp[1]), Y[t]);
          }
          const int node = base + (((n32 >> 2) - base) & 7), comp = n32 & 3;
          const float pt = pdot(IC<4>{});
          if (node <= last) {
            float* mp = (float*)(lds + L_MG) + node * 132;
            if (comp == 3) {
#pragma unroll
              for (int t = 0; t < 4; t++)
#pragma unroll
                for (int r = 0; r < 16; r++) atomicAdd(mp + 4 * hh + 32 * t + 8 * (r >> 2) + (r & 3), Y[t][r]);
            } else if (hh == 0) {
              atomicAdd(mp + 128 + comp, pt);
            }
          }
        }
        LGKMWAIT0();
      }
    }
    __syncthreads();
    for (int q = tid; q < 40 * 66; q += 512) {
      const int row = q / 66, c = q - row * 66;
      const f32x2 v = *(const f32x2*)(lds + L_MG + row * 528 + c * 8);
      if (c < 64) *(u32*)(lds + L_IMG + row * 528 + 256 + c * 4) = pk2(v[0], v[1]);
      else *(f32x2*)(lds + L_IMG + row * 528 + c * 8) = v;
    }
    __syncthreads();
    const u32x4 srd_wh = mksrd(wh, 65536);
    SETM0(m0a); DMA1(0, vsrc, srd_wh, 0); DMA1(1024, vsrc, srd_wh, 7168); DMA1(2048, vsrc, srd_wh, 14336); DMA1(3072, vsrc, srd_wh, 21504);
    SETM0(m0b); DMA1(0, vsrc, srd_wh, 32768); DMA1(1024, vsrc, srd_wh, 39936); DMA1(2048, vsrc, srd_wh, 47104); DMA1(3072, vsrc, srd_wh, 54272);
    VMWAIT0();
  }

  int tid2 = threadIdx.x;
  asm volatile("" : "+v"(tid2));
  const int c16e = tid2 & 15, gqe = (tid2 >> 4) & 3;
  const char* const xbe = lds + L_IMG + c16e * 528 + 16 * gqe;
  f32x4 acc[3], acm[3];
  const char* const wfp = lds + L_STG + w * 8192 + (8 * gqe + (c16e >> 2)) * 32 + 8 * (c16e & 3);
  VMWAIT(4);
  u32x4 wf2[4];
#pragma unroll
  for (int rt = 0; rt < 3; rt++) acc[rt] = acm[rt] = (f32x4)(0.f);
  {
    u32x4 wf1[4], xf[4][3];
#pragma unroll
    for (int s = 0; s < 4; s++) {
      const u32x2 x0 = trread(wfp + s * 1024), x1 = trread(wfp + s * 1024 + 128);
      const u32x2 y0 = trread(wfp + 4096 + s * 1024), y1 = trread(wfp + 4096 + s * 1024 + 128);
      wf1[s] = (u32x4){x0[0], x0[1], x1[0], x1[1]};
      wf2[s] = (u32x4){y0[0], y0[1], y1[0], y1[1]};
#pragma unroll
      for (int rt = 0; rt < 3; rt++) xf[s][rt] = *(const u32x4*)(xbe + rt * 8448 + 64 * s);
    }
    SB();
#pragma unroll
    for (int s = 0; s < 4; s++)
#pragma unroll
      for (int rt = 0; rt < 3; rt++) acc[rt] = MFMA16(asbf(wf1[s]), asbf(xf[s][rt]), acc[rt]);
  }
  u32x2 hr[3];
  u32 hm[3];
  const char* xp[3];
#pragma unroll
  for (int rt = 0; rt < 3; rt++) {
    hr[rt] = *(const u32x2*)(lds + L_IMG + c16e * 528 + 8 * gqe + rt * 8448 + 32 * w);
    hm[rt] = 16 * rt + c16e < 40 ? ((const u32*)(lds + L_HM))[16 * rt + c16e] : 0u;
    xp[rt] = lds + L_IMG + 256 + 16 * gqe + (39 + __builtin_ctz(hm[rt] | 256u)) * 528;
    hm[rt] &= hm[rt] - 1;
  }
  STAMP(1, 12);
  __syncthreads();
  STAMP(1, 13);
  {
    u32x4 xf[2][3][4];
#pragma unroll
    for (int s = 0; s < 4; s++)
#pragma unroll
      for (int rt = 0; rt < 3; rt++) {
        xf[0][rt][s] = *(const u32x4*)(xbe + 256 + rt * 8448 + 64 * s);
        xf[1][rt][s] = *(const u32x4*)(xp[rt] + 64 * s);
      }
    SB();
#pragma unroll
    for (int s = 0; s < 4; s++)
#pragma unroll
      for (int j = 0; j < 2; j++)
#pragma unroll
        for (int rt = 0; rt < 3; rt++) acm[rt] = MFMA16(asbf(wf2[s]), asbf(xf[j][rt][s]), acm[rt]);
  }
  while (__builtin_amdgcn_ballot_w64((hm[0] | hm[1] | hm[2]) != 0) != 0) {
#pragma unroll
    for (int rt = 0; rt < 3; rt++) {
      const char* p = lds + L_IMG + 256 + 16 * gqe + (39 + __builtin_ctz(hm[rt] | 256u)) * 528;
      hm[rt] &= hm[rt] - 1;
#pragma unroll
      for (int s = 0; s < 4; s++) acm[rt] = MFMA16(asbf(wf2[s]), asbf(*(const u32x4*)(p + 64 * s)), acm[rt]);
    }
  }
  STAMP(1, 14);
  {
    const rsrc_t rs_o = mkrs(out + (size_t)n0 * 128, nn * 256);
#pragma unroll
    for (int rt = 0; rt < 3; rt++) {
      u32 o[2];
#pragma unroll
      for (int r = 0; r < 2; r++) {
        const f32x2 z = (f32x2){acc[rt][2 * r], acc[rt][2 * r + 1]} + (f32x2){acm[rt][2 * r], acm[rt][2 * r + 1]} * NLN2;
        const f32x2 t = z * NLOG2E;
        const f32x2 d = (f32x2){__builtin_amdgcn_exp2f(t[0]), __builtin_amdgcn_exp2f(t[1])} + 1.0f;
        const f32x2 v = z * (f32x2){__builtin_amdgcn_rcpf(d[0]), __builtin_amdgcn_rcpf(d[1])} + (f32x2){bf2f(hr[rt][r]), bfhi(hr[rt][r])};
        o[r] = pk2(v[0], v[1]);
      }
      __builtin_amdgcn_raw_buffer_store_b64((u32x2){o[0], o[1]}, rs_o, c16e * 256 + 8 * gqe, rt * 4096 + 32 * w, 0);
    }
  }
  if (w < 2) {
    const int nl = __umul24(tid2, 21846) >> 16, k = tid2 - 3 * nl;
    if (tid2 < 3 * nn) {
      const char* mp = lds + L_IMG + 512 + 4 * k;
      u32 hq = ((const u32*)(lds + L_HM))[nl];
      float pv = *(const float*)(mp + nl * 528) + bf2f(*(const u16*)(lds + L_NODE + nl * 2 + tid2 * 2));
      while (hq) {
        pv += *(const float*)(mp + (39 + __builtin_ctz(hq)) * 528);
        hq &= hq - 1;
      }
      out[(size_t)NN * 128 + n0 * 3 + tid2] = (u16)pk2(pv, 0.f);
    }
  }
  VMWAIT0();
  STAMP(1, 15); RSTAMP(1, 31);
}

extern "C" void kernel_launch(void* const* d_in, const int* in_sizes, int n_in, void* d_out, int out_size,
                              void* d_ws, size_t ws_size, hipStream_t stream) {
  const u16* h = (const u16*)d_in[0];
  const u16* pos = (const u16*)d_in[1];
  const u16* ea = (const u16*)d_in[2];
  const int* src = (const int*)d_in[3];
  const int* dst = (const int*)d_in[4];
  const u16* we = (const u16*)d_in[5];
  const u16* wx = (const u16*)d_in[6];
  const u16* wh = (const u16*)d_in[7];
  egnn_k1<<<NB, 512, 0, stream>>>(h, ea, src, dst, we, (char*)d_ws);
  egnn_k2<<<NB, 512, 0, stream>>>(h, pos, we, wx, wh, (const char*)d_ws, (u16*)d_out);
}
```

```cpp
#include <hip/hip_runtime.h>
#include <hip/hip_bf16.h>

typedef unsigned short u16;
typedef unsigned int u32;
typedef float f32x2 __attribute__((ext_vector_type(2)));
typedef float f32x4 __attribute__((ext_vector_type(4)));
typedef float f32x16 __attribute__((ext_vector_type(16)));
typedef __bf16 bf16x8 __attribute__((ext_vector_type(8)));
typedef __bf16 bf16x2 __attribute__((ext_vector_type(2)));
typedef u32 u32x2 __attribute__((ext_vector_type(2)));
typedef u32 u32x3 __attribute__((ext_vector_type(3)));
typedef u32 u32x4 __attribute__((ext_vector_type(4)));
typedef int i32x4 __attribute__((ext_vector_type(4)));
typedef short s16x4 __attribute__((ext_vector_type(4)));
typedef u16 u16x2 __attribute__((ext_vector_type(2)));
#define LAS __attribute__((address_space(3)))
typedef __amdgpu_buffer_rsrc_t rsrc_t;

#define PFWG 64
constexpr int NN = 10000;
constexpr int NE = 320000;
constexpr int NCH = 250;
constexpr int CH = 1280;
constexpr int NB = 256;
constexpr int CAP = 1408;
constexpr float NLOG2E = -1.4426950408889634f;
constexpr float NLN2 = -0.6931471805599453f;
constexpr u32 OH = 0x3F81u;
constexpr u32 OHINV = 0x81u;

constexpr size_t WS_T = 0;
constexpr size_t WS_R = WS_T + (size_t)NB * 256 * 4;
constexpr size_t WS_B = WS_R + (size_t)NE * 12;

__device__ __forceinline__ float bf2f(u32 v) { return __uint_as_float(v << 16); }
__device__ __forceinline__ float bfhi(u32 v) { return __uint_as_float(v & 0xffff0000u); }
__device__ __forceinline__ u32 pk2(float a, float b) {
  f32x2 v = {a, b};
  return __builtin_bit_cast(u32, __builtin_convertvector(v, bf16x2));
}
__device__ __forceinline__ u32 mul16x2(u32 a, u32 b) {
  return __builtin_bit_cast(u32, __builtin_bit_cast(u16x2, a) * __builtin_bit_cast(u16x2, b));
}
__device__ __forceinline__ int wave_incl_scan(int x) {
  x += __builtin_amdgcn_update_dpp(0, x, 0x111, 0xf, 0xf, false);
  x += __builtin_amdgcn_update_dpp(0, x, 0x112, 0xf, 0xf, false);
  x += __builtin_amdgcn_update_dpp(0, x, 0x114, 0xf, 0xf, false);
  x += __builtin_amdgcn_update_dpp(0, x, 0x118, 0xf, 0xf, false);
  x += __builtin_amdgcn_update_dpp(0, x, 0x142, 0xa, 0xf, false);
  x += __builtin_amdgcn_update_dpp(0, x, 0x143, 0xc, 0xf, false);
  return x;
}
__device__ __forceinline__ bf16x8 asbf(u32x4 v) { return __builtin_bit_cast(bf16x8, v); }
__device__ __forceinline__ u32x2 trread(const char* p) {
  return __builtin_bit_cast(u32x2, __builtin_amdgcn_ds_read_tr16_b64_v4i16((LAS s16x4*)p));
}
__device__ __forceinline__ rsrc_t mkrs(const void* p, u32 bytes) { return __builtin_amdgcn_make_buffer_rsrc((void*)p, 0, bytes, 0x00020000); }
__device__ __forceinline__ void dma16(rsrc_t rs, char* ldsp, u32 voff, u32 soff) {
  __builtin_amdgcn_raw_ptr_buffer_load_lds(rs, (LAS void*)ldsp, 16, voff, soff, 0, 0);
}
#define MFMA32(a, b, c) __builtin_amdgcn_mfma_f32_32x32x16_bf16(a, b, c, 0, 0, 0)
#define MFMA16(a, b, c) __builtin_amdgcn_mfma_f32_16x16x32_bf16(a, b, c, 0, 0, 0)
#define SB() __builtin_amdgcn_sched_barrier(0)
#define VMWAIT0() asm volatile("s_waitcnt vmcnt(0)" ::: "memory")
#define STAMPDECL
#define STAMP(buf, i)
#define STAMPV(buf, i)
#define RSTAMP(buf, i)
#define DRAIN()

template <int V> struct IC { static constexpr int value = V; };
__device__ __forceinline__ u32x4 mksrd(const void* p, u32 bytes) {
  const unsigned long long a = (unsigned long long)p;
  return (u32x4){(u32)a, (u32)(a >> 32) & 0xffffu, bytes, 0x00020000u};
}
#define DMA1(J, A, SRD, SOFF) asm volatile("buffer_load_dwordx4 %0, %1, %2 offen offset:" #J " lds" :: "v"(A), "s"(SRD), "s"(SOFF) : "memory")
#define LDB(T, DST, VO, SRD, SO) asm volatile("buffer_load_" T " %0, %1, %2, %3 offen" : "=v"(DST) : "v"(VO), "s"(SRD), "s"(SO) : "memory")
#define SETM0(V) asm volatile("s_mov_b32 m0, %0\n\ts_nop 0" :: "s"(V) : "memory")
#define VMWAIT(N) asm volatile("s_waitcnt vmcnt(" #N ")" ::: "memory")
#define LGKMWAIT0() asm volatile("s_waitcnt lgkmcnt(0)" ::: "memory")
#define PIN(x) do { asm volatile("" : "+v"(x)); SB(); } while (0)

constexpr int K1_H = 32768;
constexpr int K1_IMG = K1_H + 48 * 256;
constexpr int K1_HIST = K1_IMG + 16384;
constexpr int K1_LDS = K1_HIST + 2 * NB * 4 + 32;

extern "C" __global__ __launch_bounds__(512) void egnn_k1(const u16* __restrict__ h, const u16* __restrict__ ea,
                                               const int* __restrict__ src, const int* __restrict__ dst,
                                               const u16* __restrict__ we, char* __restrict__ ws) {
  __shared__ __attribute__((aligned(128))) char lds[K1_LDS];
  const int tid = threadIdx.x, lane = tid & 63;
  const int w = __builtin_amdgcn_readfirstlane(tid >> 6);
  const int c = (blockIdx.x & 7) * 32 + (blockIdx.x >> 3);
  const int gq = lane >> 4, c16 = lane & 15;
  u32* const hist = (u32*)(lds + K1_HIST);
  asm volatile("" :: "s"(h), "s"(ea), "s"(src), "s"(dst), "s"(we), "s"(ws));
  STAMPDECL; RSTAMP(0, 30); STAMP(0, 0);
  if (tid < NB) hist[tid] = 0;
  else if (tid < NB + 8) hist[NB + tid] = 0;
  __syncthreads();
  auto meet = [&](int k) {
    LGKMWAIT0();
    if (lane == 0) __hip_atomic_fetch_add(&hist[2 * NB + k], 1u, __ATOMIC_RELAXED, __HIP_MEMORY_SCOPE_WORKGROUP);
    while (__hip_atomic_load(&hist[2 * NB + k], __ATOMIC_RELAXED, __HIP_MEMORY_SCOPE_WORKGROUP) < 4u) __builtin_amdgcn_s_sleep(1);
    asm volatile("" ::: "memory");
  };
  if (w < 4) {
    const u32 eo = tid * 20, nb = c < NCH ? CH * 4 : 0;
    const rsrc_t rs_d = mkrs(dst + c * CH, nb), rs_s = mkrs(src + c * CH, nb), rs_a = mkrs(ea + (size_t)c * CH * 4, 2 * nb);
    const u32x4 d4 = __builtin_amdgcn_raw_buffer_load_b128(rs_d, eo, 0, 0);
    const u32 d1 = __builtin_amdgcn_raw_buffer_load_b32(rs_d, eo + 16, 0, 0);
    const u32x4 s4 = __builtin_amdgcn_raw_buffer_load_b128(rs_s, eo, 0, 0);
    const u32 s1 = __builtin_amdgcn_raw_buffer_load_b32(rs_s, eo + 16, 0, 0);
    const u32x4 a0 = __builtin_amdgcn_raw_buffer_load_b128(rs_a, 2 * eo, 0, 0);
    const u32x4 a1 = __builtin_amdgcn_raw_buffer_load_b128(rs_a, 2 * eo + 16, 0, 0);
    const u32x2 a2 = __builtin_amdgcn_raw_buffer_load_b64(rs_a, 2 * eo + 32, 0, 0);
    __builtin_amdgcn_s_setprio(1);
    STAMP(0, 1);
    if (c < NCH) {
      u32* const img = (u32*)(lds + K1_IMG);
      u32* const base = hist + NB;
      u32 bk[5], rk[5];
#pragma unroll
      for (int i = 0; i < 5; i++) {
        bk[i] = __umul24(i < 4 ? d4[i & 3] : d1, 214749u) >> 23;
        rk[i] = atomicAdd(&hist[bk[i]], 1u);
      }
      STAMP(0, 2);
      meet(1);
      STAMP(0, 3);
      {
        const u32x4 v = *(const u32x4*)(hist + 4 * lane);
        const int s = v[0] + v[1] + v[2] + v[3];
        const int ex = wave_incl_scan(s) - s;
        const u32x4 bs = {(u32)ex, ex + v[0], ex + v[0] + v[1], ex + v[0] + v[1] + v[2]};
        *(u32x4*)(base + 4 * lane) = bs;
        const u32 bb = w == 0 ? bs[0] : w == 1 ? bs[1] : w == 2 ? bs[2] : bs[3];
        const u32 vv = w == 0 ? v[0] : w == 1 ? v[1] : w == 2 ? v[2] : v[3];
        ((u32*)(ws + WS_T))[(4 * lane + w) * 256 + c] = bb | (vv << 16);
      }
#pragma unroll
      for (int i = 0; i < 5; i++) {
        u32* p = img + 3 * (base[bk[i]] + rk[i]);
        p[0] = i < 2 ? a0[2 * (i & 1)] : i < 4 ? a1[2 * (i & 1)] : a2[0];
        p[1] = i < 2 ? a0[2 * (i & 1) + 1] : i < 4 ? a1[2 * (i & 1) + 1] : a2[1];
        p[2] = (i < 4 ? s4[i & 3] : s1) | ((i < 4 ? d4[i & 3] : d1) << 16);
      }
      STAMP(0, 4);
      meet(2);
      STAMP(0, 5);
      const rsrc_t rs_r = mkrs(ws + WS_R + (size_t)c * (CH * 12), CH * 12);
#pragma unroll
      for (int i = 0; i < 4; i++)
        __builtin_amdgcn_raw_buffer_store_b128(((const u32x4*)img)[tid + 256 * i], rs_r, tid * 16 + i * 4096, 0, 0);
    }
    STAMP(0, 6); DRAIN(); STAMP(0, 7); RSTAMP(0, 31);
  } else {
    const int u = w - 4, n0 = (c * 625 + 15) >> 4, nn = (((c + 1) * 625 + 15) >> 4) - n0;
    const u32 ldsb = (u32)(size_t)(LAS char*)lds;
    {
      const u32x4 srd_h = mksrd(h + (size_t)n0 * 128, nn * 256), srd_w = mksrd(we + 128 * 128, 32768);
      const u32 vw = gq * 256 + 16 * (c16 ^ (4 * gq));
      SETM0(ldsb + u * 8192);
      DMA1(0, vw, srd_w, u * 8192); DMA1(1024, vw, srd_w, u * 8192); DMA1(2048, vw, srd_w, u * 8192); DMA1(3072, vw, srd_w, u * 8192);
      SETM0(ldsb + u * 8192 + 4096);
      DMA1(0, vw, srd_w, u * 8192 + 4096); DMA1(1024, vw, srd_w, u * 8192 + 4096); DMA1(2048, vw, srd_w, u * 8192 + 4096); DMA1(3072, vw, srd_w, u * 8192 + 4096);
      SETM0(ldsb + K1_H + u * 3072);
      DMA1(0, gq * 256 + 16 * (c16 ^ ((12 * u + gq) & 15)), srd_h, u * 3072);
      DMA1(1024, gq * 256 + 16 * (c16 ^ ((12 * u + 4 + gq) & 15)), srd_h, u * 3072);
      DMA1(2048, gq * 256 + 16 * (c16 ^ ((12 * u + 8 + gq) & 15)), srd_h, u * 3072);
    }
    STAMP(0, 1);
    VMWAIT0();
    STAMP(0, 2);
    u32 pfv = 0;
    const bool pf = blockIdx.x < PFWG && u < 3;
    if (pf) {
      unsigned long long pc;
      u32 off;
      asm volatile("s_getpc_b64 %0\n.Lpc%=:\n\ts_mov_b32 %1, egnn_k2-.Lpc%=" : "=s"(pc), "=s"(off));
      asm volatile("global_load_dword %0, %1, %2" : "=v"(pfv) : "v"(min(u * 64 + lane, 147) * 128), "s"(pc + off) : "memory");
    }
    meet(0);
    STAMP(0, 3);
    f32x4 acc[3][2];
#pragma unroll
    for (int rt = 0; rt < 3; rt++) acc[rt][0] = acc[rt][1] = (f32x4)(0.f);
    const char* wb = lds + (8 * gq + (c16 >> 2)) * 256 + 16 * ((4 * u + (c16 & 3)) ^ (c16 & 12));
    const int hb = K1_H + c16 * 256 + 16 * (gq ^ c16);
    u32x4 fa[4], fb[4], hf[4][3];
#pragma unroll
    for (int s = 0; s < 4; s++) {
      const u32x2 x0 = trread(wb + (32 * s) * 256), x1 = trread(wb + (32 * s + 4) * 256);
      const u32x2 y0 = trread(wb + (32 * s) * 256 + 8), y1 = trread(wb + (32 * s + 4) * 256 + 8);
      fa[s] = (u32x4){x0[0], x0[1], x1[0], x1[1]};
      fb[s] = (u32x4){y0[0], y0[1], y1[0], y1[1]};
#pragma unroll
      for (int rt = 0; rt < 3; rt++) hf[s][rt] = *(const u32x4*)(lds + (hb ^ (64 * s)) + rt * 4096);
    }
    SB();
#pragma unroll
    for (int s = 0; s < 4; s++)
#pragma unroll
      for (int rt = 0; rt < 3; rt++) {
        acc[rt][0] = MFMA16(asbf(fa[s]), asbf(hf[s][rt]), acc[rt][0]);
        acc[rt][1] = MFMA16(asbf(fb[s]), asbf(hf[s][rt]), acc[rt][1]);
      }
    const rsrc_t rs_b = mkrs(ws + WS_B + (size_t)n0 * 256, nn * 256);
#pragma unroll
    for (int rt = 0; rt < 3; rt++) {
      const f32x4 a = acc[rt][0] * NLOG2E, b = acc[rt][1] * NLOG2E;
      const u32x4 o = {pk2(a[0], a[1]), pk2(a[2], a[3]), pk2(b[0], b[1]), pk2(b[2], b[3])};
      __builtin_amdgcn_raw_buffer_store_b128(o, rs_b, c16 * 256 + 16 * gq + 64 * u + rt * 4096, 0, 0);
    }
    if (pf) asm volatile("s_waitcnt vmcnt(0)" :: "v"(pfv) : "memory");
    STAMP(0, 6); DRAIN(); STAMP(0, 7); RSTAMP(0, 31);
  }
}

constexpr int OHS = 2 * CAP + 16;
constexpr int L_NODE = 0;
constexpr int L_WX = L_NODE + 320;
constexpr int L_WS = L_WX + 512;
constexpr int L_REC = L_WS + 16;
constexpr int L_CV = L_REC + 16 * (CAP + 1);
constexpr int L_SRC = L_CV + 8 * (CAP + 2);
constexpr int L_OH = L_SRC + 2 * (CAP + 8);
constexpr int L_CNT = L_OH + 8 * OHS;
constexpr int L_HM = L_CNT + 256;
constexpr int L_IMG = L_HM + 160;
constexpr int L_AT = L_IMG + 48 * 528;
constexpr int L_STG = (L_AT + 128 * 96 + 127) & ~127;
constexpr int K2_LDS = L_STG + 65536;
constexpr int L_MG = L_STG + 32768;
static_assert(K2_LDS <= 163840, "LDS");
static_assert(L_CV % 16 == 0 && L_OH % 16 == 0 && L_SRC % 16 == 0 && L_IMG % 16 == 0 && L_AT % 16 == 0, "align");
static_assert(L_IMG - L_OH > 2 * 8192 && L_IMG - L_OH <= 3 * 8192, "zero sweep");

extern "C" __global__ __launch_bounds__(512, 2) void egnn_k2(const u16* __restrict__ h, const u16* __restrict__ pos,
                                                  const u16* __restrict__ we, const u16* __restrict__ wx,
                                                  const u16* __restrict__ wh, const char* __restrict__ ws,
                                                  u16* __restrict__ out) {
  __shared__ __attribute__((aligned(128))) char lds[K2_LDS];
  int tid = threadIdx.x, lane = tid & 63;
  const int w = __builtin_amdgcn_readfirstlane(tid >> 6);
  const int b = (blockIdx.x & 7) * 32 + (blockIdx.x >> 3);
  const int n0 = (b * 625 + 15) >> 4, nn = (((b + 1) * 625 + 15) >> 4) - n0;
  int gq = lane >> 4, c16 = lane & 15, hh = lane >> 5, n32 = lane & 31;
  const u32 ldsb = (u32)(size_t)(LAS char*)lds;
  const u32x4 srd_g = mksrd(ws + WS_B - 4096, 4096 + NN * 256), srd_pos = mksrd(pos, NN * 6);

  asm volatile("" :: "s"(h), "s"(pos), "s"(we), "s"(wx), "s"(wh), "s"(ws), "s"(out));
  STAMPDECL; RSTAMP(1, 30); STAMP(1, 0);
  u32 cwv[6];
  u32x4 hv[2], t4;
  if (w < 4) LDB("dwordx4", t4, lane * 16, mksrd(ws + WS_T + b * 1024, 1024), 0);
  {
    const u32x4 srd_h = mksrd(h + (size_t)n0 * 128, nn * 256);
    LDB("dwordx4", hv[0], tid * 16, srd_h, 0);
    LDB("dwordx4", hv[1], tid * 16, srd_h, 8192);
    const u32x4 srd_we = mksrd(we, 261 * 256);
    const u32 vw = gq * 256 + 16 * (c16 ^ (4 * gq));
    SETM0(ldsb + L_STG + w * 4096);
    DMA1(0, vw, srd_we, w * 4096); DMA1(1024, vw, srd_we, w * 4096); DMA1(2048, vw, srd_we, w * 4096); DMA1(3072, vw, srd_we, w * 4096);
    if (w >= 6) {
#pragma unroll
      for (int k = 0; k < 5; k++) LDB("ushort", cwv[k], tid * 2, srd_we, (256 + k) * 256 - 768);
      LDB("ushort", cwv[5], tid * 2 - 768, mksrd(wx, 256), 0);
    } else {
      LDB("ushort", cwv[0], tid * 2 - 512, srd_pos, n0 * 6);
    }
  }
  SB();
#pragma unroll
  for (int i = 0; i < 2; i++) *(u32x4*)(lds + L_OH + tid * 16 + i * 8192) = (u32x4)(0u);
  if (tid < (L_IMG - L_OH - 2 * 8192) / 16) *(u32x4*)(lds + L_OH + tid * 16 + 2 * 8192) = (u32x4)(0u);
  {
    char* ip = lds + L_IMG + (tid >> 4) * 528 + 256 + c16 * 16;
    *(u32x4*)ip = (u32x4)(0u);
    if (w < 4) *(u32x4*)(ip + 32 * 528) = (u32x4)(0u);
    else if (tid < 304) *(u32x4*)(lds + L_IMG + (tid - 256) * 528 + 512) = (u32x4)(0u);
  }
  u32x4 id0, id1, one4 = {hh ? 0u : 0x3F80u, 0u, 0u, 0u};
  int ga[4], recoff, ohoff, cvoff, axoff, srcoff;
  u32 go[4], vsrc;
  auto lane_consts = [&]() {
    const int k0 = n32 - 8 * hh;
    const u32 one = 0x3F80u << ((k0 & 1) * 16);
#pragma unroll
    for (int i = 0; i < 4; i++) {
      id0[i] = (k0 >> 1) == i ? one : 0u;
      id1[i] = (k0 >> 1) == 8 + i ? one : 0u;
    }
    const int prow = (n32 & 19) | ((n32 & 4) << 1) | ((n32 & 8) >> 1);
    const int sa = prow & 3, sb = prow >> 2;
#pragma unroll
    for (int s = 0; s < 4; s++) ga[s] = (L_STG + w * 8192 + (8 * sa + sb) * 128 + 16 * (hh ^ (2 * sa + ((sb >> 1) & 1)))) ^ (32 * s);
#pragma unroll
    for (int j = 0; j < 4; j++) go[j] = 4096 - 1024 * j + 16 * ((lane & 7) ^ (2 * j + ((lane >> 4) & 1)));
    recoff = L_REC + prow * 16;
    ohoff = L_OH + (n32 >> 2) * OHS + 16 * hh;
    cvoff = L_CV + (8 * hh + (c16 >> 2)) * 8;
    axoff = L_OH + (c16 >> 2) * OHS + 32 * (gq & 1) + 16 * (c16 & 1) + 8 * ((c16 >> 1) & 1);
    srcoff = L_SRC + (lane >> 3) * 8;
    vsrc = (lane >> 1) * 256 + 32 * w + 16 * (lane & 1);
  };
  lane_consts();
  const u32 m0a = ldsb + L_STG + w * 8192, m0b = m0a + 4096;
  asm volatile("" : "+v"(one4), "+v"(id0), "+v"(id1), "+v"(ga[0]), "+v"(ga[1]), "+v"(ga[2]), "+v"(ga[3]), "+v"(go[0]), "+v"(go[1]), "+v"(go[2]), "+v"(go[3]));
  asm volatile("" : "+v"(recoff), "+v"(ohoff), "+v"(cvoff), "+v"(axoff), "+v"(srcoff), "+v"(vsrc));
  SB();
  int mtot, scnt, sexc;
  u32 sbase;
  auto decode = [&]() {
    if (lane == 62) t4[2] = t4[3] = 0;
    if (lane == 63) t4 = (u32x4)(0u);
    const int c0 = t4[0] >> 16, c1 = c0 + (t4[1] >> 16), c2 = c1 + (t4[2] >> 16), c3 = c2 + (t4[3] >> 16);
    const int inc = wave_incl_scan(c3);
    mtot = __builtin_amdgcn_readlane(inc, 63);
    const int wq = w & 3;
    const u32 tv = wq == 0 ? t4[0] : wq == 1 ? t4[1] : wq == 2 ? t4[2] : t4[3];
    scnt = tv >> 16;
    sexc = inc - c3 + (wq == 0 ? 0 : wq == 1 ? c0 : wq == 2 ? c1 : c2);
    sbase = ((4 * lane + wq) * CH + (tv & 0xffffu)) * 12;
  };
  if (w < 4) {
    asm volatile("s_waitcnt vmcnt(7)" ::: "memory"); SB();
    STAMP(1, 1);
    decode();
    if (tid == 0) *(int*)(lds + L_WS) = mtot;
    if (mtot <= CAP) {
      u32* p = (u32*)(lds + L_CV) + sexc;
      u32 v = sbase;
#pragma clang loop vectorize(disable) unroll(disable)
      for (int i = 0; i < scnt; i++, p++, v += 12) *p = v;
    }
  }
  asm volatile("s_waitcnt vmcnt(0)" ::: "memory"); SB();
  asm volatile("" : "+v"(cwv[0]), "+v"(cwv[1]), "+v"(cwv[2]), "+v"(cwv[3]), "+v"(cwv[4]), "+v"(cwv[5]));
  {
    char* hp = lds + L_IMG + (tid >> 4) * 528 + c16 * 16;
    *(u32x4*)hp = hv[0];
    if (w < 4) *(u32x4*)(hp + 32 * 528) = hv[1];
  }
  if (w == 4 || w == 5) {
    const int t3 = tid - 256, nl = __umul24(t3, 21846) >> 16;
    if (t3 < 120) *(u16*)(lds + L_NODE + nl * 2 + t3 * 2) = (u16)cwv[0];
  }
  STAMP(1, 3);
  __syncthreads();
  STAMP(1, 4);
  u32x3 rec[3];
  bool general;
  {
    const u32x4 srd_rec = mksrd(ws + WS_R, NE * 12);
    u32 ro[3];
#pragma unroll
    for (int k = 0; k < 3; k++) ro[k] = ((const u32*)(lds + L_CV))[tid + 512 * k];
    mtot = __builtin_amdgcn_readfirstlane(*(const int*)(lds + L_WS));
    general = mtot > CAP;
    const int lim = general ? 0 : mtot;
#pragma unroll
    for (int k = 0; k < 3; k++) LDB("dwordx3", rec[k], tid + 512 * k < lim ? ro[k] : 0x7fffff00u, srd_rec, 0);
  }
  if (general) asm volatile("s_waitcnt vmcnt(0)" :: "v"(rec[0]), "v"(rec[1]), "v"(rec[2]) : "memory");
  SB();
  if (w >= 6) {
    const float c0 = bf2f(cwv[0]) * NLOG2E;
    const u32 d_hi = pk2(c0, 0.f);
    const u32x4 o = {pk2(bf2f(cwv[1]) * NLOG2E, bf2f(cwv[2]) * NLOG2E), pk2(bf2f(cwv[3]) * NLOG2E, bf2f(cwv[4]) * NLOG2E),
                     pk2(c0, c0 - bf2f(d_hi)), 0u};
    *(u32x4*)(lds + L_AT + (tid - 384) * 96 + 80) = o;
    ((float*)(lds + L_WX))[tid - 384] = bf2f(cwv[5]) * NLN2;
  }
  const char* const xb = lds + L_IMG + c16 * 528 + 16 * gq;
  if (w < 4) {
    f32x4 acc[3][2];
#pragma unroll
    for (int rt = 0; rt < 3; rt++) acc[rt][0] = acc[rt][1] = (f32x4)(0.f);
    const char* wb = lds + L_STG + (8 * gq + (c16 >> 2)) * 256 + 16 * ((4 * w + (c16 & 3)) ^ (c16 & 12));
    u32x4 fa[4], fb[4], hf[4][3];
#pragma unroll
    for (int s = 0; s < 4; s++) {
      const u32x2 x0 = trread(wb + (32 * s) * 256), x1 = trread(wb + (32 * s + 4) * 256);
      const u32x2 y0 = trread(wb + (32 * s) * 256 + 8), y1 = trread(wb + (32 * s + 4) * 256 + 8);
      fa[s] = (u32x4){x0[0], x0[1], x1[0], x1[1]};
      fb[s] = (u32x4){y0[0], y0[1], y1[0], y1[1]};
#pragma unroll
      for (int rt = 0; rt < 3; rt++) hf[s][rt] = *(const u32x4*)(xb + rt * 8448 + 64 * s);
    }
    SB();
#pragma unroll
    for (int s = 0; s < 4; s++)
#pragma unroll
      for (int rt = 0; rt < 3; rt++) {
        acc[rt][0] = MFMA16(asbf(hf[s][rt]), asbf(fa[s]), acc[rt][0]);
        acc[rt][1] = MFMA16(asbf(hf[s][rt]), asbf(fb[s]), acc[rt][1]);
      }
    char* ap = lds + L_AT + (32 * w + 8 * (c16 >> 2) + (c16 & 3)) * 96 + 8 * gq;
#pragma unroll
    for (int rt = 0; rt < 3; rt++)
      if (16 * rt + 4 * gq < 40)
#pragma unroll
        for (int j = 0; j < 2; j++) {
          const f32x4 a = acc[rt][j] * (NLOG2E / 1.0078125f);
          *(u32x2*)(ap + j * 384 + rt * 32) = (u32x2){pk2(a[0], a[1]), pk2(a[2], a[3])};
        }
  }
  f32x16 Y[4], XA, XB, e;
  u32x4 sp0 = (u32x4)(0u), sp1 = (u32x4)(0u), pa = (u32x4)(0u), pb = (u32x4)(0u);
  asm volatile("" : "+v"(sp0), "+v"(sp1), "+v"(pa), "+v"(pb));
  Y[0] = MFMA32(asbf(pa), asbf(pa), (f32x16)(0.f));
  Y[1] = MFMA32(asbf(pb), asbf(pb), (f32x16)(0.f));
  Y[2] = MFMA32(asbf(sp0), asbf(sp0), (f32x16)(0.f));
  Y[3] = MFMA32(asbf(sp1), asbf(sp1), (f32x16)(0.f));
  XB = MFMA32(asbf(pa), asbf(pb), (f32x16)(0.f));
  asm volatile("" : "+v"(Y[0]), "+v"(Y[1]), "+v"(Y[2]), "+v"(Y[3]), "+v"(XB));
  SB();
  STAMP(1, 28);
  u32x4 wex[4];
  u32 ad[4];
  int ninc;
  u32x4 srd = srd_g;
  u32 sfb = 128;
  auto prep = [&](u32x2 sv, bool real) {
    if (real) {
      asm("v_mad_u32_u16 %0, %1, %2, %3" : "=v"(ad[0]) : "v"(sv[0]), "s"(256), "v"(go[0]));
      asm("v_mad_u32_u16 %0, %1, %2, %3 op_sel:[1,0,0,0]" : "=v"(ad[1]) : "v"(sv[0]), "s"(256), "v"(go[1]));
      asm("v_mad_u32_u16 %0, %1, %2, %3" : "=v"(ad[2]) : "v"(sv[1]), "s"(256), "v"(go[2]));
      asm("v_mad_u32_u16 %0, %1, %2, %3 op_sel:[1,0,0,0]" : "=v"(ad[3]) : "v"(sv[1]), "s"(256), "v"(go[3]));
    } else {
#pragma unroll
      for (int j = 0; j < 4; j++) ad[j] = vsrc + 7168 * j;
      srd = mksrd(wh, 65536);
      sfb = 32768;
    }
  };
  auto issue_a = [&]() { SETM0(m0a); DMA1(0, ad[0], srd, 0); DMA1(1024, ad[1], srd, 0); DMA1(2048, ad[2], srd, 0); DMA1(3072, ad[3], srd, 0); };
  auto issue_b = [&]() { SETM0(m0b); DMA1(0, ad[0], srd, sfb); DMA1(1024, ad[1], srd, sfb); DMA1(2048, ad[2], srd, sfb); DMA1(3072, ad[3], srd, sfb); };
  auto node_at = [&](int x) { return (int)__builtin_popcountll(__builtin_amdgcn_ballot_w64(ninc <= x)); };
  auto loadw = [&](int base) {
    const int g0 = base >> 3, bm = base & 7;
    const char* pa = lds + L_AT + n32 * 96 + (hh ? g0 * 16 : 80);
    const char* pb = lds + L_AT + n32 * 96 + (hh ? g0 * 16 + 16 : 80);
    const unsigned long long lo = ~0ull << (16 * min(bm, 3)) << (bm > 3 ? 16 : 0), hi = bm > 4 ? ~0ull << (16 * (bm - 4)) : ~0ull;
    const u32 mk[4] = {(u32)lo, (u32)(lo >> 32), (u32)hi, (u32)(hi >> 32)};
#pragma unroll
    for (int t = 0; t < 4; t++) {
      const u32x4 a = *(const u32x4*)(pa + t * 3072), bb = *(const u32x4*)(pb + t * 3072);
#pragma unroll
      for (int i = 0; i < 4; i++) wex[t][i] = (a[i] & mk[i]) | (bb[i] & ~mk[i]);
    }
  };
  auto pdot = [&](auto nbc) {
    constexpr int nb = decltype(nbc)::value;
    f32x4 p4[2] = {(f32x4)(0.f), (f32x4)(0.f)};
#pragma unroll
    for (int q0 = 0; q0 < 16; q0 += nb) {
      f32x4 wv[16];
#pragma unroll
      for (int q = q0; q < q0 + nb; q++) wv[q] = *(const f32x4*)(lds + L_WX + (8 * q + 4 * hh) * 4);
      SB();
#pragma unroll
      for (int q = q0; q < q0 + nb; q++)
        p4[q & 1] += (f32x4){Y[q >> 2][4 * (q & 3)], Y[q >> 2][4 * (q & 3) + 1], Y[q >> 2][4 * (q & 3) + 2], Y[q >> 2][4 * (q & 3) + 3]} * wv[q];
      SB();
    }
    p4[0] += p4[1];
    const f32x2 pa = (f32x2){p4[0][0], p4[0][1]} + (f32x2){p4[0][2], p4[0][3]};
    const u32 ph = __float_as_uint(pa[0] + pa[1]);
    const auto sw = __builtin_amdgcn_permlane32_swap(ph, ph, false, false);
    return __uint_as_float(sw[0]) + __uint_as_float(sw[1]);
  };
  auto place = [&](u32x3 rec, u32x2 ps, u32x2 pd, u32 dl, u32 p) {
    const u32 sh = (rec[2] & 1) * 16;
    const u32 sxy = __builtin_amdgcn_alignbit(ps[1], ps[0], sh), sz = ps[1] >> sh;
    const u32 dxy = pk2(bf2f(pd[0]) - bf2f(sxy), bfhi(pd[0]) - bfhi(sxy));
    const u32 dz1 = pk2(bf2f(pd[1]) - bf2f(sz), 1.0f);
    const float fx = bf2f(dxy), fy = bfhi(dxy), fz = bf2f(dz1);
    const float d2 = fx * fx + fy * fy + fz * fz;
    *(u32x4*)(lds + L_REC + p * 16) = (u32x4){rec[0], rec[1], pk2(d2, d2), dl};
    *(u32x2*)(lds + L_CV + p * 8) = (u32x2){mul16x2(dxy, OHINV * 0x10001u), mul16x2(dz1, OHINV * 0x10001u)};
    *(u16*)(lds + L_OH + (dl & 7) * OHS + 2 * p) = (u16)OH;
  };
  auto count_nodes = [&](int m) {
    const int cn = ((const u32*)(lds + L_CNT))[lane];
    ninc = wave_incl_scan(cn);
    if (w == 7) {
      const int p = m + n32 < ((m + 31) & ~31) ? m + n32 : CAP;
      *(u32x4*)(lds + L_REC + p * 16) = (u32x4)(0u);
      ((u16*)(lds + L_SRC))[p] = 0;
    }
    return ninc - cn;
  };
#define RDG(s, half) (*(const u32x4*)(lds + ga[s] + (half) * 4096))
#define EXP_(lo, hi) _Pragma("unroll") for (int r = lo; r < hi; r++) e[r] = __builtin_amdgcn_exp2f(xc[r]); SB()
#define RCP_(lo, hi) _Pragma("unroll") for (int r = lo; r < hi; r++) e[r] = __builtin_amdgcn_rcpf(e[r]); SB()
#define PACK_() _Pragma("unroll") for (int i = 0; i < 4; i++) { pa[i] = pk2(e[2 * i], e[2 * i + 1]); pb[i] = pk2(e[8 + 2 * i], e[8 + 2 * i + 1]); } \
  asm volatile("" : "+v"(pa), "+v"(pb)); SB()
#define SPROD(o, ca, cb) (u32x4){mul16x2(o[0], ca[0]), mul16x2(o[1], ca[1]), mul16x2(o[2], cb[0]), mul16x2(o[3], cb[1])}

  const int m = mtot;
  const bool third = w * 64 + 1024 < m;
  u32 rk[3], dl[3];
  u32x2 ps[3], pd[3];
  if (!general) {
    VMWAIT0(); SB();
    STAMP(1, 27);
#pragma unroll
    for (int k = 0; k < 3; k++)
      if (k < 2 || third) {
        LDB("dwordx2", ps[k], ((rec[k][2] & 0xffffu) * 6) & ~3u, srd_pos, 0);
        dl[k] = tid + 512 * k < m ? (rec[k][2] >> 16) - n0 : 48u + c16;
        rk[k] = atomicAdd((u32*)(lds + L_CNT) + dl[k], 1u);
        pd[k] = *(const u32x2*)(lds + L_NODE + dl[k] * 8);
      }
    STAMP(1, 5);
    __syncthreads();
    STAMP(1, 6);
    const int nst = count_nodes(m);
    const int nprev = __builtin_amdgcn_ds_bpermute(lane * 4 - 28, ninc);
#pragma unroll
    for (int k = 0; k < 3; k++)
      if (k < 2 || third) {
        rk[k] += __builtin_amdgcn_ds_bpermute(dl[k] * 4, nst);
        rk[k] = dl[k] < 48 ? rk[k] : CAP;
        *(u16*)(lds + L_SRC + 2 * rk[k]) = (u16)rec[k][2];
      }
    general = m < 256 || __builtin_amdgcn_ballot_w64(lane >= 7 && lane < nn && ninc - nprev <= 30) != 0;
    if (general) asm volatile("s_waitcnt vmcnt(0)" :: "v"(ps[0]), "v"(ps[1]), "v"(ps[2]) : "memory");
  }
  if (!general) {
    {
      const int nt = (m + 31) >> 5, tq = nt >> 3, trm = nt & 7;
      const int tbeg = w * tq + min(w, trm);
      const int tend = tbeg + tq + (w < trm ? 1 : 0);
      srcoff += tbeg * 64;
      __syncthreads();
      STAMP(1, 29);
      const u32x2 sv0 = *(const u32x2*)(lds + srcoff), sv1 = *(const u32x2*)(lds + srcoff + 64);
      prep(sv0, true);
      if (w < 4) {
        issue_a();
        issue_b();
        VMWAIT(8);
      } else {
        VMWAIT(0);
      }
      SB();
      STAMP(1, 26);
      int base = node_at(tbeg * 32);
      loadw(base);
      const int lnode = node_at(min(tend * 32, m) - 1);
      const int hnode = (tbeg > 0 && node_at(tbeg * 32 - 1) == base) ? base : -1;
      if (hnode >= 0 && lane == 0) __hip_atomic_fetch_or((u32*)(lds + L_HM) + hnode, 1u << w, __ATOMIC_RELAXED, __HIP_MEMORY_SCOPE_WORKGROUP);
      recoff += tbeg * 512; cvoff += tbeg * 256 - 256; ohoff += tbeg * 64 - 64; axoff += tbeg * 64;
#pragma unroll
      for (int k = 0; k < 3; k++)
        if (k < 2 || third) place(rec[k], ps[k], pd[k], dl[k], rk[k]);
      if (w >= 4) {
        issue_a();
        issue_b();
      }
      prep(sv1, tbeg + 1 < tend);
#pragma unroll
      for (int t = 0; t < 4; t++) asm volatile("" : "+v"(wex[t]));
      auto flush = [&](int base, int lim, auto clearc) {
        const int node = base + (((n32 >> 2) - base) & 7), comp = n32 & 3;
        constexpr bool clear = decltype(clearc)::value != 0;
        const float pt = pdot(IC<(clear ? 2 : 16)>{});
        if (node < lim) {
          char* mp = lds + L_IMG + (node == hnode ? 39 + w : node) * 528;
          if (comp == 3) {
#pragma unroll
            for (int q = 0; q < 16; q++)
              *(u32x2*)(mp + 256 + 8 * hh + 16 * q) = (u32x2){pk2(Y[q >> 2][4 * (q & 3)], Y[q >> 2][4 * (q & 3) + 1]),
                                                              pk2(Y[q >> 2][4 * (q & 3) + 2], Y[q >> 2][4 * (q & 3) + 3])};
          } else {
            *(float*)(mp + 512 + 4 * comp) = pt;
          }
        }
        if (clear) {
          const float keep = node < lim ? 0.f : 1.f;
#pragma unroll
          for (int t = 0; t < 4; t++) Y[t] *= keep;
        }
      };
      STAMP(1, 7);
      __syncthreads();
      STAMP(1, 8);
      u32x4 aex;
      {
        const u32x2 t0 = trread(lds + axoff), t1 = trread(lds + axoff + 4 * OHS);
        const u32x4 ra = *(const u32x4*)(lds + recoff);
        aex = hh ? (u32x4){t0[0], t0[1], t1[0], t1[1]} : ra;
      }
      XA = MFMA32(asbf(aex), asbf(wex[0]), (f32x16)(0.f)); PIN(XA);
      VMWAIT(4);
      STAMP(1, 23);
      u32x4 g0 = RDG(0, 0), g1 = RDG(1, 0), g2 = RDG(2, 0), g3 = RDG(3, 0);
      LGKMWAIT0();
      SB();
      XA = MFMA32(asbf(g0), asbf(id0), XA); PIN(XA);
      issue_a();
      XA = MFMA32(asbf(g1), asbf(id1), XA); PIN(XA);
      int fbase = base;
      __builtin_amdgcn_s_setprio(3);
      STAMP(1, 9);
      int tcount_ = 0;
#define EXPX(X, lo, hi) _Pragma("unroll") for (int r = lo; r < hi; r++) e[r] = __builtin_amdgcn_exp2f(X[r]); SB()
#define PACKX(X) _Pragma("unroll") for (int i = 0; i < 4; i++) { pa[i] = pk2(X[2 * i], X[2 * i + 1]); pb[i] = pk2(X[8 + 2 * i], X[8 + 2 * i + 1]); } \
  asm volatile("" : "+v"(pa), "+v"(pb)); SB()
#define SEG(t, p, sp) Y[t] = MFMA32(asbf(p), asbf(sp), Y[t]); PIN(Y[t])
#define PLUS1() e = MFMA32(asbf(one4), asbf(one4), e); PIN(e)
      for (int left = tend - tbeg, x = tbeg * 32 + 32; left > 0; left--, x += 32) {
        STAMPV(1, 16 + min(tcount_, 4)); tcount_++;
        if (__builtin_expect(left == 1, 0)) __builtin_amdgcn_s_setprio(1);
        SB();
        SEG(2, pa, sp0);
        EXPX(XA, 0, 8);
        SEG(2, pb, sp1);
        EXPX(XA, 8, 16);
        PLUS1();
        PACKX(XB);
        srcoff += 64; recoff += 512; cvoff += 256; ohoff += 64; axoff += 64;
        asm volatile("" : "+v"(srcoff), "+v"(recoff), "+v"(cvoff), "+v"(ohoff), "+v"(axoff)); SB();
        RCP_(0, 4);
        XB = MFMA32(asbf(aex), asbf(wex[1]), (f32x16)(0.f)); PIN(XB);
        RCP_(4, 8);
        XB = MFMA32(asbf(g2), asbf(id0), XB); PIN(XB);
        RCP_(8, 12);
        XB = MFMA32(asbf(g3), asbf(id1), XB); PIN(XB);
        RCP_(12, 16);
        XA = XA * e; PIN(XA);
        SEG(3, pa, sp0);
        VMWAIT(4);
        g0 = RDG(0, 1); g1 = RDG(1, 1); g2 = RDG(2, 1); g3 = RDG(3, 1);
        const u32x2 sv = *(const u32x2*)(lds + srcoff + 64);
        SB();
        EXPX(XB, 0, 8);
        SEG(3, pb, sp1);
        const u32x2 c0 = trread(lds + cvoff), c1 = trread(lds + cvoff + 32), c2 = trread(lds + cvoff + 128), c3 = trread(lds + cvoff + 160);
        const u32x4 o0 = *(const u32x4*)(lds + ohoff), o1 = *(const u32x4*)(lds + ohoff + 32);
        SB();
        EXPX(XB, 8, 16);
        PLUS1();
        PACKX(XA);
        sp0 = SPROD(o0, c0, c1);
        sp1 = SPROD(o1, c2, c3);
        asm volatile("" : "+v"(sp0), "+v"(sp1));
        LGKMWAIT0();
        SB();
        SETM0(m0b); DMA1(0, ad[0], srd, sfb); SB();
        RCP_(0, 4);
        XA = MFMA32(asbf(aex), asbf(wex[2]), (f32x16)(0.f)); PIN(XA);
        DMA1(1024, ad[1], srd, sfb); SB();
        RCP_(4, 8);
        XA = MFMA32(asbf(g0), asbf(id0), XA); PIN(XA);
        DMA1(2048, ad[2], srd, sfb); SB();
        RCP_(8, 12);
        XA = MFMA32(asbf(g1), asbf(id1), XA); PIN(XA);
        DMA1(3072, ad[3], srd, sfb); SB();
        RCP_(12, 16);
        XB = XB * e; PIN(XB);
        if (__builtin_expect(fbase != base, 0)) {
          flush(fbase, base, IC<1>{});
          fbase = base;
        }
        if (__builtin_expect(left <= 2, 0)) {
          if (left == 2) __builtin_amdgcn_s_setprio(2);
          else __builtin_amdgcn_s_setprio(0);
          prep(sv, false);
        } else {
          prep(sv, true);
        }
        asm volatile("" : "+v"(ad[0]), "+v"(ad[1]), "+v"(ad[2]), "+v"(ad[3]));
        SB();
        SEG(0, pa, sp0);
        const int nlast = node_at(min(x + 31, m - 1));
        SB();
        EXPX(XA, 0, 8);
        SEG(0, pb, sp1);
        EXPX(XA, 8, 16);
        PLUS1();
        PACKX(XB);
        RCP_(0, 4);
        XB = MFMA32(asbf(aex), asbf(wex[3]), (f32x16)(0.f)); PIN(XB);
        RCP_(4, 8);
        XB = MFMA32(asbf(g2), asbf(id0), XB); PIN(XB);
        RCP_(8, 12);
        XB = MFMA32(asbf(g3), asbf(id1), XB); PIN(XB);
        RCP_(12, 16);
        XA = XA * e; PIN(XA);
        if (__builtin_expect(left > 1 && nlast >= base + 8, 0)) {
          base = node_at(x);
          loadw(base);
        }
        SB();
        SEG(1, pa, sp0);
        VMWAIT(4);
        const u32x2 t0 = trread(lds + axoff), t1 = trread(lds + axoff + 4 * OHS);
        const u32x4 ra = *(const u32x4*)(lds + recoff);
        g0 = RDG(0, 0); g1 = RDG(1, 0); g2 = RDG(2, 0); g3 = RDG(3, 0);
        SB();
        EXPX(XB, 0, 8);
        SEG(1, pb, sp1);
        EXPX(XB, 8, 16);
        PLUS1();
        PACKX(XA);
        LGKMWAIT0();
        SB();
        aex = hh ? (u32x4){t0[0], t0[1], t1[0], t1[1]} : ra;
        PIN(aex);
        SETM0(m0a); DMA1(0, ad[0], srd, 0); SB();
        RCP_(0, 4);
        XA = MFMA32(asbf(aex), asbf(wex[0]), (f32x16)(0.f)); PIN(XA);
        DMA1(1024, ad[1], srd, 0); SB();
        RCP_(4, 8);
        XA = MFMA32(asbf(g0), asbf(id0), XA); PIN(XA);
        DMA1(2048, ad[2], srd, 0); SB();
        RCP_(8, 12);
        XA = MFMA32(asbf(g1), asbf(id1), XA); PIN(XA);
        DMA1(3072, ad[3], srd, 0); SB();
        RCP_(12, 16);
        XB = XB * e; PIN(XB);
      }
      SEG(2, pa, sp0);
      SEG(2, pb, sp1);
      PACKX(XB);
      SEG(3, pa, sp0);
      SEG(3, pb, sp1);
      STAMP(1, 10);
      flush(base, lnode + 1, IC<0>{});
      STAMP(1, 11);
    }
  } else {
    tid = threadIdx.x;
    asm volatile("" : "+v"(tid));
    lane = tid & 63; gq = lane >> 4; c16 = lane & 15; hh = lane >> 5; n32 = lane & 31;
    lane_consts();
    t4 = *(const u32x4*)(ws + WS_T + b * 1024 + lane * 16);
    decode();
    const rsrc_t rs_rec = mkrs(ws + WS_R, NE * 12), rs_pos = mkrs(pos, NN * 6);
    for (int q = tid; q < 40 * 33; q += 512) *(u32x4*)(lds + L_MG + q * 16) = (u32x4)(0u);
    for (int r0 = 0; r0 < mtot; r0 += CAP) {
      const int m = min(mtot - r0, CAP);
      __syncthreads();
      for (int q = tid; q < (L_HM - L_OH) / 16; q += 512) *(u32x4*)(lds + L_OH + q * 16) = (u32x4)(0u);
      const int lim = min(scnt, r0 + CAP - sexc);
      for (int i = max(r0 - sexc, 0) + (w >> 2); i < lim; i += 2)
        *(u32x3*)(lds + L_STG + (sexc + i - r0) * 16) = __builtin_amdgcn_raw_buffer_load_b96(rs_rec, sbase + i * 12, 0, 0);
      __syncthreads();
      for (int f = tid; f < m; f += 512) {
        u32* rp = (u32*)(lds + L_STG + f * 16);
        rp[3] = atomicAdd((u32*)(lds + L_CNT) + ((rp[2] >> 16) - n0), 1u);
      }
      __syncthreads();
      const int nst = count_nodes(m);
      for (int f0 = w * 64; f0 < m; f0 += 512) {
        const bool ok = f0 + lane < m;
        const u32x4 rc = *(const u32x4*)(lds + L_STG + (f0 + lane) * 16);
        const u32 dl = ok ? (rc[2] >> 16) - n0 : 0u;
        const u32 pp = rc[3] + __builtin_amdgcn_ds_bpermute(dl * 4, nst), p = ok ? pp : CAP;
        const u32x2 ps = __builtin_amdgcn_raw_buffer_load_b64(rs_pos, ((rc[2] & 0xffffu) * 6) & ~3u, 0, 0);
        *(u16*)(lds + L_SRC + 2 * p) = (u16)rc[2];
        place((u32x3){rc[0], rc[1], rc[2]}, ps, *(const u32x2*)(lds + L_NODE + dl * 8), dl, p);
      }
      __syncthreads();
      const int nt = w < 4 ? (m + 31) >> 5 : 0;
      for (int tile = w; tile < nt; tile += 4) {
        const int last = node_at(min(tile * 32 + 31, m - 1));
        prep(*(const u32x2*)(lds + srcoff + tile * 64), true);
        SETM0(m0a); DMA1(0, ad[0], srd_g, 0); DMA1(1024, ad[1], srd_g, 0); DMA1(2048, ad[2], srd_g, 0); DMA1(3072, ad[3], srd_g, 0);
        SETM0(m0b); DMA1(0, ad[0], srd_g, 128); DMA1(1024, ad[1], srd_g, 128); DMA1(2048, ad[2], srd_g, 128); DMA1(3072, ad[3], srd_g, 128);
        const u32x2 t0 = trread(lds + axoff + tile * 64), t1 = trread(lds + axoff + tile * 64 + 4 * OHS);
        const u32x4 ra = *(const u32x4*)(lds + recoff + tile * 512);
        const u32x4 aex = hh ? (u32x4){t0[0], t0[1], t1[0], t1[1]} : ra;
        for (int base = node_at(tile * 32); base <= last; base += 8) {
          loadw(base);
          u32x4 sp[2];
#pragma unroll
          for (int s = 0; s < 2; s++) {
            const u32x2 c0 = trread(lds + cvoff + tile * 256 + s * 128), c1 = trread(lds + cvoff + tile * 256 + s * 128 + 32);
            const u32x4 oh = *(const u32x4*)(lds + ohoff + tile * 64 + 32 * s);
            sp[s] = SPROD(oh, c0, c1);
          }
#pragma unroll 1
          for (int j = 0; j < 16; j++) {
            const int dn = *(const u32*)(lds + L_REC + (tile * 32 + 16 * (j >> 3) + 8 * hh + (j & 7)) * 16 + 12);
            const u32 mk = (dn < base || dn >= base + 8) ? ((j & 1) ? 0x0000ffffu : 0xffff0000u) : 0xffffffffu;
#pragma unroll
            for (int i = 0; i < 8; i++) sp[i >> 2][i & 3] &= (i == (j >> 1)) ? mk : 0xffffffffu;
          }
          VMWAIT0();
#pragma unroll
          for (int t = 0; t < 4; t++) {
            const u32x4 ga_ = RDG((2 * t) & 3, t >> 1), gb_ = RDG((2 * t + 1) & 3, t >> 1);
            f32x16 xc = MFMA32(asbf(aex), asbf(wex[t]), (f32x16)(0.f)), e;
            xc = MFMA32(asbf(ga_), asbf(id0), xc);
            xc = MFMA32(asbf(gb_), asbf(id1), xc);
            u32x4 pa, pb;
            EXP_(0, 16);
            e = e + 1.0f;
            RCP_(0, 16);
            e = e * xc;
            PACK_();
            Y[t] = MFMA32(asbf(pa), asbf(sp[0]), (f32x16)(0.f));
            Y[t] = MFMA32(asbf(pb), asbf(sp[1]), Y[t]);
          }
          const int node = base + (((n32 >> 2) - base) & 7), comp = n32 & 3;
          const float pt = pdot(IC<4>{});
          if (node <= last) {
            float* mp = (float*)(lds + L_MG) + node * 132;
            if (comp == 3) {
#pragma unroll
              for (int t = 0; t < 4; t++)
#pragma unroll
                for (int r = 0; r < 16; r++) atomicAdd(mp + 4 * hh + 32 * t + 8 * (r >> 2) + (r & 3), Y[t][r]);
            } else if (hh == 0) {
              atomicAdd(mp + 128 + comp, pt);
            }
          }
        }
        LGKMWAIT0();
      }
    }
    __syncthreads();
    for (int q = tid; q < 40 * 66; q += 512) {
      const int row = q / 66, c = q - row * 66;
      const f32x2 v = *(const f32x2*)(lds + L_MG + row * 528 + c * 8);
      if (c < 64) *(u32*)(lds + L_IMG + row * 528 + 256 + c * 4) = pk2(v[0], v[1]);
      else *(f32x2*)(lds + L_IMG + row * 528 + c * 8) = v;
    }
    __syncthreads();
    const u32x4 srd_wh = mksrd(wh, 65536);
    SETM0(m0a); DMA1(0, vsrc, srd_wh, 0); DMA1(1024, vsrc, srd_wh, 7168); DMA1(2048, vsrc, srd_wh, 14336); DMA1(3072, vsrc, srd_wh, 21504);
    SETM0(m0b); DMA1(0, vsrc, srd_wh, 32768); DMA1(1024, vsrc, srd_wh, 39936); DMA1(2048, vsrc, srd_wh, 47104); DMA1(3072, vsrc, srd_wh, 54272);
    VMWAIT0();
  }

  int tid2 = threadIdx.x;
  asm volatile("" : "+v"(tid2));
  const int c16e = tid2 & 15, gqe = (tid2 >> 4) & 3;
  const char* const xbe = lds + L_IMG + c16e * 528 + 16 * gqe;
  f32x4 acc[3], acm[3];
  const char* const wfp = lds + L_STG + w * 8192 + (8 * gqe + (c16e >> 2)) * 32 + 8 * (c16e & 3);
  VMWAIT(4);
  u32x4 wf2[4];
#pragma unroll
  for (int rt = 0; rt < 3; rt++) acc[rt] = acm[rt] = (f32x4)(0.f);
  {
    u32x4 wf1[4], xf[4][3];
#pragma unroll
    for (int s = 0; s < 4; s++) {
      const u32x2 x0 = trread(wfp + s * 1024), x1 = trread(wfp + s * 1024 + 128);
      const u32x2 y0 = trread(wfp + 4096 + s * 1024), y1 = trread(wfp + 4096 + s * 1024 + 128);
      wf1[s] = (u32x4){x0[0], x0[1], x1[0], x1[1]};
      wf2[s] = (u32x4){y0[0], y0[1], y1[0], y1[1]};
#pragma unroll
      for (int rt = 0; rt < 3; rt++) xf[s][rt] = *(const u32x4*)(xbe + rt * 8448 + 64 * s);
    }
    SB();
#pragma unroll
    for (int s = 0; s < 4; s++)
#pragma unroll
      for (int rt = 0; rt < 3; rt++) acc[rt] = MFMA16(asbf(wf1[s]), asbf(xf[s][rt]), acc[rt]);
  }
  u32x2 hr[3];
  u32 hm[3];
  const char* xp[3];
#pragma unroll
  for (int rt = 0; rt < 3; rt++) {
    hr[rt] = *(const u32x2*)(lds + L_IMG + c16e * 528 + 8 * gqe + rt * 8448 + 32 * w);
    hm[rt] = 16 * rt + c16e < 40 ? ((const u32*)(lds + L_HM))[16 * rt + c16e] : 0u;
    xp[rt] = lds + L_IMG + 256 + 16 * gqe + (39 + __builtin_ctz(hm[rt] | 256u)) * 528;
    hm[rt] &= hm[rt] - 1;
  }
  STAMP(1, 12);
  __syncthreads();
  STAMP(1, 13);
  {
    u32x4 xf[2][3][4];
#pragma unroll
    for (int s = 0; s < 4; s++)
#pragma unroll
      for (int rt = 0; rt < 3; rt++) {
        xf[0][rt][s] = *(const u32x4*)(xbe + 256 + rt * 8448 + 64 * s);
        xf[1][rt][s] = *(const u32x4*)(xp[rt] + 64 * s);
      }
    SB();
#pragma unroll
    for (int s = 0; s < 4; s++)
#pragma unroll
      for (int j = 0; j < 2; j++)
#pragma unroll
        for (int rt = 0; rt < 3; rt++) acm[rt] = MFMA16(asbf(wf2[s]), asbf(xf[j][rt][s]), acm[rt]);
  }
  while (__builtin_amdgcn_ballot_w64((hm[0] | hm[1] | hm[2]) != 0) != 0) {
#pragma unroll
    for (int rt = 0; rt < 3; rt++) {
      const char* p = lds + L_IMG + 256 + 16 * gqe + (39 + __builtin_ctz(hm[rt] | 256u)) * 528;
      hm[rt] &= hm[rt] - 1;
#pragma unroll
      for (int s = 0; s < 4; s++) acm[rt] = MFMA16(asbf(wf2[s]), asbf(*(const u32x4*)(p + 64 * s)), acm[rt]);
    }
  }
  STAMP(1, 14);
  {
    const rsrc_t rs_o = mkrs(out + (size_t)n0 * 128, nn * 256);
#pragma unroll
    for (int rt = 0; rt < 3; rt++) {
      u32 o[2];
#pragma unroll
      for (int r = 0; r < 2; r++) {
        const f32x2 z = (f32x2){acc[rt][2 * r], acc[rt][2 * r + 1]} + (f32x2){acm[rt][2 * r], acm[rt][2 * r + 1]} * NLN2;
        const f32x2 t = z * NLOG2E;
        const f32x2 d = (f32x2){__builtin_amdgcn_exp2f(t[0]), __builtin_amdgcn_exp2f(t[1])} + 1.0f;
        const f32x2 v = z * (f32x2){__builtin_amdgcn_rcpf(d[0]), __builtin_amdgcn_rcpf(d[1])} + (f32x2){bf2f(hr[rt][r]), bfhi(hr[rt][r])};
        o[r] = pk2(v[0], v[1]);
      }
      __builtin_amdgcn_raw_buffer_store_b64((u32x2){o[0], o[1]}, rs_o, c16e * 256 + 8 * gqe, rt * 4096 + 32 * w, 0);
    }
  }
  if (w < 2) {
    const int nl = __umul24(tid2, 21846) >> 16, k = tid2 - 3 * nl;
    if (tid2 < 3 * nn) {
      const char* mp = lds + L_IMG + 512 + 4 * k;
      u32 hq = ((const u32*)(lds + L_HM))[nl];
      float pv = *(const float*)(mp + nl * 528) + bf2f(*(const u16*)(lds + L_NODE + nl * 2 + tid2 * 2));
      while (hq) {
        pv += *(const float*)(mp + (39 + __builtin_ctz(hq)) * 528);
        hq &= hq - 1;
      }
      out[(size_t)NN * 128 + n0 * 3 + tid2] = (u16)pk2(pv, 0.f);
    }
  }
  VMWAIT0();
  STAMP(1, 15); RSTAMP(1, 31);
}

extern "C" void kernel_launch(void* const* d_in, const int* in_sizes, int n_in, void* d_out, int out_size,
                              void* d_ws, size_t ws_size, hipStream_t stream) {
  const u16* h = (const u16*)d_in[0];
  const u16* pos = (const u16*)d_in[1];
  const u16* ea = (const u16*)d_in[2];
  const int* src = (const int*)d_in[3];
  const int* dst = (const int*)d_in[4];
  const u16* we = (const u16*)d_in[5];
  const u16* wx = (const u16*)d_in[6];
  const u16* wh = (const u16*)d_in[7];
  egnn_k1<<<NB, 512, 0, stream>>>(h, ea, src, dst, we, (char*)d_ws);
  egnn_k2<<<NB, 512, 0, stream>>>(h, pos, we, wx, wh, (const char*)d_ws, (u16*)d_out);
}
```

```cpp
#include <hip/hip_runtime.h>
#include <hip/hip_bf16.h>

typedef unsigned short u16;
typedef unsigned int u32;
typedef float f32x2 __attribute__((ext_vector_type(2)));
typedef float f32x4 __attribute__((ext_vector_type(4)));
typedef float f32x16 __attribute__((ext_vector_type(16)));
typedef __bf16 bf16x8 __attribute__((ext_vector_type(8)));
typedef __bf16 bf16x2 __attribute__((ext_vector_type(2)));
typedef u32 u32x2 __attribute__((ext_vector_type(2)));
typedef u32 u32x3 __attribute__((ext_vector_type(3)));
typedef u32 u32x4 __attribute__((ext_vector_type(4)));
typedef int i32x4 __attribute__((ext_vector_type(4)));
typedef short s16x4 __attribute__((ext_vector_type(4)));
typedef u16 u16x2 __attribute__((ext_vector_type(2)));
#define LAS __attribute__((address_space(3)))
typedef __amdgpu_buffer_rsrc_t rsrc_t;

#define PFWG 64
constexpr int NN = 10000;
constexpr int NE = 320000;
constexpr int NCH = 250;
constexpr int CH = 1280;
constexpr int NB = 256;
constexpr int CAP = 1408;
constexpr float NLOG2E = -1.4426950408889634f;
constexpr float NLN2 = -0.6931471805599453f;
constexpr u32 OH = 0x3F81u;
constexpr u32 OHINV = 0x81u;

constexpr size_t WS_T = 0;
constexpr size_t WS_R = WS_T + (size_t)NB * 256 * 4;
constexpr size_t WS_B = WS_R + (size_t)NE * 12;

__device__ __forceinline__ float bf2f(u32 v) { return __uint_as_float(v << 16); }
__device__ __forceinline__ float bfhi(u32 v) { return __uint_as_float(v & 0xffff0000u); }
__device__ __forceinline__ u32 pk2(float a, float b) {
  f32x2 v = {a, b};
  return __builtin_bit_cast(u32, __builtin_convertvector(v, bf16x2));
}
__device__ __forceinline__ u32 mul16x2(u32 a, u32 b) {
  return __builtin_bit_cast(u32, __builtin_bit_cast(u16x2, a) * __builtin_bit_cast(u16x2, b));
}
__device__ __forceinline__ int wave_incl_scan(int x) {
  x += __builtin_amdgcn_update_dpp(0, x, 0x111, 0xf, 0xf, false);
  x += __builtin_amdgcn_update_dpp(0, x, 0x112, 0xf, 0xf, false);
  x += __builtin_amdgcn_update_dpp(0, x, 0x114, 0xf, 0xf, false);
  x += __builtin_amdgcn_update_dpp(0, x, 0x118, 0xf, 0xf, false);
  x += __builtin_amdgcn_update_dpp(0, x, 0x142, 0xa, 0xf, false);
  x += __builtin_amdgcn_update_dpp(0, x, 0x143, 0xc, 0xf, false);
  return x;
}
__device__ __forceinline__ bf16x8 asbf(u32x4 v) { return __builtin_bit_cast(bf16x8, v); }
__device__ __forceinline__ u32x2 trread(const char* p) {
  return __builtin_bit_cast(u32x2, __builtin_amdgcn_ds_read_tr16_b64_v4i16((LAS s16x4*)p));
}
__device__ __forceinline__ rsrc_t mkrs(const void* p, u32 bytes) { return __builtin_amdgcn_make_buffer_rsrc((void*)p, 0, bytes, 0x00020000); }
__device__ __forceinline__ void dma16(rsrc_t rs, char* ldsp, u32 voff, u32 soff) {
  __builtin_amdgcn_raw_ptr_buffer_load_lds(rs, (LAS void*)ldsp, 16, voff, soff, 0, 0);
}
#define MFMA32(a, b, c) __builtin_amdgcn_mfma_f32_32x32x16_bf16(a, b, c, 0, 0, 0)
#define MFMA16(a, b, c) __builtin_amdgcn_mfma_f32_16x16x32_bf16(a, b, c, 0, 0, 0)
#define SB() __builtin_amdgcn_sched_barrier(0)
#define VMWAIT0() asm volatile("s_waitcnt vmcnt(0)" ::: "memory")
#define STAMPDECL
#define STAMP(buf, i)
#define STAMPV(buf, i)
#define RSTAMP(buf, i)
#define DRAIN()

template <int V> struct IC { static constexpr int value = V; };
__device__ __forceinline__ u32x4 mksrd(const void* p, u32 bytes) {
  const unsigned long long a = (unsigned long long)p;
  return (u32x4){(u32)a, (u32)(a >> 32) & 0xffffu, bytes, 0x00020000u};
}
#define DMA1(J, A, SRD, SOFF) asm volatile("buffer_load_dwordx4 %0, %1, %2 offen offset:" #J " lds" :: "v"(A), "s"(SRD), "s"(SOFF) : "memory")
#define LDB(T, DST, VO, SRD, SO) asm volatile("buffer_load_" T " %0, %1, %2, %3 offen" : "=v"(DST) : "v"(VO), "s"(SRD), "s"(SO) : "memory")
#define SETM0(V) asm volatile("s_mov_b32 m0, %0\n\ts_nop 0" :: "s"(V) : "memory")
#define VMWAIT(N) asm volatile("s_waitcnt vmcnt(" #N ")" ::: "memory")
#define LGKMWAIT0() asm volatile("s_waitcnt lgkmcnt(0)" ::: "memory")
#define PIN(x) do { asm volatile("" : "+v"(x)); SB(); } while (0)

constexpr int K1_H = 32768;
constexpr int K1_IMG = K1_H + 48 * 256;
constexpr int K1_HIST = K1_IMG + 16384;
constexpr int K1_LDS = K1_HIST + 2 * NB * 4 + 32;

extern "C" __global__ __launch_bounds__(512) void egnn_k1(const u16* __restrict__ h, const u16* __restrict__ ea,
                                               const int* __restrict__ src, const int* __restrict__ dst,
                                               const u16* __restrict__ we, char* __restrict__ ws) {
  __shared__ __attribute__((aligned(128))) char lds[K1_LDS];
  const int tid = threadIdx.x, lane = tid & 63;
  const int w = __builtin_amdgcn_readfirstlane(tid >> 6);
  const int c = (blockIdx.x & 7) * 32 + (blockIdx.x >> 3);
  const int gq = lane >> 4, c16 = lane & 15;
  u32* const hist = (u32*)(lds + K1_HIST);
  asm volatile("" :: "s"(h), "s"(ea), "s"(src), "s"(dst), "s"(we), "s"(ws));
  STAMPDECL; RSTAMP(0, 30); STAMP(0, 0);
  if (tid < NB) hist[tid] = 0;
  else if (tid < NB + 8) hist[NB + tid] = 0;
  __syncthreads();
  auto meet = [&](int k) {
    LGKMWAIT0();
    if (lane == 0) __hip_atomic_fetch_add(&hist[2 * NB + k], 1u, __ATOMIC_RELAXED, __HIP_MEMORY_SCOPE_WORKGROUP);
    while (__hip_atomic_load(&hist[2 * NB + k], __ATOMIC_RELAXED, __HIP_MEMORY_SCOPE_WORKGROUP) < 4u) __builtin_amdgcn_s_sleep(1);
    asm volatile("" ::: "memory");
  };
  if (w < 4) {
    const u32 eo = tid * 20, nb = c < NCH ? CH * 4 : 0;
    const rsrc_t rs_d = mkrs(dst + c * CH, nb), rs_s = mkrs(src + c * CH, nb), rs_a = mkrs(ea + (size_t)c * CH * 4, 2 * nb);
    const u32x4 d4 = __builtin_amdgcn_raw_buffer_load_b128(rs_d, eo, 0, 0);
    const u32 d1 = __builtin_amdgcn_raw_buffer_load_b32(rs_d, eo + 16, 0, 0);
    const u32x4 s4 = __builtin_amdgcn_raw_buffer_load_b128(rs_s, eo, 0, 0);
    const u32 s1 = __builtin_amdgcn_raw_buffer_load_b32(rs_s, eo + 16, 0, 0);
    const u32x4 a0 = __builtin_amdgcn_raw_buffer_load_b128(rs_a, 2 * eo, 0, 0);
    const u32x4 a1 = __builtin_amdgcn_raw_buffer_load_b128(rs_a, 2 * eo + 16, 0, 0);
    const u32x2 a2 = __builtin_amdgcn_raw_buffer_load_b64(rs_a, 2 * eo + 32, 0, 0);
    __builtin_amdgcn_s_setprio(1);
    STAMP(0, 1);
    if (c < NCH) {
      u32* const img = (u32*)(lds + K1_IMG);
      u32* const base = hist + NB;
      u32 bk[5], rk[5];
#pragma unroll
      for (int i = 0; i < 5; i++) {
        bk[i] = __umul24(i < 4 ? d4[i & 3] : d1, 214749u) >> 23;
        rk[i] = atomicAdd(&hist[bk[i]], 1u);
      }
      STAMP(0, 2);
      meet(1);
      STAMP(0, 3);
      {
        const u32x4 v = *(const u32x4*)(hist + 4 * lane);
        const int s = v[0] + v[1] + v[2] + v[3];
        const int ex = wave_incl_scan(s) - s;
        const u32x4 bs = {(u32)ex, ex + v[0], ex + v[0] + v[1], ex + v[0] + v[1] + v[2]};
        *(u32x4*)(base + 4 * lane) = bs;
        const u32 bb = w == 0 ? bs[0] : w == 1 ? bs[1] : w == 2 ? bs[2] : bs[3];
        const u32 vv = w == 0 ? v[0] : w == 1 ? v[1] : w == 2 ? v[2] : v[3];
        ((u32*)(ws + WS_T))[(4 * lane + w) * 256 + c] = bb | (vv << 16);
      }
#pragma unroll
      for (int i = 0; i < 5; i++) {
        u32* p = img + 3 * (base[bk[i]] + rk[i]);
        p[0] = i < 2 ? a0[2 * (i & 1)] : i < 4 ? a1[2 * (i & 1)] : a2[0];
        p[1] = i < 2 ? a0[2 * (i & 1) + 1] : i < 4 ? a1[2 * (i & 1) + 1] : a2[1];
        p[2] = (i < 4 ? s4[i & 3] : s1) | ((i < 4 ? d4[i & 3] : d1) << 16);
      }
      STAMP(0, 4);
      meet(2);
      STAMP(0, 5);
      const rsrc_t rs_r = mkrs(ws + WS_R + (size_t)c * (CH * 12), CH * 12);
#pragma unroll
      for (int i = 0; i < 4; i++)
        __builtin_amdgcn_raw_buffer_store_b128(((const u32x4*)img)[tid + 256 * i], rs_r, tid * 16 + i * 4096, 0, 0);
    }
    STAMP(0, 6); DRAIN(); STAMP(0, 7); RSTAMP(0, 31);
  } else {
    const int u = w - 4, n0 = (c * 625 + 15) >> 4, nn = (((c + 1) * 625 + 15) >> 4) - n0;
    const u32 ldsb = (u32)(size_t)(LAS char*)lds;
    {
      const u32x4 srd_h = mksrd(h + (size_t)n0 * 128, nn * 256), srd_w = mksrd(we + 128 * 128, 32768);
      const u32 vw = gq * 256 + 16 * (c16 ^ (4 * gq));
      SETM0(ldsb + u * 8192);
      DMA1(0, vw, srd_w, u * 8192); DMA1(1024, vw, srd_w, u * 8192); DMA1(2048, vw, srd_w, u * 8192); DMA1(3072, vw, srd_w, u * 8192);
      SETM0(ldsb + u * 8192 + 4096);
      DMA1(0, vw, srd_w, u * 8192 + 4096); DMA1(1024, vw, srd_w, u * 8192 + 4096); DMA1(2048, vw, srd_w, u * 8192 + 4096); DMA1(3072, vw, srd_w, u * 8192 + 4096);
      SETM0(ldsb + K1_H + u * 3072);
      DMA1(0, gq * 256 + 16 * (c16 ^ ((12 * u + gq) & 15)), srd_h, u * 3072);
      DMA1(1024, gq * 256 + 16 * (c16 ^ ((12 * u + 4 + gq) & 15)), srd_h, u * 3072);
      DMA1(2048, gq * 256 + 16 * (c16 ^ ((12 * u + 8 + gq) & 15)), srd_h, u * 3072);
    }
    STAMP(0, 1);
    VMWAIT0();
    STAMP(0, 2);
    u32 pfv = 0;
    const bool pf = blockIdx.x < PFWG && u < 3;
    if (pf) {
      unsigned long long pc;
      u32 off;
      asm volatile("s_getpc_b64 %0\n.Lpc%=:\n\ts_mov_b32 %1, egnn_k2-.Lpc%=" : "=s"(pc), "=s"(off));
      asm volatile("global_load_dword %0, %1, %2" : "=v"(pfv) : "v"(min(u * 64 + lane, 147) * 128), "s"(pc + off) : "memory");
    }
    meet(0);
    STAMP(0, 3);
    f32x4 acc[3][2];
#pragma unroll
    for (int rt = 0; rt < 3; rt++) acc[rt][0] = acc[rt][1] = (f32x4)(0.f);
    const char* wb = lds + (8 * gq + (c16 >> 2)) * 256 + 16 * ((4 * u + (c16 & 3)) ^ (c16 & 12));
    const int hb = K1_H + c16 * 256 + 16 * (gq ^ c16);
    u32x4 fa[4], fb[4], hf[4][3];
#pragma unroll
    for (int s = 0; s < 4; s++) {
      const u32x2 x0 = trread(wb + (32 * s) * 256), x1 = trread(wb + (32 * s + 4) * 256);
      const u32x2 y0 = trread(wb + (32 * s) * 256 + 8), y1 = trread(wb + (32 * s + 4) * 256 + 8);
      fa[s] = (u32x4){x0[0], x0[1], x1[0], x1[1]};
      fb[s] = (u32x4){y0[0], y0[1], y1[0], y1[1]};
#pragma unroll
      for (int rt = 0; rt < 3; rt++) hf[s][rt] = *(const u32x4*)(lds + (hb ^ (64 * s)) + rt * 4096);
    }
    SB();
#pragma unroll
    for (int s = 0; s < 4; s++)
#pragma unroll
      for (int rt = 0; rt < 3; rt++) {
        acc[rt][0] = MFMA16(asbf(fa[s]), asbf(hf[s][rt]), acc[rt][0]);
        acc[rt][1] = MFMA16(asbf(fb[s]), asbf(hf[s][rt]), acc[rt][1]);
      }
    const rsrc_t rs_b = mkrs(ws + WS_B + (size_t)n0 * 256, nn * 256);
#pragma unroll
    for (int rt = 0; rt < 3; rt++) {
      const f32x4 a = acc[rt][0] * NLOG2E, b = acc[rt][1] * NLOG2E;
      const u32x4 o = {pk2(a[0], a[1]), pk2(a[2], a[3]), pk2(b[0], b[1]), pk2(b[2], b[3])};
      __builtin_amdgcn_raw_buffer_store_b128(o, rs_b, c16 * 256 + 16 * gq + 64 * u + rt * 4096, 0, 0);
    }
    if (pf) asm volatile("s_waitcnt vmcnt(0)" :: "v"(pfv) : "memory");
    STAMP(0, 6); DRAIN(); STAMP(0, 7); RSTAMP(0, 31);
  }
}

constexpr int OHS = 2 * CAP + 16;
constexpr int L_NODE = 0;
constexpr int L_WX = L_NODE + 320;
constexpr int L_WS = L_WX + 512;
constexpr int L_REC = L_WS + 16;
constexpr int L_CV = L_REC + 16 * (CAP + 1);
constexpr int L_SRC = L_CV + 8 * (CAP + 2);
constexpr int L_OH = L_SRC + 2 * (CAP + 8);
constexpr int L_CNT = L_OH + 8 * OHS;
constexpr int L_HM = L_CNT + 256;
constexpr int L_IMG = L_HM + 160;
constexpr int L_AT = L_IMG + 48 * 528;
constexpr int L_STG = (L_AT + 128 * 96 + 127) & ~127;
constexpr int K2_LDS = L_STG + 65536;
constexpr int L_MG = L_STG + 32768;
static_assert(K2_LDS <= 163840, "LDS");
static_assert(L_CV % 16 == 0 && L_OH % 16 == 0 && L_SRC % 16 == 0 && L_IMG % 16 == 0 && L_AT % 16 == 0, "align");
static_assert(L_IMG - L_OH > 2 * 8192 && L_IMG - L_OH <= 3 * 8192, "zero sweep");

extern "C" __global__ __launch_bounds__(512, 2) void egnn_k2(const u16* __restrict__ h, const u16* __restrict__ pos,
                                                  const u16* __restrict__ we, const u16* __restrict__ wx,
                                                  const u16* __restrict__ wh, const char* __restrict__ ws,
                                                  u16* __restrict__ out) {
  __shared__ __attribute__((aligned(128))) char lds[K2_LDS];
  int tid = threadIdx.x, lane = tid & 63;
  const int w = __builtin_amdgcn_readfirstlane(tid >> 6);
  const int b = (blockIdx.x & 7) * 32 + (blockIdx.x >> 3);
  const int n0 = (b * 625 + 15) >> 4, nn = (((b + 1) * 625 + 15) >> 4) - n0;
  int gq = lane >> 4, c16 = lane & 15, hh = lane >> 5, n32 = lane & 31;
  const u32 ldsb = (u32)(size_t)(LAS char*)lds;
  const u32x4 srd_g = mksrd(ws + WS_B - 4096, 4096 + NN * 256), srd_pos = mksrd(pos, NN * 6);

  asm volatile("" :: "s"(h), "s"(pos), "s"(we), "s"(wx), "s"(wh), "s"(ws), "s"(out));
  STAMPDECL; RSTAMP(1, 30); STAMP(1, 0);
  u32 cwv[6];
  u32x4 hv[2], t4;
  if (w < 4) LDB("dwordx4", t4, lane * 16, mksrd(ws + WS_T + b * 1024, 1024), 0);
  {
    const u32x4 srd_h = mksrd(h + (size_t)n0 * 128, nn * 256);
    LDB("dwordx4", hv[0], tid * 16, srd_h, 0);
    LDB("dwordx4", hv[1], tid * 16, srd_h, 8192);
    const u32x4 srd_we = mksrd(we, 261 * 256);
    const u32 vw = gq * 256 + 16 * (c16 ^ (4 * gq));
    SETM0(ldsb + L_STG + w * 4096);
    DMA1(0, vw, srd_we, w * 4096); DMA1(1024, vw, srd_we, w * 4096); DMA1(2048, vw, srd_we, w * 4096); DMA1(3072, vw, srd_we, w * 4096);
    if (w >= 6) {
#pragma unroll
      for (int k = 0; k < 5; k++) LDB("ushort", cwv[k], tid * 2, srd_we, (256 + k) * 256 - 768);
      LDB("ushort", cwv[5], tid * 2 - 768, mksrd(wx, 256), 0);
    } else {
      LDB("ushort", cwv[0], tid * 2 - 512, srd_pos, n0 * 6);
    }
  }
  SB();
#pragma unroll
  for (int i = 0; i < 2; i++) *(u32x4*)(lds + L_OH + tid * 16 + i * 8192) = (u32x4)(0u);
  if (tid < (L_IMG - L_OH - 2 * 8192) / 16) *(u32x4*)(lds + L_OH + tid * 16 + 2 * 8192) = (u32x4)(0u);
  {
    char* ip = lds + L_IMG + (tid >> 4) * 528 + 256 + c16 * 16;
    *(u32x4*)ip = (u32x4)(0u);
    if (w < 4) *(u32x4*)(ip + 32 * 528) = (u32x4)(0u);
    else if (tid < 304) *(u32x4*)(lds + L_IMG + (tid - 256) * 528 + 512) = (u32x4)(0u);
  }
  u32x4 id0, id1, one4 = {hh ? 0u : 0x3F80u, 0u, 0u, 0u};
  int ga[4], recoff, ohoff, cvoff, axoff, srcoff;
  u32 go[4], vsrc;
  auto lane_consts = [&]() {
    const int k0 = n32 - 8 * hh;
    const u32 one = 0x3F80u << ((k0 & 1) * 16);
#pragma unroll
    for (int i = 0; i < 4; i++) {
      id0[i] = (k0 >> 1) == i ? one : 0u;
      id1[i] = (k0 >> 1) == 8 + i ? one : 0u;
    }
    const int prow = (n32 & 19) | ((n32 & 4) << 1) | ((n32 & 8) >> 1);
    const int sa = prow & 3, sb = prow >> 2;
#pragma unroll
    for (int s = 0; s < 4; s++) ga[s] = (L_STG + w * 8192 + (8 * sa + sb) * 128 + 16 * (hh ^ (2 * sa + ((sb >> 1) & 1)))) ^ (32 * s);
#pragma unroll
    for (int j = 0; j < 4; j++) go[j] = 4096 - 1024 * j + 16 * ((lane & 7) ^ (2 * j + ((lane >> 4) & 1)));
    recoff = L_REC + prow * 16;
    ohoff = L_OH + (n32 >> 2) * OHS + 16 * hh;
    cvoff = L_CV + (8 * hh + (c16 >> 2)) * 8;
    axoff = L_OH + (c16 >> 2) * OHS + 32 * (gq & 1) + 16 * (c16 & 1) + 8 * ((c16 >> 1) & 1);
    srcoff = L_SRC + (lane >> 3) * 8;
    vsrc = (lane >> 1) * 256 + 32 * w + 16 * (lane & 1);
  };
  lane_consts();
  const u32 m0a = ldsb + L_STG + w * 8192, m0b = m0a + 4096;
  asm volatile("" : "+v"(one4), "+v"(id0), "+v"(id1), "+v"(ga[0]), "+v"(ga[1]), "+v"(ga[2]), "+v"(ga[3]), "+v"(go[0]), "+v"(go[1]), "+v"(go[2]), "+v"(go[3]));
  asm volatile("" : "+v"(recoff), "+v"(ohoff), "+v"(cvoff), "+v"(axoff), "+v"(srcoff), "+v"(vsrc));
  SB();
  int mtot, scnt, sexc;
  u32 sbase;
  auto decode = [&]() {
    if (lane == 62) t4[2] = t4[3] = 0;
    if (lane == 63) t4 = (u32x4)(0u);
    const int c0 = t4[0] >> 16, c1 = c0 + (t4[1] >> 16), c2 = c1 + (t4[2] >> 16), c3 = c2 + (t4[3] >> 16);
    const int inc = wave_incl_scan(c3);
    mtot = __builtin_amdgcn_readlane(inc, 63);
    const int wq = w & 3;
    const u32 tv = wq == 0 ? t4[0] : wq == 1 ? t4[1] : wq == 2 ? t4[2] : t4[3];
    scnt = tv >> 16;
    sexc = inc - c3 + (wq == 0 ? 0 : wq == 1 ? c0 : wq == 2 ? c1 : c2);
    sbase = ((4 * lane + wq) * CH + (tv & 0xffffu)) * 12;
  };
  if (w < 4) {
    asm volatile("s_waitcnt vmcnt(7)" ::: "memory"); SB();
    STAMP(1, 1);
    decode();
    if (tid == 0) *(int*)(lds + L_WS) = mtot;
    if (mtot <= CAP) {
      u32* p = (u32*)(lds + L_CV) + sexc;
      u32 v = sbase;
#pragma clang loop vectorize(disable) unroll(disable)
      for (int i = 0; i < scnt; i++, p++, v += 12) *p = v;
    }
  }
  asm volatile("s_waitcnt vmcnt(0)" ::: "memory"); SB();
  asm volatile("" : "+v"(cwv[0]), "+v"(cwv[1]), "+v"(cwv[2]), "+v"(cwv[3]), "+v"(cwv[4]), "+v"(cwv[5]));
  {
    char* hp = lds + L_IMG + (tid >> 4) * 528 + c16 * 16;
    *(u32x4*)hp = hv[0];
    if (w < 4) *(u32x4*)(hp + 32 * 528) = hv[1];
  }
  if (w == 4 || w == 5) {
    const int t3 = tid - 256, nl = __umul24(t3, 21846) >> 16;
    if (t3 < 120) *(u16*)(lds + L_NODE + nl * 2 + t3 * 2) = (u16)cwv[0];
  }
  STAMP(1, 3);
  __syncthreads();
  STAMP(1, 4);
  u32x3 rec[3];
  bool general;
  {
    const u32x4 srd_rec = mksrd(ws + WS_R, NE * 12);
    u32 ro[3];
#pragma unroll
    for (int k = 0; k < 3; k++) ro[k] = ((const u32*)(lds + L_CV))[tid + 512 * k];
    mtot = __builtin_amdgcn_readfirstlane(*(const int*)(lds + L_WS));
    general = mtot > CAP;
    const int lim = general ? 0 : mtot;
#pragma unroll
    for (int k = 0; k < 3; k++) LDB("dwordx3", rec[k], tid + 512 * k < lim ? ro[k] : 0x7fffff00u, srd_rec, 0);
  }
  if (general) asm volatile("s_waitcnt vmcnt(0)" :: "v"(rec[0]), "v"(rec[1]), "v"(rec[2]) : "memory");
  SB();
  if (w >= 6) {
    const float c0 = bf2f(cwv[0]) * NLOG2E;
    const u32 d_hi = pk2(c0, 0.f);
    const u32x4 o = {pk2(bf2f(cwv[1]) * NLOG2E, bf2f(cwv[2]) * NLOG2E), pk2(bf2f(cwv[3]) * NLOG2E, bf2f(cwv[4]) * NLOG2E),
                     pk2(c0, c0 - bf2f(d_hi)), 0u};
    *(u32x4*)(lds + L_AT + (tid - 384) * 96 + 80) = o;
    ((float*)(lds + L_WX))[tid - 384] = bf2f(cwv[5]) * NLN2;
  }
  const char* const xb = lds + L_IMG + c16 * 528 + 16 * gq;
  if (w < 4) {
    f32x4 acc[3][2];
#pragma unroll
    for (int rt = 0; rt < 3; rt++) acc[rt][0] = acc[rt][1] = (f32x4)(0.f);
    const char* wb = lds + L_STG + (8 * gq + (c16 >> 2)) * 256 + 16 * ((4 * w + (c16 & 3)) ^ (c16 & 12));
    u32x4 fa[4], fb[4], hf[4][3];
#pragma unroll
    for (int s = 0; s < 4; s++) {
      const u32x2 x0 = trread(wb + (32 * s) * 256), x1 = trread(wb + (32 * s + 4) * 256);
      const u32x2 y0 = trread(wb + (32 * s) * 256 + 8), y1 = trread(wb + (32 * s + 4) * 256 + 8);
      fa[s] = (u32x4){x0[0], x0[1], x1[0], x1[1]};
      fb[s] = (u32x4){y0[0], y0[1], y1[0], y1[1]};
#pragma unroll
      for (int rt = 0; rt < 3; rt++) hf[s][rt] = *(const u32x4*)(xb + rt * 8448 + 64 * s);
    }
    SB();
#pragma unroll
    for (int s = 0; s < 4; s++)
#pragma unroll
      for (int rt = 0; rt < 3; rt++) {
        acc[rt][0] = MFMA16(asbf(hf[s][rt]), asbf(fa[s]), acc[rt][0]);
        acc[rt][1] = MFMA16(asbf(hf[s][rt]), asbf(fb[s]), acc[rt][1]);
      }
    char* ap = lds + L_AT + (32 * w + 8 * (c16 >> 2) + (c16 & 3)) * 96 + 8 * gq;
#pragma unroll
    for (int rt = 0; rt < 3; rt++)
      if (16 * rt + 4 * gq < 40)
#pragma unroll
        for (int j = 0; j < 2; j++) {
          const f32x4 a = acc[rt][j] * (NLOG2E / 1.0078125f);
          *(u32x2*)(ap + j * 384 + rt * 32) = (u32x2){pk2(a[0], a[1]), pk2(a[2], a[3])};
        }
  }
  f32x16 Y[4], XA, XB, e;
  u32x4 sp0 = (u32x4)(0u), sp1 = (u32x4)(0u), pa = (u32x4)(0u), pb = (u32x4)(0u);
  asm volatile("" : "+v"(sp0), "+v"(sp1), "+v"(pa), "+v"(pb));
  Y[0] = MFMA32(asbf(pa), asbf(pa), (f32x16)(0.f));
  Y[1] = MFMA32(asbf(pb), asbf(pb), (f32x16)(0.f));
  Y[2] = MFMA32(asbf(sp0), asbf(sp0), (f32x16)(0.f));
  Y[3] = MFMA32(asbf(sp1), asbf(sp1), (f32x16)(0.f));
  XB = MFMA32(asbf(pa), asbf(pb), (f32x16)(0.f));
  asm volatile("" : "+v"(Y[0]), "+v"(Y[1]), "+v"(Y[2]), "+v"(Y[3]), "+v"(XB));
  SB();
  STAMP(1, 28);
  u32x4 wex[4];
  u32 ad[4];
  int ninc;
  u32x4 srd = srd_g;
  u32 sfb = 128;
  auto prep = [&](u32x2 sv, bool real) {
    if (real) {
      asm("v_mad_u32_u16 %0, %1, %2, %3" : "=v"(ad[0]) : "v"(sv[0]), "s"(256), "v"(go[0]));
      asm("v_mad_u32_u16 %0, %1, %2, %3 op_sel:[1,0,0,0]" : "=v"(ad[1]) : "v"(sv[0]), "s"(256), "v"(go[1]));
      asm("v_mad_u32_u16 %0, %1, %2, %3" : "=v"(ad[2]) : "v"(sv[1]), "s"(256), "v"(go[2]));
      asm("v_mad_u32_u16 %0, %1, %2, %3 op_sel:[1,0,0,0]" : "=v"(ad[3]) : "v"(sv[1]), "s"(256), "v"(go[3]));
    } else {
#pragma unroll
      for (int j = 0; j < 4; j++) ad[j] = vsrc + 7168 * j;
      srd = mksrd(wh, 65536);
      sfb = 32768;
    }
  };
  auto issue_a = [&]() { SETM0(m0a); DMA1(0, ad[0], srd, 0); DMA1(1024, ad[1], srd, 0); DMA1(2048, ad[2], srd, 0); DMA1(3072, ad[3], srd, 0); };
  auto issue_b = [&]() { SETM0(m0b); DMA1(0, ad[0], srd, sfb); DMA1(1024, ad[1], srd, sfb); DMA1(2048, ad[2], srd, sfb); DMA1(3072, ad[3], srd, sfb); };
  auto node_at = [&](int x) { return (int)__builtin_popcountll(__builtin_amdgcn_ballot_w64(ninc <= x)); };
  auto loadw = [&](int base) {
    const int g0 = base >> 3, bm = base & 7;
    const char* pa = lds + L_AT + n32 * 96 + (hh ? g0 * 16 : 80);
    const char* pb = lds + L_AT + n32 * 96 + (hh ? g0 * 16 + 16 : 80);
    const unsigned long long lo = ~0ull << (16 * min(bm, 3)) << (bm > 3 ? 16 : 0), hi = bm > 4 ? ~0ull << (16 * (bm - 4)) : ~0ull;
    const u32 mk[4] = {(u32)lo, (u32)(lo >> 32), (u32)hi, (u32)(hi >> 32)};
#pragma unroll
    for (int t = 0; t < 4; t++) {
      const u32x4 a = *(const u32x4*)(pa + t * 3072), bb = *(const u32x4*)(pb + t * 3072);
#pragma unroll
      for (int i = 0; i < 4; i++) wex[t][i] = (a[i] & mk[i]) | (bb[i] & ~mk[i]);
    }
  };
  auto pdot = [&](auto nbc) {
    constexpr int nb = decltype(nbc)::value;
    f32x4 p4[2] = {(f32x4)(0.f), (f32x4)(0.f)};
#pragma unroll
    for (int q0 = 0; q0 < 16; q0 += nb) {
      f32x4 wv[16];
#pragma unroll
      for (int q = q0; q < q0 + nb; q++) wv[q] = *(const f32x4*)(lds + L_WX + (8 * q + 4 * hh) * 4);
      SB();
#pragma unroll
      for (int q = q0; q < q0 + nb; q++)
        p4[q & 1] += (f32x4){Y[q >> 2][4 * (q & 3)], Y[q >> 2][4 * (q & 3) + 1], Y[q >> 2][4 * (q & 3) + 2], Y[q >> 2][4 * (q & 3) + 3]} * wv[q];
      SB();
    }
    p4[0] += p4[1];
    const f32x2 pa = (f32x2){p4[0][0], p4[0][1]} + (f32x2){p4[0][2], p4[0][3]};
    const u32 ph = __float_as_uint(pa[0] + pa[1]);
    const auto sw = __builtin_amdgcn_permlane32_swap(ph, ph, false, false);
    return __uint_as_float(sw[0]) + __uint_as_float(sw[1]);
  };
  auto place = [&](u32x3 rec, u32x2 ps, u32x2 pd, u32 dl, u32 p) {
    const u32 sh = (rec[2] & 1) * 16;
    const u32 sxy = __builtin_amdgcn_alignbit(ps[1], ps[0], sh), sz = ps[1] >> sh;
    const u32 dxy = pk2(bf2f(pd[0]) - bf2f(sxy), bfhi(pd[0]) - bfhi(sxy));
    const u32 dz1 = pk2(bf2f(pd[1]) - bf2f(sz), 1.0f);
    const float fx = bf2f(dxy), fy = bfhi(dxy), fz = bf2f(dz1);
    const float d2 = fx * fx + fy * fy + fz * fz;
    *(u32x4*)(lds + L_REC + p * 16) = (u32x4){rec[0], rec[1], pk2(d2, d2), dl};
    *(u32x2*)(lds + L_CV + p * 8) = (u32x2){mul16x2(dxy, OHINV * 0x10001u), mul16x2(dz1, OHINV * 0x10001u)};
    *(u16*)(lds + L_OH + (dl & 7) * OHS + 2 * p) = (u16)OH;
  };
  auto count_nodes = [&](int m) {
    const int cn = ((const u32*)(lds + L_CNT))[lane];
    ninc = wave_incl_scan(cn);
    if (w == 7) {
      const int p = m + n32 < ((m + 31) & ~31) ? m + n32 : CAP;
      *(u32x4*)(lds + L_REC + p * 16) = (u32x4)(0u);
      ((u16*)(lds + L_SRC))[p] = 0;
    }
    return ninc - cn;
  };
#define RDG(s, half) (*(const u32x4*)(lds + ga[s] + (half) * 4096))
#define EXP_(lo, hi) _Pragma("unroll") for (int r = lo; r < hi; r++) e[r] = __builtin_amdgcn_exp2f(xc[r]); SB()
#define RCP_(lo, hi) _Pragma("unroll") for (int r = lo; r < hi; r++) e[r] = __builtin_amdgcn_rcpf(e[r]); SB()
#define PACK_() _Pragma("unroll") for (int i = 0; i < 4; i++) { pa[i] = pk2(e[2 * i], e[2 * i + 1]); pb[i] = pk2(e[8 + 2 * i], e[8 + 2 * i + 1]); } \
  asm volatile("" : "+v"(pa), "+v"(pb)); SB()
#define SPROD(o, ca, cb) (u32x4){mul16x2(o[0], ca[0]), mul16x2(o[1], ca[1]), mul16x2(o[2], cb[0]), mul16x2(o[3], cb[1])}

  const int m = mtot;
  const bool third = w * 64 + 1024 < m;
  u32 rk[3], dl[3];
  u32x2 ps[3], pd[3];
  if (!general) {
    VMWAIT0(); SB();
    STAMP(1, 27);
#pragma unroll
    for (int k = 0; k < 3; k++)
      if (k < 2 || third) {
        LDB("dwordx2", ps[k], ((rec[k][2] & 0xffffu) * 6) & ~3u, srd_pos, 0);
        dl[k] = tid + 512 * k < m ? (rec[k][2] >> 16) - n0 : 48u + c16;
        rk[k] = atomicAdd((u32*)(lds + L_CNT) + dl[k], 1u);
        pd[k] = *(const u32x2*)(lds + L_NODE + dl[k] * 8);
      }
    STAMP(1, 5);
    __syncthreads();
    STAMP(1, 6);
    const int nst = count_nodes(m);
    const int nprev = __builtin_amdgcn_ds_bpermute(lane * 4 - 28, ninc);
#pragma unroll
    for (int k = 0; k < 3; k++)
      if (k < 2 || third) {
        rk[k] += __builtin_amdgcn_ds_bpermute(dl[k] * 4, nst);
        rk[k] = dl[k] < 48 ? rk[k] : CAP;
        *(u16*)(lds + L_SRC + 2 * rk[k]) = (u16)rec[k][2];
      }
    general = m < 256 || __builtin_amdgcn_ballot_w64(lane >= 7 && lane < nn && ninc - nprev <= 30) != 0;
    if (general) asm volatile("s_waitcnt vmcnt(0)" :: "v"(ps[0]), "v"(ps[1]), "v"(ps[2]) : "memory");
  }
  if (!general) {
    {
      const int nt = (m + 31) >> 5, tq = nt >> 3, trm = nt & 7;
      const int tbeg = w * tq + min(w, trm);
      const int tend = tbeg + tq + (w < trm ? 1 : 0);
      srcoff += tbeg * 64;
      __syncthreads();
      STAMP(1, 29);
      const u32x2 sv0 = *(const u32x2*)(lds + srcoff), sv1 = *(const u32x2*)(lds + srcoff + 64);
      prep(sv0, true);
      if (w < 4) {
        issue_a();
        issue_b();
        VMWAIT(8);
      } else {
        VMWAIT(0);
      }
      SB();
      STAMP(1, 26);
      int base = node_at(tbeg * 32);
      loadw(base);
      const int lnode = node_at(min(tend * 32, m) - 1);
      const int hnode = (tbeg > 0 && node_at(tbeg * 32 - 1) == base) ? base : -1;
      if (hnode >= 0 && lane == 0) __hip_atomic_fetch_or((u32*)(lds + L_HM) + hnode, 1u << w, __ATOMIC_RELAXED, __HIP_MEMORY_SCOPE_WORKGROUP);
      recoff += tbeg * 512; cvoff += tbeg * 256 - 256; ohoff += tbeg * 64 - 64; axoff += tbeg * 64;
#pragma unroll
      for (int k = 0; k < 3; k++)
        if (k < 2 || third) place(rec[k], ps[k], pd[k], dl[k], rk[k]);
      if (w >= 4) {
        issue_a();
        issue_b();
      }
      prep(sv1, tbeg + 1 < tend);
#pragma unroll
      for (int t = 0; t < 4; t++) asm volatile("" : "+v"(wex[t]));
      auto flush = [&](int base, int lim, auto clearc) {
        const int node = base + (((n32 >> 2) - base) & 7), comp = n32 & 3;
        constexpr bool clear = decltype(clearc)::value != 0;
        const float pt = pdot(IC<(clear ? 2 : 16)>{});
        if (node < lim) {
          char* mp = lds + L_IMG + (node == hnode ? 39 + w : node) * 528;
          if (comp == 3) {
#pragma unroll
            for (int q = 0; q < 16; q++)
              *(u32x2*)(mp + 256 + 8 * hh + 16 * q) = (u32x2){pk2(Y[q >> 2][4 * (q & 3)], Y[q >> 2][4 * (q & 3) + 1]),
                                                              pk2(Y[q >> 2][4 * (q & 3) + 2], Y[q >> 2][4 * (q & 3) + 3])};
          } else {
            *(float*)(mp + 512 + 4 * comp) = pt;
          }
        }
        if (clear) {
          const float keep = node < lim ? 0.f : 1.f;
#pragma unroll
          for (int t = 0; t < 4; t++) Y[t] *= keep;
        }
      };
      STAMP(1, 7);
      __syncthreads();
      STAMP(1, 8);
      u32x4 aex;
      {
        const u32x2 t0 = trread(lds + axoff), t1 = trread(lds + axoff + 4 * OHS);
        const u32x4 ra = *(const u32x4*)(lds + recoff);
        aex = hh ? (u32x4){t0[0], t0[1], t1[0], t1[1]} : ra;
      }
      XA = MFMA32(asbf(aex), asbf(wex[0]), (f32x16)(0.f)); PIN(XA);
      VMWAIT(4);
      STAMP(1, 23);
      u32x4 g0 = RDG(0, 0), g1 = RDG(1, 0), g2 = RDG(2, 0), g3 = RDG(3, 0);
      LGKMWAIT0();
      SB();
      XA = MFMA32(asbf(g0), asbf(id0), XA); PIN(XA);
      issue_a();
      XA = MFMA32(asbf(g1), asbf(id1), XA); PIN(XA);
      int fbase = base;
      __builtin_amdgcn_s_setprio(3);
      STAMP(1, 9);
      int tcount_ = 0;
#define EXPX(X, lo, hi) _Pragma("unroll") for (int r = lo; r < hi; r++) e[r] = __builtin_amdgcn_exp2f(X[r]); SB()
#define PACKX(X) _Pragma("unroll") for (int i = 0; i < 4; i++) { pa[i] = pk2(X[2 * i], X[2 * i + 1]); pb[i] = pk2(X[8 + 2 * i], X[8 + 2 * i + 1]); } \
  asm volatile("" : "+v"(pa), "+v"(pb)); SB()
#define SEG(t, p, sp) Y[t] = MFMA32(asbf(p), asbf(sp), Y[t]); PIN(Y[t])
#define PLUS1() e = MFMA32(asbf(one4), asbf(one4), e); PIN(e)
      for (int left = tend - tbeg, x = tbeg * 32 + 32; left > 0; left--, x += 32) {
        STAMPV(1, 16 + min(tcount_, 4)); tcount_++;
        if (__builtin_expect(left == 1, 0)) __builtin_amdgcn_s_setprio(1);
        SB();
        SEG(2, pa, sp0);
        EXPX(XA, 0, 8);
        SEG(2, pb, sp1);
        EXPX(XA, 8, 16);
        PLUS1();
        PACKX(XB);
        srcoff += 64; recoff += 512; cvoff += 256; ohoff += 64; axoff += 64;
        asm volatile("" : "+v"(srcoff), "+v"(recoff), "+v"(cvoff), "+v"(ohoff), "+v"(axoff)); SB();
        RCP_(0, 4);
        XB = MFMA32(asbf(aex), asbf(wex[1]), (f32x16)(0.f)); PIN(XB);
        RCP_(4, 8);
        XB = MFMA32(asbf(g2), asbf(id0), XB); PIN(XB);
        RCP_(8, 12);
        XB = MFMA32(asbf(g3), asbf(id1), XB); PIN(XB);
        RCP_(12, 16);
        XA = XA * e; PIN(XA);
        SEG(3, pa, sp0);
        VMWAIT(4);
        g0 = RDG(0, 1); g1 = RDG(1, 1); g2 = RDG(2, 1); g3 = RDG(3, 1);
        const u32x2 sv = *(const u32x2*)(lds + srcoff + 64);
        SB();
        EXPX(XB, 0, 8);
        SEG(3, pb, sp1);
        const u32x2 c0 = trread(lds + cvoff), c1 = trread(lds + cvoff + 32), c2 = trread(lds + cvoff + 128), c3 = trread(lds + cvoff + 160);
        const u32x4 o0 = *(const u32x4*)(lds + ohoff), o1 = *(const u32x4*)(lds + ohoff + 32);
        SB();
        EXPX(XB, 8, 16);
        PLUS1();
        PACKX(XA);
        sp0 = SPROD(o0, c0, c1);
        sp1 = SPROD(o1, c2, c3);
        asm volatile("" : "+v"(sp0), "+v"(sp1));
        LGKMWAIT0();
        SB();
        SETM0(m0b); DMA1(0, ad[0], srd, sfb); SB();
        RCP_(0, 4);
        XA = MFMA32(asbf(aex), asbf(wex[2]), (f32x16)(0.f)); PIN(XA);
        DMA1(1024, ad[1], srd, sfb); SB();
        RCP_(4, 8);
        XA = MFMA32(asbf(g0), asbf(id0), XA); PIN(XA);
        DMA1(2048, ad[2], srd, sfb); SB();
        RCP_(8, 12);
        XA = MFMA32(asbf(g1), asbf(id1), XA); PIN(XA);
        DMA1(3072, ad[3], srd, sfb); SB();
        RCP_(12, 16);
        XB = XB * e; PIN(XB);
        if (__builtin_expect(fbase != base, 0)) {
          flush(fbase, base, IC<1>{});
          fbase = base;
        }
        if (__builtin_expect(left <= 2, 0)) {
          if (left == 2) __builtin_amdgcn_s_setprio(2);
          else __builtin_amdgcn_s_setprio(0);
          prep(sv, false);
        } else {
          prep(sv, true);
        }
        asm volatile("" : "+v"(ad[0]), "+v"(ad[1]), "+v"(ad[2]), "+v"(ad[3]));
        SB();
        SEG(0, pa, sp0);
        const int nlast = node_at(min(x + 31, m - 1));
        SB();
        EXPX(XA, 0, 8);
        SEG(0, pb, sp1);
        EXPX(XA, 8, 16);
        PLUS1();
        PACKX(XB);
        RCP_(0, 4);
        XB = MFMA32(asbf(aex), asbf(wex[3]), (f32x16)(0.f)); PIN(XB);
        RCP_(4, 8);
        XB = MFMA32(asbf(g2), asbf(id0), XB); PIN(XB);
        RCP_(8, 12);
        XB = MFMA32(asbf(g3), asbf(id1), XB); PIN(XB);
        RCP_(12, 16);
        XA = XA * e; PIN(XA);
        if (__builtin_expect(left > 1 && nlast >= base + 8, 0)) {
          base = node_at(x);
          loadw(base);
        }
        SB();
        SEG(1, pa, sp0);
        VMWAIT(4);
        const u32x2 t0 = trread(lds + axoff), t1 = trread(lds + axoff + 4 * OHS);
        const u32x4 ra = *(const u32x4*)(lds + recoff);
        g0 = RDG(0, 0); g1 = RDG(1, 0); g2 = RDG(2, 0); g3 = RDG(3, 0);
        SB();
        EXPX(XB, 0, 8);
        SEG(1, pb, sp1);
        EXPX(XB, 8, 16);
        PLUS1();
        PACKX(XA);
        LGKMWAIT0();
        SB();
        aex = hh ? (u32x4){t0[0], t0[1], t1[0], t1[1]} : ra;
        PIN(aex);
        SETM0(m0a); DMA1(0, ad[0], srd, 0); SB();
        RCP_(0, 4);
        XA = MFMA32(asbf(aex), asbf(wex[0]), (f32x16)(0.f)); PIN(XA);
        DMA1(1024, ad[1], srd, 0); SB();
        RCP_(4, 8);
        XA = MFMA32(asbf(g0), asbf(id0), XA); PIN(XA);
        DMA1(2048, ad[2], srd, 0); SB();
        RCP_(8, 12);
        XA = MFMA32(asbf(g1), asbf(id1), XA); PIN(XA);
        DMA1(3072, ad[3], srd, 0); SB();
        RCP_(12, 16);
        XB = XB * e; PIN(XB);
      }
      SEG(2, pa, sp0);
      SEG(2, pb, sp1);
      PACKX(XB);
      SEG(3, pa, sp0);
      SEG(3, pb, sp1);
      STAMP(1, 10);
      flush(base, lnode + 1, IC<0>{});
      STAMP(1, 11);
    }
  } else {
    tid = threadIdx.x;
    asm volatile("" : "+v"(tid));
    lane = tid & 63; gq = lane >> 4; c16 = lane & 15; hh = lane >> 5; n32 = lane & 31;
    lane_consts();
    t4 = *(const u32x4*)(ws + WS_T + b * 1024 + lane * 16);
    decode();
    const rsrc_t rs_rec = mkrs(ws + WS_R, NE * 12), rs_pos = mkrs(pos, NN * 6);
    for (int q = tid; q < 40 * 33; q += 512) *(u32x4*)(lds + L_MG + q * 16) = (u32x4)(0u);
    for (int r0 = 0; r0 < mtot; r0 += CAP) {
      const int m = min(mtot - r0, CAP);
      __syncthreads();
      for (int q = tid; q < (L_HM - L_OH) / 16; q += 512) *(u32x4*)(lds + L_OH + q * 16) = (u32x4)(0u);
      const int lim = min(scnt, r0 + CAP - sexc);
      for (int i = max(r0 - sexc, 0) + (w >> 2); i < lim; i += 2)
        *(u32x3*)(lds + L_STG + (sexc + i - r0) * 16) = __builtin_amdgcn_raw_buffer_load_b96(rs_rec, sbase + i * 12, 0, 0);
      __syncthreads();
      for (int f = tid; f < m; f += 512) {
        u32* rp = (u32*)(lds + L_STG + f * 16);
        rp[3] = atomicAdd((u32*)(lds + L_CNT) + ((rp[2] >> 16) - n0), 1u);
      }
      __syncthreads();
      const int nst = count_nodes(m);
      for (int f0 = w * 64; f0 < m; f0 += 512) {
        const bool ok = f0 + lane < m;
        const u32x4 rc = *(const u32x4*)(lds + L_STG + (f0 + lane) * 16);
        const u32 dl = ok ? (rc[2] >> 16) - n0 : 0u;
        const u32 pp = rc[3] + __builtin_amdgcn_ds_bpermute(dl * 4, nst), p = ok ? pp : CAP;
        const u32x2 ps = __builtin_amdgcn_raw_buffer_load_b64(rs_pos, ((rc[2] & 0xffffu) * 6) & ~3u, 0, 0);
        *(u16*)(lds + L_SRC + 2 * p) = (u16)rc[2];
        place((u32x3){rc[0], rc[1], rc[2]}, ps, *(const u32x2*)(lds + L_NODE + dl * 8), dl, p);
      }
      __syncthreads();
      const int nt = w < 4 ? (m + 31) >> 5 : 0;
      for (int tile = w; tile < nt; tile += 4) {
        const int last = node_at(min(tile * 32 + 31, m - 1));
        prep(*(const u32x2*)(lds + srcoff + tile * 64), true);
        SETM0(m0a); DMA1(0, ad[0], srd_g, 0); DMA1(1024, ad[1], srd_g, 0); DMA1(2048, ad[2], srd_g, 0); DMA1(3072, ad[3], srd_g, 0);
        SETM0(m0b); DMA1(0, ad[0], srd_g, 128); DMA1(1024, ad[1], srd_g, 128); DMA1(2048, ad[2], srd_g, 128); DMA1(3072, ad[3], srd_g, 128);
        const u32x2 t0 = trread(lds + axoff + tile * 64), t1 = trread(lds + axoff + tile * 64 + 4 * OHS);
        const u32x4 ra = *(const u32x4*)(lds + recoff + tile * 512);
        const u32x4 aex = hh ? (u32x4){t0[0], t0[1], t1[0], t1[1]} : ra;
        for (int base = node_at(tile * 32); base <= last; base += 8) {
          loadw(base);
          u32x4 sp[2];
#pragma unroll
          for (int s = 0; s < 2; s++) {
            const u32x2 c0 = trread(lds + cvoff + tile * 256 + s * 128), c1 = trread(lds + cvoff + tile * 256 + s * 128 + 32);
            const u32x4 oh = *(const u32x4*)(lds + ohoff + tile * 64 + 32 * s);
            sp[s] = SPROD(oh, c0, c1);
          }
#pragma unroll 1
          for (int j = 0; j < 16; j++) {
            const int dn = *(const u32*)(lds + L_REC + (tile * 32 + 16 * (j >> 3) + 8 * hh + (j & 7)) * 16 + 12);
            const u32 mk = (dn < base || dn >= base + 8) ? ((j & 1) ? 0x0000ffffu : 0xffff0000u) : 0xffffffffu;
#pragma unroll
            for (int i = 0; i < 8; i++) sp[i >> 2][i & 3] &= (i == (j >> 1)) ? mk : 0xffffffffu;
          }
          VMWAIT0();
#pragma unroll
          for (int t = 0; t < 4; t++) {
            const u32x4 ga_ = RDG((2 * t) & 3, t >> 1), gb_ = RDG((2 * t + 1) & 3, t >> 1);
            f32x16 xc = MFMA32(asbf(aex), asbf(wex[t]), (f32x16)(0.f)), e;
            xc = MFMA32(asbf(ga_), asbf(id0), xc);
            xc = MFMA32(asbf(gb_), asbf(id1), xc);
            u32x4 pa, pb;
            EXP_(0, 16);
            e = e + 1.0f;
            RCP_(0, 16);
            e = e * xc;
            PACK_();
            Y[t] = MFMA32(asbf(pa), asbf(sp[0]), (f32x16)(0.f));
            Y[t] = MFMA32(asbf(pb), asbf(sp[1]), Y[t]);
          }
          const int node = base + (((n32 >> 2) - base) & 7), comp = n32 & 3;
          const float pt = pdot(IC<4>{});
          if (node <= last) {
            float* mp = (float*)(lds + L_MG) + node * 132;
            if (comp == 3) {
#pragma unroll
              for (int t = 0; t < 4; t++)
#pragma unroll
                for (int r = 0; r < 16; r++) atomicAdd(mp + 4 * hh + 32 * t + 8 * (r >> 2) + (r & 3), Y[t][r]);
            } else if (hh == 0) {
              atomicAdd(mp + 128 + comp, pt);
            }
          }
        }
        LGKMWAIT0();
      }
    }
    __syncthreads();
    for (int q = tid; q < 40 * 66; q += 512) {
      const int row = q / 66, c = q - row * 66;
      const f32x2 v = *(const f32x2*)(lds + L_MG + row * 528 + c * 8);
      if (c < 64) *(u32*)(lds + L_IMG + row * 528 + 256 + c * 4) = pk2(v[0], v[1]);
      else *(f32x2*)(lds + L_IMG + row * 528 + c * 8) = v;
    }
    __syncthreads();
    const u32x4 srd_wh = mksrd(wh, 65536);
    SETM0(m0a); DMA1(0, vsrc, srd_wh, 0); DMA1(1024, vsrc, srd_wh, 7168); DMA1(2048, vsrc, srd_wh, 14336); DMA1(3072, vsrc, srd_wh, 21504);
    SETM0(m0b); DMA1(0, vsrc, srd_wh, 32768); DMA1(1024, vsrc, srd_wh, 39936); DMA1(2048, vsrc, srd_wh, 47104); DMA1(3072, vsrc, srd_wh, 54272);
    VMWAIT0();
  }

  int tid2 = threadIdx.x;
  asm volatile("" : "+v"(tid2));
  const int c16e = tid2 & 15, gqe = (tid2 >> 4) & 3;
  const char* const xbe = lds + L_IMG + c16e * 528 + 16 * gqe;
  f32x4 acc[3], acm[3];
  const char* const wfp = lds + L_STG + w * 8192 + (8 * gqe + (c16e >> 2)) * 32 + 8 * (c16e & 3);
  VMWAIT(4);
  u32x4 wf2[4];
#pragma unroll
  for (int rt = 0; rt < 3; rt++) acc[rt] = acm[rt] = (f32x4)(0.f);
  {
    u32x4 wf1[4], xf[4][3];
#pragma unroll
    for (int s = 0; s < 4; s++) {
      const u32x2 x0 = trread(wfp + s * 1024), x1 = trread(wfp + s * 1024 + 128);
      const u32x2 y0 = trread(wfp + 4096 + s * 1024), y1 = trread(wfp + 4096 + s * 1024 + 128);
      wf1[s] = (u32x4){x0[0], x0[1], x1[0], x1[1]};
      wf2[s] = (u32x4){y0[0], y0[1], y1[0], y1[1]};
#pragma unroll
      for (int rt = 0; rt < 3; rt++) xf[s][rt] = *(const u32x4*)(xbe + rt * 8448 + 64 * s);
    }
    SB();
#pragma unroll
    for (int s = 0; s < 4; s++)
#pragma unroll
      for (int rt = 0; rt < 3; rt++) acc[rt] = MFMA16(asbf(wf1[s]), asbf(xf[s][rt]), acc[rt]);
  }
  u32x2 hr[3];
  u32 hm[3], hl[3];
#pragma unroll
  for (int rt = 0; rt < 3; rt++) {
    hr[rt] = *(const u32x2*)(lds + L_IMG + c16e * 528 + 8 * gqe + rt * 8448 + 32 * w);
    hm[rt] = 16 * rt + c16e < 40 ? ((const u32*)(lds + L_HM))[16 * rt + c16e] : 0u;
    hl[rt] = 64 * gqe + 28 + 4 * __builtin_ctz(hm[rt] | 256u);
    hm[rt] &= hm[rt] - 1;
  }
  STAMP(1, 12);
  __syncthreads();
  STAMP(1, 13);
  {
    u32x4 xf[3][4];
#pragma unroll
    for (int s = 0; s < 4; s++)
#pragma unroll
      for (int rt = 2; rt >= 0; rt--) xf[rt][s] = *(const u32x4*)(xbe + 256 + rt * 8448 + 64 * s);
    SB();
#pragma unroll
    for (int s = 0; s < 4; s++)
#pragma unroll
      for (int rt = 2; rt >= 0; rt--) acm[rt] = MFMA16(asbf(wf2[s]), asbf(xf[rt][s]), acm[rt]);
  }
  const f32x4 hd = acm[2];
  for (;;) {
#pragma unroll
    for (int rt = 0; rt < 3; rt++)
#pragma unroll
      for (int r = 0; r < 4; r++) {
        const float hv_ = hd[r];
        acm[rt][r] += __uint_as_float(__builtin_amdgcn_ds_bpermute(hl[rt], __float_as_uint(hv_)));
      }
    if (__builtin_expect(__builtin_amdgcn_ballot_w64((hm[0] | hm[1] | hm[2]) != 0) == 0, 1)) break;
#pragma unroll
    for (int rt = 0; rt < 3; rt++) {
      hl[rt] = 64 * gqe + 28 + 4 * __builtin_ctz(hm[rt] | 256u);
      hm[rt] &= hm[rt] - 1;
    }
  }
  STAMP(1, 14);
  {
    const rsrc_t rs_o = mkrs(out + (size_t)n0 * 128, nn * 256);
#pragma unroll
    for (int rt = 0; rt < 3; rt++) {
      u32 o[2];
#pragma unroll
      for (int r = 0; r < 2; r++) {
        const f32x2 z = (f32x2){acc[rt][2 * r], acc[rt][2 * r + 1]} + (f32x2){acm[rt][2 * r], acm[rt][2 * r + 1]} * NLN2;
        const f32x2 t = z * NLOG2E;
        const f32x2 d = (f32x2){__builtin_amdgcn_exp2f(t[0]), __builtin_amdgcn_exp2f(t[1])} + 1.0f;
        const f32x2 v = z * (f32x2){__builtin_amdgcn_rcpf(d[0]), __builtin_amdgcn_rcpf(d[1])} + (f32x2){bf2f(hr[rt][r]), bfhi(hr[rt][r])};
        o[r] = pk2(v[0], v[1]);
      }
      __builtin_amdgcn_raw_buffer_store_b64((u32x2){o[0], o[1]}, rs_o, c16e * 256 + 8 * gqe, rt * 4096 + 32 * w, 2);
    }
  }
  if (w < 2) {
    const int nl = __umul24(tid2, 21846) >> 16, k = tid2 - 3 * nl;
    if (tid2 < 3 * nn) {
      const char* mp = lds + L_IMG + 512 + 4 * k;
      u32 hq = ((const u32*)(lds + L_HM))[nl];
      float pv = *(const float*)(mp + nl * 528) + bf2f(*(const u16*)(lds + L_NODE + nl * 2 + tid2 * 2));
      while (hq) {
        pv += *(const float*)(mp + (39 + __builtin_ctz(hq)) * 528);
        hq &= hq - 1;
      }
      out[(size_t)NN * 128 + n0 * 3 + tid2] = (u16)pk2(pv, 0.f);
    }
  }
  VMWAIT0();
  STAMP(1, 15); RSTAMP(1, 31);
}

extern "C" void kernel_launch(void* const* d_in, const int* in_sizes, int n_in, void* d_out, int out_size,
                              void* d_ws, size_t ws_size, hipStream_t stream) {
  const u16* h = (const u16*)d_in[0];
  const u16* pos = (const u16*)d_in[1];
  const u16* ea = (const u16*)d_in[2];
  const int* src = (const int*)d_in[3];
  const int* dst = (const int*)d_in[4];
  const u16* we = (const u16*)d_in[5];
  const u16* wx = (const u16*)d_in[6];
  const u16* wh = (const u16*)d_in[7];
  egnn_k1<<<NB, 512, 0, stream>>>(h, ea, src, dst, we, (char*)d_ws);
  egnn_k2<<<NB, 512, 0, stream>>>(h, pos, we, wx, wh, (const char*)d_ws, (u16*)d_out);
}
```

```cpp
#include <hip/hip_runtime.h>
#include <hip/hip_bf16.h>

typedef unsigned short u16;
typedef unsigned int u32;
typedef float f32x2 __attribute__((ext_vector_type(2)));
typedef float f32x4 __attribute__((ext_vector_type(4)));
typedef float f32x16 __attribute__((ext_vector_type(16)));
typedef __bf16 bf16x8 __attribute__((ext_vector_type(8)));
typedef __bf16 bf16x2 __attribute__((ext_vector_type(2)));
typedef u32 u32x2 __attribute__((ext_vector_type(2)));
typedef u32 u32x3 __attribute__((ext_vector_type(3)));
typedef u32 u32x4 __attribute__((ext_vector_type(4)));
typedef int i32x4 __attribute__((ext_vector_type(4)));
typedef short s16x4 __attribute__((ext_vector_type(4)));
typedef u16 u16x2 __attribute__((ext_vector_type(2)));
#define LAS __attribute__((address_space(3)))
typedef __amdgpu_buffer_rsrc_t rsrc_t;

#define PFWG 64
constexpr int NN = 10000;
constexpr int NE = 320000;
constexpr int NCH = 250;
constexpr int CH = 1280;
constexpr int NB = 256;
constexpr int CAP = 1408;
constexpr float NLOG2E = -1.4426950408889634f;
constexpr float NLN2 = -0.6931471805599453f;
constexpr u32 OH = 0x3F81u;
constexpr u32 OHINV = 0x81u;

constexpr size_t WS_T = 0;
constexpr size_t WS_R = WS_T + (size_t)NB * 256 * 4;
constexpr size_t WS_B = WS_R + (size_t)NE * 12;

__device__ __forceinline__ float bf2f(u32 v) { return __uint_as_float(v << 16); }
__device__ __forceinline__ float bfhi(u32 v) { return __uint_as_float(v & 0xffff0000u); }
__device__ __forceinline__ u32 pk2(float a, float b) {
  f32x2 v = {a, b};
  return __builtin_bit_cast(u32, __builtin_convertvector(v, bf16x2));
}
__device__ __forceinline__ u32 mul16x2(u32 a, u32 b) {
  return __builtin_bit_cast(u32, __builtin_bit_cast(u16x2, a) * __builtin_bit_cast(u16x2, b));
}
__device__ __forceinline__ int wave_incl_scan(int x) {
  x += __builtin_amdgcn_update_dpp(0, x, 0x111, 0xf, 0xf, false);
  x += __builtin_amdgcn_update_dpp(0, x, 0x112, 0xf, 0xf, false);
  x += __builtin_amdgcn_update_dpp(0, x, 0x114, 0xf, 0xf, false);
  x += __builtin_amdgcn_update_dpp(0, x, 0x118, 0xf, 0xf, false);
  x += __builtin_amdgcn_update_dpp(0, x, 0x142, 0xa, 0xf, false);
  x += __builtin_amdgcn_update_dpp(0, x, 0x143, 0xc, 0xf, false);
  return x;
}
__device__ __forceinline__ bf16x8 asbf(u32x4 v) { return __builtin_bit_cast(bf16x8, v); }
__device__ __forceinline__ u32x2 trread(const char* p) {
  return __builtin_bit_cast(u32x2, __builtin_amdgcn_ds_read_tr16_b64_v4i16((LAS s16x4*)p));
}
__device__ __forceinline__ rsrc_t mkrs(const void* p, u32 bytes) { return __builtin_amdgcn_make_buffer_rsrc((void*)p, 0, bytes, 0x00020000); }
__device__ __forceinline__ void dma16(rsrc_t rs, char* ldsp, u32 voff, u32 soff) {
  __builtin_amdgcn_raw_ptr_buffer_load_lds(rs, (LAS void*)ldsp, 16, voff, soff, 0, 0);
}
#define MFMA32(a, b, c) __builtin_amdgcn_mfma_f32_32x32x16_bf16(a, b, c, 0, 0, 0)
#define MFMA16(a, b, c) __builtin_amdgcn_mfma_f32_16x16x32_bf16(a, b, c, 0, 0, 0)
#define SB() __builtin_amdgcn_sched_barrier(0)
#define VMWAIT0() asm volatile("s_waitcnt vmcnt(0)" ::: "memory")
#define STAMPDECL
#define STAMP(buf, i)
#define STAMPV(buf, i)
#define RSTAMP(buf, i)
#define DRAIN()

template <int V> struct IC { static constexpr int value = V; };
__device__ __forceinline__ u32x4 mksrd(const void* p, u32 bytes) {
  const unsigned long long a = (unsigned long long)p;
  return (u32x4){(u32)a, (u32)(a >> 32) & 0xffffu, bytes, 0x00020000u};
}
#define DMA1(J, A, SRD, SOFF) asm volatile("buffer_load_dwordx4 %0, %1, %2 offen offset:" #J " lds" :: "v"(A), "s"(SRD), "s"(SOFF) : "memory")
#define LDB(T, DST, VO, SRD, SO) asm volatile("buffer_load_" T " %0, %1, %2, %3 offen" : "=v"(DST) : "v"(VO), "s"(SRD), "s"(SO) : "memory")
#define SETM0(V) asm volatile("s_mov_b32 m0, %0\n\ts_nop 0" :: "s"(V) : "memory")
#define VMWAIT(N) asm volatile("s_waitcnt vmcnt(" #N ")" ::: "memory")
#define LGKMWAIT0() asm volatile("s_waitcnt lgkmcnt(0)" ::: "memory")
#define PIN(x) do { asm volatile("" : "+v"(x)); SB(); } while (0)

constexpr int K1_H = 32768;
constexpr int K1_IMG = K1_H + 48 * 256;
constexpr int K1_HIST = K1_IMG + 16384;
constexpr int K1_LDS = K1_HIST + 2 * NB * 4 + 32;

extern "C" __global__ __launch_bounds__(512) void egnn_k1(const u16* __restrict__ h, const u16* __restrict__ ea,
                                               const int* __restrict__ src, const int* __restrict__ dst,
                                               const u16* __restrict__ we, char* __restrict__ ws) {
  __shared__ __attribute__((aligned(128))) char lds[K1_LDS];
  const int tid = threadIdx.x, lane = tid & 63;
  const int w = __builtin_amdgcn_readfirstlane(tid >> 6);
  const int c = (blockIdx.x & 7) * 32 + (blockIdx.x >> 3);
  const int gq = lane >> 4, c16 = lane & 15;
  u32* const hist = (u32*)(lds + K1_HIST);
  asm volatile("" :: "s"(h), "s"(ea), "s"(src), "s"(dst), "s"(we), "s"(ws));
  STAMPDECL; RSTAMP(0, 30); STAMP(0, 0);
  if (tid < NB) hist[tid] = 0;
  else if (tid < NB + 8) hist[NB + tid] = 0;
  __syncthreads();
  auto meet = [&](int k) {
    LGKMWAIT0();
    if (lane == 0) __hip_atomic_fetch_add(&hist[2 * NB + k], 1u, __ATOMIC_RELAXED, __HIP_MEMORY_SCOPE_WORKGROUP);
    while (__hip_atomic_load(&hist[2 * NB + k], __ATOMIC_RELAXED, __HIP_MEMORY_SCOPE_WORKGROUP) < 4u) __builtin_amdgcn_s_sleep(1);
    asm volatile("" ::: "memory");
  };
  if (w < 4) {
    const u32 eo = tid * 20, nb = c < NCH ? CH * 4 : 0;
    const rsrc_t rs_d = mkrs(dst + c * CH, nb), rs_s = mkrs(src + c * CH, nb), rs_a = mkrs(ea + (size_t)c * CH * 4, 2 * nb);
    const u32x4 d4 = __builtin_amdgcn_raw_buffer_load_b128(rs_d, eo, 0, 0);
    const u32 d1 = __builtin_amdgcn_raw_buffer_load_b32(rs_d, eo + 16, 0, 0);
    const u32x4 s4 = __builtin_amdgcn_raw_buffer_load_b128(rs_s, eo, 0, 0);
    const u32 s1 = __builtin_amdgcn_raw_buffer_load_b32(rs_s, eo + 16, 0, 0);
    const u32x4 a0 = __builtin_amdgcn_raw_buffer_load_b128(rs_a, 2 * eo, 0, 0);
    const u32x4 a1 = __builtin_amdgcn_raw_buffer_load_b128(rs_a, 2 * eo + 16, 0, 0);
    const u32x2 a2 = __builtin_amdgcn_raw_buffer_load_b64(rs_a, 2 * eo + 32, 0, 0);
    __builtin_amdgcn_s_setprio(1);
    STAMP(0, 1);
    if (c < NCH) {
      u32* const img = (u32*)(lds + K1_IMG);
      u32* const base = hist + NB;
      u32 bk[5], rk[5];
#pragma unroll
      for (int i = 0; i < 5; i++) {
        bk[i] = __umul24(i < 4 ? d4[i & 3] : d1, 214749u) >> 23;
        rk[i] = atomicAdd(&hist[bk[i]], 1u);
      }
      STAMP(0, 2);
      meet(1);
      STAMP(0, 3);
      {
        const u32x4 v = *(const u32x4*)(hist + 4 * lane);
        const int s = v[0] + v[1] + v[2] + v[3];
        const int ex = wave_incl_scan(s) - s;
        const u32x4 bs = {(u32)ex, ex + v[0], ex + v[0] + v[1], ex + v[0] + v[1] + v[2]};
        *(u32x4*)(base + 4 * lane) = bs;
        const u32 bb = w == 0 ? bs[0] : w == 1 ? bs[1] : w == 2 ? bs[2] : bs[3];
        const u32 vv = w == 0 ? v[0] : w == 1 ? v[1] : w == 2 ? v[2] : v[3];
        ((u32*)(ws + WS_T))[(4 * lane + w) * 256 + c] = bb | (vv << 16);
      }
#pragma unroll
      for (int i = 0; i < 5; i++) {
        u32* p = img + 3 * (base[bk[i]] + rk[i]);
        p[0] = i < 2 ? a0[2 * (i & 1)] : i < 4 ? a1[2 * (i & 1)] : a2[0];
        p[1] = i < 2 ? a0[2 * (i & 1) + 1] : i < 4 ? a1[2 * (i & 1) + 1] : a2[1];
        p[2] = (i < 4 ? s4[i & 3] : s1) | ((i < 4 ? d4[i & 3] : d1) << 16);
      }
      STAMP(0, 4);
      meet(2);
      STAMP(0, 5);
      const rsrc_t rs_r = mkrs(ws + WS_R + (size_t)c * (CH * 12), CH * 12);
#pragma unroll
      for (int i = 0; i < 4; i++)
        __builtin_amdgcn_raw_buffer_store_b128(((const u32x4*)img)[tid + 256 * i], rs_r, tid * 16 + i * 4096, 0, 0);
    }
    STAMP(0, 6); DRAIN(); STAMP(0, 7); RSTAMP(0, 31);
  } else {
    const int u = w - 4, n0 = (c * 625 + 15) >> 4, nn = (((c + 1) * 625 + 15) >> 4) - n0;
    const u32 ldsb = (u32)(size_t)(LAS char*)lds;
    {
      const u32x4 srd_h = mksrd(h + (size_t)n0 * 128, nn * 256), srd_w = mksrd(we + 128 * 128, 32768);
      const u32 vw = gq * 256 + 16 * (c16 ^ (4 * gq));
      SETM0(ldsb + u * 8192);
      DMA1(0, vw, srd_w, u * 8192); DMA1(1024, vw, srd_w, u * 8192); DMA1(2048, vw, srd_w, u * 8192); DMA1(3072, vw, srd_w, u * 8192);
      SETM0(ldsb + u * 8192 + 4096);
      DMA1(0, vw, srd_w, u * 8192 + 4096); DMA1(1024, vw, srd_w, u * 8192 + 4096); DMA1(2048, vw, srd_w, u * 8192 + 4096); DMA1(3072, vw, srd_w, u * 8192 + 4096);
      SETM0(ldsb + K1_H + u * 3072);
      DMA1(0, gq * 256 + 16 * (c16 ^ ((12 * u + gq) & 15)), srd_h, u * 3072);
      DMA1(1024, gq * 256 + 16 * (c16 ^ ((12 * u + 4 + gq) & 15)), srd_h, u * 3072);
      DMA1(2048, gq * 256 + 16 * (c16 ^ ((12 * u + 8 + gq) & 15)), srd_h, u * 3072);
    }
    STAMP(0, 1);
    VMWAIT0();
    STAMP(0, 2);
    u32 pfv = 0;
    const bool pf = blockIdx.x < PFWG && u < 3;
    if (pf) {
      unsigned long long pc;
      u32 off;
      asm volatile("s_getpc_b64 %0\n.Lpc%=:\n\ts_mov_b32 %1, egnn_k2-.Lpc%=" : "=s"(pc), "=s"(off));
      asm volatile("global_load_dword %0, %1, %2" : "=v"(pfv) : "v"(min(u * 64 + lane, 147) * 128), "s"(pc + off) : "memory");
    }
    meet(0);
    STAMP(0, 3);
    f32x4 acc[3][2];
#pragma unroll
    for (int rt = 0; rt < 3; rt++) acc[rt][0] = acc[rt][1] = (f32x4)(0.f);
    const char* wb = lds + (8 * gq + (c16 >> 2)) * 256 + 16 * ((4 * u + (c16 & 3)) ^ (c16 & 12));
    const int hb = K1_H + c16 * 256 + 16 * (gq ^ c16);
    u32x4 fa[4], fb[4], hf[4][3];
#pragma unroll
    for (int s = 0; s < 4; s++) {
      const u32x2 x0 = trread(wb + (32 * s) * 256), x1 = trread(wb + (32 * s + 4) * 256);
      const u32x2 y0 = trread(wb + (32 * s) * 256 + 8), y1 = trread(wb + (32 * s + 4) * 256 + 8);
      fa[s] = (u32x4){x0[0], x0[1], x1[0], x1[1]};
      fb[s] = (u32x4){y0[0], y0[1], y1[0], y1[1]};
#pragma unroll
      for (int rt = 0; rt < 3; rt++) hf[s][rt] = *(const u32x4*)(lds + (hb ^ (64 * s)) + rt * 4096);
    }
    SB();
#pragma unroll
    for (int s = 0; s < 4; s++)
#pragma unroll
      for (int rt = 0; rt < 3; rt++) {
        acc[rt][0] = MFMA16(asbf(fa[s]), asbf(hf[s][rt]), acc[rt][0]);
        acc[rt][1] = MFMA16(asbf(fb[s]), asbf(hf[s][rt]), acc[rt][1]);
      }
    const rsrc_t rs_b = mkrs(ws + WS_B + (size_t)n0 * 256, nn * 256);
#pragma unroll
    for (int rt = 0; rt < 3; rt++) {
      const f32x4 a = acc[rt][0] * NLOG2E, b = acc[rt][1] * NLOG2E;
      const u32x4 o = {pk2(a[0], a[1]), pk2(a[2], a[3]), pk2(b[0], b[1]), pk2(b[2], b[3])};
      __builtin_amdgcn_raw_buffer_store_b128(o, rs_b, c16 * 256 + 16 * gq + 64 * u + rt * 4096, 0, 0);
    }
    if (pf) asm volatile("s_waitcnt vmcnt(0)" :: "v"(pfv) : "memory");
    STAMP(0, 6); DRAIN(); STAMP(0, 7); RSTAMP(0, 31);
  }
}

constexpr int OHS = 2 * CAP + 16;
constexpr int L_NODE = 0;
constexpr int L_WX = L_NODE + 320;
constexpr int L_WS = L_WX + 512;
constexpr int L_REC = L_WS + 16;
constexpr int L_CV = L_REC + 16 * (CAP + 1);
constexpr int L_SRC = L_CV + 8 * (CAP + 2);
constexpr int L_OH = L_SRC + 2 * (CAP + 8);
constexpr int L_CNT = L_OH + 8 * OHS;
constexpr int L_HM = L_CNT + 256;
constexpr int L_IMG = L_HM + 160;
constexpr int L_AT = L_IMG + 48 * 528;
constexpr int L_STG = (L_AT + 128 * 96 + 127) & ~127;
constexpr int K2_LDS = L_STG + 65536;
constexpr int L_MG = L_STG + 32768;
static_assert(K2_LDS <= 163840, "LDS");
static_assert(L_CV % 16 == 0 && L_OH % 16 == 0 && L_SRC % 16 == 0 && L_IMG % 16 == 0 && L_AT % 16 == 0, "align");
static_assert(L_IMG - L_OH > 5 * 4096 && L_IMG - L_OH <= 6 * 4096, "zero sweep");

extern "C" __global__ __launch_bounds__(512, 2) void egnn_k2(const u16* __restrict__ h, const u16* __restrict__ pos,
                                                  const u16* __restrict__ we, const u16* __restrict__ wx,
                                                  const u16* __restrict__ wh, const char* __restrict__ ws,
                                                  u16* __restrict__ out) {
  __shared__ __attribute__((aligned(128))) char lds[K2_LDS];
  int tid = threadIdx.x, lane = tid & 63;
  const int w = __builtin_amdgcn_readfirstlane(tid >> 6);
  const int b = (blockIdx.x & 7) * 32 + (blockIdx.x >> 3);
  const int n0 = (b * 625 + 15) >> 4, nn = (((b + 1) * 625 + 15) >> 4) - n0;
  int gq = lane >> 4, c16 = lane & 15, hh = lane >> 5, n32 = lane & 31;
  const u32 ldsb = (u32)(size_t)(LAS char*)lds;
  const u32x4 srd_g = mksrd(ws + WS_B - 4096, 4096 + NN * 256), srd_pos = mksrd(pos, NN * 6);

  asm volatile("" :: "s"(h), "s"(pos), "s"(we), "s"(wx), "s"(wh), "s"(ws), "s"(out));
  STAMPDECL; RSTAMP(1, 30); STAMP(1, 0);
  u32 cwv[6];
  u32x4 hv[3], t4;
  if (w < 4) {
    LDB("dwordx4", t4, lane * 16, mksrd(ws + WS_T + b * 1024, 1024), 0);
  } else {
    const int ty = tid - 256;
    const u32x4 srd_h = mksrd(h + (size_t)n0 * 128, nn * 256), srd_we = mksrd(we, 261 * 256);
#pragma unroll
    for (int i = 0; i < 3; i++) LDB("dwordx4", hv[i], ty * 16, srd_h, i * 4096);
    if (w >= 6) {
#pragma unroll
      for (int k = 0; k < 5; k++) LDB("ushort", cwv[k], tid * 2, srd_we, (256 + k) * 256 - 768);
      LDB("ushort", cwv[5], tid * 2 - 768, mksrd(wx, 256), 0);
    } else {
      LDB("ushort", cwv[0], ty * 2, srd_pos, n0 * 6);
    }
    const u32 vw = gq * 256 + 16 * (c16 ^ (4 * gq)), so = (w - 4) * 8192;
    SETM0(ldsb + L_STG + so);
    DMA1(0, vw, srd_we, so); DMA1(1024, vw, srd_we, so); DMA1(2048, vw, srd_we, so); DMA1(3072, vw, srd_we, so);
    SETM0(ldsb + L_STG + so + 4096);
    DMA1(0, vw, srd_we, so + 4096); DMA1(1024, vw, srd_we, so + 4096); DMA1(2048, vw, srd_we, so + 4096); DMA1(3072, vw, srd_we, so + 4096);
    SB();
#pragma unroll
    for (int i = 0; i < 5; i++) *(u32x4*)(lds + L_OH + ty * 16 + i * 4096) = (u32x4)(0u);
    if (ty < (L_IMG - L_OH - 5 * 4096) / 16) *(u32x4*)(lds + L_OH + ty * 16 + 5 * 4096) = (u32x4)(0u);
    char* ip = lds + L_IMG + (ty >> 4) * 528 + 256 + c16 * 16;
#pragma unroll
    for (int i = 0; i < 3; i++) *(u32x4*)(ip + i * 16 * 528) = (u32x4)(0u);
    if (ty < 48) *(u32x4*)(lds + L_IMG + ty * 528 + 512) = (u32x4)(0u);
  }
  SB();
  u32x4 id0, id1, one4 = {hh ? 0u : 0x3F80u, 0u, 0u, 0u};
  int ga[4], recoff, ohoff, cvoff, axoff, srcoff;
  u32 go[4], vsrc;
  auto lane_consts = [&]() {
    const int k0 = n32 - 8 * hh;
    const u32 one = 0x3F80u << ((k0 & 1) * 16);
#pragma unroll
    for (int i = 0; i < 4; i++) {
      id0[i] = (k0 >> 1) == i ? one : 0u;
      id1[i] = (k0 >> 1) == 8 + i ? one : 0u;
    }
    const int prow = (n32 & 19) | ((n32 & 4) << 1) | ((n32 & 8) >> 1);
    const int sa = prow & 3, sb = prow >> 2;
#pragma unroll
    for (int s = 0; s < 4; s++) ga[s] = (L_STG + w * 8192 + (8 * sa + sb) * 128 + 16 * (hh ^ (2 * sa + ((sb >> 1) & 1)))) ^ (32 * s);
#pragma unroll
    for (int j = 0; j < 4; j++) go[j] = 4096 - 1024 * j + 16 * ((lane & 7) ^ (2 * j + ((lane >> 4) & 1)));
    recoff = L_REC + prow * 16;
    ohoff = L_OH + (n32 >> 2) * OHS + 16 * hh;
    cvoff = L_CV + (8 * hh + (c16 >> 2)) * 8;
    axoff = L_OH + (c16 >> 2) * OHS + 32 * (gq & 1) + 16 * (c16 & 1) + 8 * ((c16 >> 1) & 1);
    srcoff = L_SRC + (lane >> 3) * 8;
    vsrc = (lane >> 1) * 256 + 32 * w + 16 * (lane & 1);
  };
  lane_consts();
  const u32 m0a = ldsb + L_STG + w * 8192, m0b = m0a + 4096;
  asm volatile("" : "+v"(one4), "+v"(id0), "+v"(id1), "+v"(ga[0]), "+v"(ga[1]), "+v"(ga[2]), "+v"(ga[3]), "+v"(go[0]), "+v"(go[1]), "+v"(go[2]), "+v"(go[3]));
  asm volatile("" : "+v"(recoff), "+v"(ohoff), "+v"(cvoff), "+v"(axoff), "+v"(srcoff), "+v"(vsrc));
  SB();
  int mtot, scnt, sexc;
  u32 sbase;
  auto decode = [&]() {
    if (lane == 62) t4[2] = t4[3] = 0;
    if (lane == 63) t4 = (u32x4)(0u);
    const int c0 = t4[0] >> 16, c1 = c0 + (t4[1] >> 16), c2 = c1 + (t4[2] >> 16), c3 = c2 + (t4[3] >> 16);
    const int inc = wave_incl_scan(c3);
    mtot = __builtin_amdgcn_readlane(inc, 63);
    const int wq = w & 3;
    const u32 tv = wq == 0 ? t4[0] : wq == 1 ? t4[1] : wq == 2 ? t4[2] : t4[3];
    scnt = tv >> 16;
    sexc = inc - c3 + (wq == 0 ? 0 : wq == 1 ? c0 : wq == 2 ? c1 : c2);
    sbase = ((4 * lane + wq) * CH + (tv & 0xffffu)) * 12;
  };
  if (w < 4) {
    VMWAIT0(); SB();
    STAMP(1, 1);
    decode();
    if (tid == 0) *(int*)(lds + L_WS) = mtot;
    if (mtot <= CAP) {
      u32* p = (u32*)(lds + L_CV) + sexc;
      u32 v = sbase;
      if (scnt & 1) p[scnt - 1] = v + 12 * (scnt - 1);
#pragma clang loop vectorize(disable) unroll(disable)
      for (int i = 1; i < scnt; i += 2, p += 2, v += 24) { p[0] = v; p[1] = v + 12; }
    }
  } else {
    VMWAIT0(); SB();
    asm volatile("" : "+v"(cwv[0]), "+v"(cwv[1]), "+v"(cwv[2]), "+v"(cwv[3]), "+v"(cwv[4]), "+v"(cwv[5]));
    char* hp = lds + L_IMG + ((tid - 256) >> 4) * 528 + c16 * 16;
#pragma unroll
    for (int i = 0; i < 3; i++) *(u32x4*)(hp + i * 16 * 528) = hv[i];
    if (w < 6) {
      const int t3 = tid - 256, nl = __umul24(t3, 21846) >> 16;
      if (t3 < 120) *(u16*)(lds + L_NODE + nl * 2 + t3 * 2) = (u16)cwv[0];
    }
  }
  STAMP(1, 3);
  __syncthreads();
  STAMP(1, 4);
  u32x3 rec[3];
  bool general;
  {
    const u32x4 srd_rec = mksrd(ws + WS_R, NE * 12);
    u32 ro[3];
#pragma unroll
    for (int k = 0; k < 3; k++) ro[k] = ((const u32*)(lds + L_CV))[tid + 512 * k];
    mtot = __builtin_amdgcn_readfirstlane(*(const int*)(lds + L_WS));
    general = mtot > CAP;
    const int lim = general ? 0 : mtot;
#pragma unroll
    for (int k = 0; k < 3; k++) LDB("dwordx3", rec[k], tid + 512 * k < lim ? ro[k] : 0x7fffff00u, srd_rec, 0);
  }
  if (general) asm volatile("s_waitcnt vmcnt(0)" :: "v"(rec[0]), "v"(rec[1]), "v"(rec[2]) : "memory");
  SB();
  if (w >= 6) {
    const float c0 = bf2f(cwv[0]) * NLOG2E;
    const u32 d_hi = pk2(c0, 0.f);
    const u32x4 o = {pk2(bf2f(cwv[1]) * NLOG2E, bf2f(cwv[2]) * NLOG2E), pk2(bf2f(cwv[3]) * NLOG2E, bf2f(cwv[4]) * NLOG2E),
                     pk2(c0, c0 - bf2f(d_hi)), 0u};
    *(u32x4*)(lds + L_AT + (tid - 384) * 96 + 80) = o;
    ((float*)(lds + L_WX))[tid - 384] = bf2f(cwv[5]) * NLN2;
  }
  const char* const xb = lds + L_IMG + c16 * 528 + 16 * gq;
  if (w < 4) {
    f32x4 acc[3][2];
#pragma unroll
    for (int rt = 0; rt < 3; rt++) acc[rt][0] = acc[rt][1] = (f32x4)(0.f);
    const char* wb = lds + L_STG + (8 * gq + (c16 >> 2)) * 256 + 16 * ((4 * w + (c16 & 3)) ^ (c16 & 12));
    u32x4 fa[4], fb[4], hf[4][3];
#pragma unroll
    for (int s = 0; s < 4; s++) {
      const u32x2 x0 = trread(wb + (32 * s) * 256), x1 = trread(wb + (32 * s + 4) * 256);
      const u32x2 y0 = trread(wb + (32 * s) * 256 + 8), y1 = trread(wb + (32 * s + 4) * 256 + 8);
      fa[s] = (u32x4){x0[0], x0[1], x1[0], x1[1]};
      fb[s] = (u32x4){y0[0], y0[1], y1[0], y1[1]};
#pragma unroll
      for (int rt = 0; rt < 3; rt++) hf[s][rt] = *(const u32x4*)(xb + rt * 8448 + 64 * s);
    }
    SB();
#pragma unroll
    for (int s = 0; s < 4; s++)
#pragma unroll
      for (int rt = 0; rt < 3; rt++) {
        acc[rt][0] = MFMA16(asbf(hf[s][rt]), asbf(fa[s]), acc[rt][0]);
        acc[rt][1] = MFMA16(asbf(hf[s][rt]), asbf(fb[s]), acc[rt][1]);
      }
    char* ap = lds + L_AT + (32 * w + 8 * (c16 >> 2) + (c16 & 3)) * 96 + 8 * gq;
#pragma unroll
    for (int rt = 0; rt < 3; rt++)
      if (16 * rt + 4 * gq < 40)
#pragma unroll
        for (int j = 0; j < 2; j++) {
          const f32x4 a = acc[rt][j] * (NLOG2E / 1.0078125f);
          *(u32x2*)(ap + j * 384 + rt * 32) = (u32x2){pk2(a[0], a[1]), pk2(a[2], a[3])};
        }
  }
  f32x16 Y[4], XA, XB, e;
  u32x4 sp0 = (u32x4)(0u), sp1 = (u32x4)(0u), pa = (u32x4)(0u), pb = (u32x4)(0u);
  asm volatile("" : "+v"(sp0), "+v"(sp1), "+v"(pa), "+v"(pb));
  Y[0] = MFMA32(asbf(pa), asbf(pa), (f32x16)(0.f));
  Y[1] = MFMA32(asbf(pb), asbf(pb), (f32x16)(0.f));
  Y[2] = MFMA32(asbf(sp0), asbf(sp0), (f32x16)(0.f));
  Y[3] = MFMA32(asbf(sp1), asbf(sp1), (f32x16)(0.f));
  XB = MFMA32(asbf(pa), asbf(pb), (f32x16)(0.f));
  asm volatile("" : "+v"(Y[0]), "+v"(Y[1]), "+v"(Y[2]), "+v"(Y[3]), "+v"(XB));
  SB();
  STAMP(1, 28);
  u32x4 wex[4];
  u32 ad[4];
  int ninc;
  u32x4 srd = srd_g;
  u32 sfb = 128;
  auto prep = [&](u32x2 sv, bool real) {
    if (real) {
      asm("v_mad_u32_u16 %0, %1, %2, %3" : "=v"(ad[0]) : "v"(sv[0]), "s"(256), "v"(go[0]));
      asm("v_mad_u32_u16 %0, %1, %2, %3 op_sel:[1,0,0,0]" : "=v"(ad[1]) : "v"(sv[0]), "s"(256), "v"(go[1]));
      asm("v_mad_u32_u16 %0, %1, %2, %3" : "=v"(ad[2]) : "v"(sv[1]), "s"(256), "v"(go[2]));
      asm("v_mad_u32_u16 %0, %1, %2, %3 op_sel:[1,0,0,0]" : "=v"(ad[3]) : "v"(sv[1]), "s"(256), "v"(go[3]));
    } else {
#pragma unroll
      for (int j = 0; j < 4; j++) ad[j] = vsrc + 7168 * j;
      srd = mksrd(wh, 65536);
      sfb = 32768;
    }
  };
  auto issue_a = [&]() { SETM0(m0a); DMA1(0, ad[0], srd, 0); DMA1(1024, ad[1], srd, 0); DMA1(2048, ad[2], srd, 0); DMA1(3072, ad[3], srd, 0); };
  auto issue_b = [&]() { SETM0(m0b); DMA1(0, ad[0], srd, sfb); DMA1(1024, ad[1], srd, sfb); DMA1(2048, ad[2], srd, sfb); DMA1(3072, ad[3], srd, sfb); };
  auto node_at = [&](int x) { return (int)__builtin_popcountll(__builtin_amdgcn_ballot_w64(ninc <= x)); };
  auto loadw = [&](int base) {
    const int g0 = base >> 3, bm = base & 7;
    const char* pa = lds + L_AT + n32 * 96 + (hh ? g0 * 16 : 80);
    const char* pb = lds + L_AT + n32 * 96 + (hh ? g0 * 16 + 16 : 80);
    const unsigned long long lo = ~0ull << (16 * min(bm, 3)) << (bm > 3 ? 16 : 0), hi = bm > 4 ? ~0ull << (16 * (bm - 4)) : ~0ull;
    const u32 mk[4] = {(u32)lo, (u32)(lo >> 32), (u32)hi, (u32)(hi >> 32)};
#pragma unroll
    for (int t = 0; t < 4; t++) {
      const u32x4 a = *(const u32x4*)(pa + t * 3072), bb = *(const u32x4*)(pb + t * 3072);
#pragma unroll
      for (int i = 0; i < 4; i++) wex[t][i] = (a[i] & mk[i]) | (bb[i] & ~mk[i]);
    }
  };
  auto pdot = [&](auto nbc) {
    constexpr int nb = decltype(nbc)::value;
    f32x4 p4[2] = {(f32x4)(0.f), (f32x4)(0.f)};
#pragma unroll
    for (int q0 = 0; q0 < 16; q0 += nb) {
      f32x4 wv[16];
#pragma unroll
      for (int q = q0; q < q0 + nb; q++) wv[q] = *(const f32x4*)(lds + L_WX + (8 * q + 4 * hh) * 4);
      SB();
#pragma unroll
      for (int q = q0; q < q0 + nb; q++)
        p4[q & 1] += (f32x4){Y[q >> 2][4 * (q & 3)], Y[q >> 2][4 * (q & 3) + 1], Y[q >> 2][4 * (q & 3) + 2], Y[q >> 2][4 * (q & 3) + 3]} * wv[q];
      SB();
    }
    p4[0] += p4[1];
    const f32x2 pa = (f32x2){p4[0][0], p4[0][1]} + (f32x2){p4[0][2], p4[0][3]};
    const u32 ph = __float_as_uint(pa[0] + pa[1]);
    const auto sw = __builtin_amdgcn_permlane32_swap(ph, ph, false, false);
    return __uint_as_float(sw[0]) + __uint_as_float(sw[1]);
  };
  auto place = [&](u32x3 rec, u32x2 ps, u32x2 pd, u32 dl, u32 p) {
    const u32 sh = (rec[2] & 1) * 16;
    const u32 sxy = __builtin_amdgcn_alignbit(ps[1], ps[0], sh), sz = ps[1] >> sh;
    const u32 dxy = pk2(bf2f(pd[0]) - bf2f(sxy), bfhi(pd[0]) - bfhi(sxy));
    const u32 dz1 = pk2(bf2f(pd[1]) - bf2f(sz), 1.0f);
    const float fx = bf2f(dxy), fy = bfhi(dxy), fz = bf2f(dz1);
    const float d2 = fx * fx + fy * fy + fz * fz;
    *(u32x4*)(lds + L_REC + p * 16) = (u32x4){rec[0], rec[1], pk2(d2, d2), dl};
    *(u32x2*)(lds + L_CV + p * 8) = (u32x2){mul16x2(dxy, OHINV * 0x10001u), mul16x2(dz1, OHINV * 0x10001u)};
    *(u16*)(lds + L_OH + (dl & 7) * OHS + 2 * p) = (u16)OH;
  };
  auto count_nodes = [&](int m) {
    const int cn = ((const u32*)(lds + L_CNT))[lane];
    ninc = wave_incl_scan(cn);
    if (w == 7) {
      const int p = m + n32 < ((m + 31) & ~31) ? m + n32 : CAP;
      *(u32x4*)(lds + L_REC + p * 16) = (u32x4)(0u);
      ((u16*)(lds + L_SRC))[p] = 0;
    }
    return ninc - cn;
  };
#define RDG(s, half) (*(const u32x4*)(lds + ga[s] + (half) * 4096))
#define EXP_(lo, hi) _Pragma("unroll") for (int r = lo; r < hi; r++) e[r] = __builtin_amdgcn_exp2f(xc[r]); SB()
#define RCP_(lo, hi) _Pragma("unroll") for (int r = lo; r < hi; r++) e[r] = __builtin_amdgcn_rcpf(e[r]); SB()
#define PACK_() _Pragma("unroll") for (int i = 0; i < 4; i++) { pa[i] = pk2(e[2 * i], e[2 * i + 1]); pb[i] = pk2(e[8 + 2 * i], e[8 + 2 * i + 1]); } \
  asm volatile("" : "+v"(pa), "+v"(pb)); SB()
#define SPROD(o, ca, cb) (u32x4){mul16x2(o[0], ca[0]), mul16x2(o[1], ca[1]), mul16x2(o[2], cb[0]), mul16x2(o[3], cb[1])}

  const int m = mtot;
  const bool third = w * 64 + 1024 < m;
  u32 rk[3], dl[3];
  u32x2 ps[3], pd[3];
  if (!general) {
    VMWAIT0(); SB();
    STAMP(1, 27);
#pragma unroll
    for (int k = 0; k < 3; k++)
      if (k < 2 || third) {
        LDB("dwordx2", ps[k], ((rec[k][2] & 0xffffu) * 6) & ~3u, srd_pos, 0);
        dl[k] = tid + 512 * k < m ? (rec[k][2] >> 16) - n0 : 48u + c16;
        rk[k] = atomicAdd((u32*)(lds + L_CNT) + dl[k], 1u);
        pd[k] = *(const u32x2*)(lds + L_NODE + dl[k] * 8);
      }
    STAMP(1, 5);
    __syncthreads();
    STAMP(1, 6);
    const int nst = count_nodes(m);
    const int nprev = __builtin_amdgcn_ds_bpermute(lane * 4 - 28, ninc);
#pragma unroll
    for (int k = 0; k < 3; k++)
      if (k < 2 || third) {
        rk[k] += __builtin_amdgcn_ds_bpermute(dl[k] * 4, nst);
        rk[k] = dl[k] < 48 ? rk[k] : CAP;
        *(u16*)(lds + L_SRC + 2 * rk[k]) = (u16)rec[k][2];
      }
    general = m < 256 || __builtin_amdgcn_ballot_w64(lane >= 7 && lane < nn && ninc - nprev <= 30) != 0;
    if (general) asm volatile("s_waitcnt vmcnt(0)" :: "v"(ps[0]), "v"(ps[1]), "v"(ps[2]) : "memory");
  }
  if (!general) {
    {
      const int nt = (m + 31) >> 5, tq = nt >> 3, trm = nt & 7;
      const int tbeg = w * tq + min(w, trm);
      const int tend = tbeg + tq + (w < trm ? 1 : 0);
      srcoff += tbeg * 64;
      __syncthreads();
      STAMP(1, 29);
      const u32x2 sv0 = *(const u32x2*)(lds + srcoff), sv1 = *(const u32x2*)(lds + srcoff + 64);
      prep(sv0, true);
      if (w < 4) {
        issue_a();
        issue_b();
        VMWAIT(8);
      } else {
        VMWAIT(0);
      }
      SB();
      STAMP(1, 26);
      int base = node_at(tbeg * 32);
      loadw(base);
      const int lnode = node_at(min(tend * 32, m) - 1);
      const int hnode = (tbeg > 0 && node_at(tbeg * 32 - 1) == base) ? base : -1;
      if (hnode >= 0 && lane == 0) __hip_atomic_fetch_or((u32*)(lds + L_HM) + hnode, 1u << w, __ATOMIC_RELAXED, __HIP_MEMORY_SCOPE_WORKGROUP);
      recoff += tbeg * 512; cvoff += tbeg * 256 - 256; ohoff += tbeg * 64 - 64; axoff += tbeg * 64;
#pragma unroll
      for (int k = 0; k < 3; k++)
        if (k < 2 || third) place(rec[k], ps[k], pd[k], dl[k], rk[k]);
      if (w >= 4) {
        issue_a();
        issue_b();
      }
      prep(sv1, tbeg + 1 < tend);
#pragma unroll
      for (int t = 0; t < 4; t++) asm volatile("" : "+v"(wex[t]));
      auto flush = [&](int base, int lim, auto clearc) {
        const int node = base + (((n32 >> 2) - base) & 7), comp = n32 & 3;
        constexpr bool clear = decltype(clearc)::value != 0;
        const float pt = pdot(IC<(clear ? 2 : 16)>{});
        if (node < lim) {
          char* mp = lds + L_IMG + (node == hnode ? 39 + w : node) * 528;
          if (comp == 3) {
#pragma unroll
            for (int q = 0; q < 16; q++)
              *(u32x2*)(mp + 256 + 8 * hh + 16 * q) = (u32x2){pk2(Y[q >> 2][4 * (q & 3)], Y[q >> 2][4 * (q & 3) + 1]),
                                                              pk2(Y[q >> 2][4 * (q & 3) + 2], Y[q >> 2][4 * (q & 3) + 3])};
          } else {
            *(float*)(mp + 512 + 4 * comp) = pt;
          }
        }
        if (clear) {
          const float keep = node < lim ? 0.f : 1.f;
#pragma unroll
          for (int t = 0; t < 4; t++) Y[t] *= keep;
        }
      };
      STAMP(1, 7);
      __syncthreads();
      STAMP(1, 8);
      u32x4 aex;
      {
        const u32x2 t0 = trread(lds + axoff), t1 = trread(lds + axoff + 4 * OHS);
        const u32x4 ra = *(const u32x4*)(lds + recoff);
        aex = hh ? (u32x4){t0[0], t0[1], t1[0], t1[1]} : ra;
      }
      XA = MFMA32(asbf(aex), asbf(wex[0]), (f32x16)(0.f)); PIN(XA);
      VMWAIT(4);
      STAMP(1, 23);
      u32x4 g0 = RDG(0, 0), g1 = RDG(1, 0), g2 = RDG(2, 0), g3 = RDG(3, 0);
      LGKMWAIT0();
      SB();
      XA = MFMA32(asbf(g0), asbf(id0), XA); PIN(XA);
      issue_a();
      XA = MFMA32(asbf(g1), asbf(id1), XA); PIN(XA);
      int fbase = base;
      __builtin_amdgcn_s_setprio(3);
      STAMP(1, 9);
      int tcount_ = 0;
#define EXPX(X, lo, hi) _Pragma("unroll") for (int r = lo; r < hi; r++) e[r] = __builtin_amdgcn_exp2f(X[r]); SB()
#define PACKX(X) _Pragma("unroll") for (int i = 0; i < 4; i++) { pa[i] = pk2(X[2 * i], X[2 * i + 1]); pb[i] = pk2(X[8 + 2 * i], X[8 + 2 * i + 1]); } \
  asm volatile("" : "+v"(pa), "+v"(pb)); SB()
#define SEG(t, p, sp) Y[t] = MFMA32(asbf(p), asbf(sp), Y[t]); PIN(Y[t])
#define PLUS1() e = MFMA32(asbf(one4), asbf(one4), e); PIN(e)
      for (int left = tend - tbeg, x = tbeg * 32 + 32; left > 0; left--, x += 32) {
        STAMPV(1, 16 + min(tcount_, 4)); tcount_++;
        if (__builtin_expect(left == 1, 0)) __builtin_amdgcn_s_setprio(1);
        SB();
        SEG(2, pa, sp0);
        EXPX(XA, 0, 8);
        SEG(2, pb, sp1);
        EXPX(XA, 8, 16);
        PLUS1();
        PACKX(XB);
        srcoff += 64; recoff += 512; cvoff += 256; ohoff += 64; axoff += 64;
        asm volatile("" : "+v"(srcoff), "+v"(recoff), "+v"(cvoff), "+v"(ohoff), "+v"(axoff)); SB();
        RCP_(0, 4);
        XB = MFMA32(asbf(aex), asbf(wex[1]), (f32x16)(0.f)); PIN(XB);
        RCP_(4, 8);
        XB = MFMA32(asbf(g2), asbf(id0), XB); PIN(XB);
        RCP_(8, 12);
        XB = MFMA32(asbf(g3), asbf(id1), XB); PIN(XB);
        RCP_(12, 16);
        XA = XA * e; PIN(XA);
        SEG(3, pa, sp0);
        VMWAIT(4);
        g0 = RDG(0, 1); g1 = RDG(1, 1); g2 = RDG(2, 1); g3 = RDG(3, 1);
        const u32x2 sv = *(const u32x2*)(lds + srcoff + 64);
        SB();
        EXPX(XB, 0, 8);
        SEG(3, pb, sp1);
        const u32x2 c0 = trread(lds + cvoff), c1 = trread(lds + cvoff + 32), c2 = trread(lds + cvoff + 128), c3 = trread(lds + cvoff + 160);
        const u32x4 o0 = *(const u32x4*)(lds + ohoff), o1 = *(const u32x4*)(lds + ohoff + 32);
        SB();
        EXPX(XB, 8, 16);
        PLUS1();
        PACKX(XA);
        sp0 = SPROD(o0, c0, c1);
        sp1 = SPROD(o1, c2, c3);
        asm volatile("" : "+v"(sp0), "+v"(sp1));
        LGKMWAIT0();
        SB();
        SETM0(m0b); DMA1(0, ad[0], srd, sfb); SB();
        RCP_(0, 4);
        XA = MFMA32(asbf(aex), asbf(wex[2]), (f32x16)(0.f)); PIN(XA);
        DMA1(1024, ad[1], srd, sfb); SB();
        RCP_(4, 8);
        XA = MFMA32(asbf(g0), asbf(id0), XA); PIN(XA);
        DMA1(2048, ad[2], srd, sfb); SB();
        RCP_(8, 12);
        XA = MFMA32(asbf(g1), asbf(id1), XA); PIN(XA);
        DMA1(3072, ad[3], srd, sfb); SB();
        RCP_(12, 16);
        XB = XB * e; PIN(XB);
        if (__builtin_expect(fbase != base, 0)) {
          flush(fbase, base, IC<1>{});
          fbase = base;
        }
        if (__builtin_expect(left <= 2, 0)) {
          if (left == 2) __builtin_amdgcn_s_setprio(2);
          else __builtin_amdgcn_s_setprio(0);
          prep(sv, false);
        } else {
          prep(sv, true);
        }
        asm volatile("" : "+v"(ad[0]), "+v"(ad[1]), "+v"(ad[2]), "+v"(ad[3]));
        SB();
        SEG(0, pa, sp0);
        const int nlast = node_at(min(x + 31, m - 1));
        SB();
        EXPX(XA, 0, 8);
        SEG(0, pb, sp1);
        EXPX(XA, 8, 16);
        PLUS1();
        PACKX(XB);
        RCP_(0, 4);
        XB = MFMA32(asbf(aex), asbf(wex[3]), (f32x16)(0.f)); PIN(XB);
        RCP_(4, 8);
        XB = MFMA32(asbf(g2), asbf(id0), XB); PIN(XB);
        RCP_(8, 12);
        XB = MFMA32(asbf(g3), asbf(id1), XB); PIN(XB);
        RCP_(12, 16);
        XA = XA * e; PIN(XA);
        if (__builtin_expect(left > 1 && nlast >= base + 8, 0)) {
          base = node_at(x);
          loadw(base);
        }
        SB();
        SEG(1, pa, sp0);
        VMWAIT(4);
        const u32x2 t0 = trread(lds + axoff), t1 = trread(lds + axoff + 4 * OHS);
        const u32x4 ra = *(const u32x4*)(lds + recoff);
        g0 = RDG(0, 0); g1 = RDG(1, 0); g2 = RDG(2, 0); g3 = RDG(3, 0);
        SB();
        EXPX(XB, 0, 8);
        SEG(1, pb, sp1);
        EXPX(XB, 8, 16);
        PLUS1();
        PACKX(XA);
        LGKMWAIT0();
        SB();
        aex = hh ? (u32x4){t0[0], t0[1], t1[0], t1[1]} : ra;
        PIN(aex);
        SETM0(m0a); DMA1(0, ad[0], srd, 0); SB();
        RCP_(0, 4);
        XA = MFMA32(asbf(aex), asbf(wex[0]), (f32x16)(0.f)); PIN(XA);
        DMA1(1024, ad[1], srd, 0); SB();
        RCP_(4, 8);
        XA = MFMA32(asbf(g0), asbf(id0), XA); PIN(XA);
        DMA1(2048, ad[2], srd, 0); SB();
        RCP_(8, 12);
        XA = MFMA32(asbf(g1), asbf(id1), XA); PIN(XA);
        DMA1(3072, ad[3], srd, 0); SB();
        RCP_(12, 16);
        XB = XB * e; PIN(XB);
      }
      SEG(2, pa, sp0);
      SEG(2, pb, sp1);
      PACKX(XB);
      SEG(3, pa, sp0);
      SEG(3, pb, sp1);
      STAMP(1, 10);
      flush(base, lnode + 1, IC<0>{});
      STAMP(1, 11);
    }
  } else {
    tid = threadIdx.x;
    asm volatile("" : "+v"(tid));
    lane = tid & 63; gq = lane >> 4; c16 = lane & 15; hh = lane >> 5; n32 = lane & 31;
    lane_consts();
    t4 = *(const u32x4*)(ws + WS_T + b * 1024 + lane * 16);
    decode();
    const rsrc_t rs_rec = mkrs(ws + WS_R, NE * 12), rs_pos = mkrs(pos, NN * 6);
    for (int q = tid; q < 40 * 33; q += 512) *(u32x4*)(lds + L_MG + q * 16) = (u32x4)(0u);
    for (int r0 = 0; r0 < mtot; r0 += CAP) {
      const int m = min(mtot - r0, CAP);
      __syncthreads();
      for (int q = tid; q < (L_HM - L_OH) / 16; q += 512) *(u32x4*)(lds + L_OH + q * 16) = (u32x4)(0u);
      const int lim = min(scnt, r0 + CAP - sexc);
      for (int i = max(r0 - sexc, 0) + (w >> 2); i < lim; i += 2)
        *(u32x3*)(lds + L_STG + (sexc + i - r0) * 16) = __builtin_amdgcn_raw_buffer_load_b96(rs_rec, sbase + i * 12, 0, 0);
      __syncthreads();
      for (int f = tid; f < m; f += 512) {
        u32* rp = (u32*)(lds + L_STG + f * 16);
        rp[3] = atomicAdd((u32*)(lds + L_CNT) + ((rp[2] >> 16) - n0), 1u);
      }
      __syncthreads();
      const int nst = count_nodes(m);
      for (int f0 = w * 64; f0 < m; f0 += 512) {
        const bool ok = f0 + lane < m;
        const u32x4 rc = *(const u32x4*)(lds + L_STG + (f0 + lane) * 16);
        const u32 dl = ok ? (rc[2] >> 16) - n0 : 0u;
        const u32 pp = rc[3] + __builtin_amdgcn_ds_bpermute(dl * 4, nst), p = ok ? pp : CAP;
        const u32x2 ps = __builtin_amdgcn_raw_buffer_load_b64(rs_pos, ((rc[2] & 0xffffu) * 6) & ~3u, 0, 0);
        *(u16*)(lds + L_SRC + 2 * p) = (u16)rc[2];
        place((u32x3){rc[0], rc[1], rc[2]}, ps, *(const u32x2*)(lds + L_NODE + dl * 8), dl, p);
      }
      __syncthreads();
      const int nt = w < 4 ? (m + 31) >> 5 : 0;
      for (int tile = w; tile < nt; tile += 4) {
        const int last = node_at(min(tile * 32 + 31, m - 1));
        prep(*(const u32x2*)(lds + srcoff + tile * 64), true);
        SETM0(m0a); DMA1(0, ad[0], srd_g, 0); DMA1(1024, ad[1], srd_g, 0); DMA1(2048, ad[2], srd_g, 0); DMA1(3072, ad[3], srd_g, 0);
        SETM0(m0b); DMA1(0, ad[0], srd_g, 128); DMA1(1024, ad[1], srd_g, 128); DMA1(2048, ad[2], srd_g, 128); DMA1(3072, ad[3], srd_g, 128);
        const u32x2 t0 = trread(lds + axoff + tile * 64), t1 = trread(lds + axoff + tile * 64 + 4 * OHS);
        const u32x4 ra = *(const u32x4*)(lds + recoff + tile * 512);
        const u32x4 aex = hh ? (u32x4){t0[0], t0[1], t1[0], t1[1]} : ra;
        for (int base = node_at(tile * 32); base <= last; base += 8) {
          loadw(base);
          u32x4 sp[2];
#pragma unroll
          for (int s = 0; s < 2; s++) {
            const u32x2 c0 = trread(lds + cvoff + tile * 256 + s * 128), c1 = trread(lds + cvoff + tile * 256 + s * 128 + 32);
            const u32x4 oh = *(const u32x4*)(lds + ohoff + tile * 64 + 32 * s);
            sp[s] = SPROD(oh, c0, c1);
          }
#pragma unroll 1
          for (int j = 0; j < 16; j++) {
            const int dn = *(const u32*)(lds + L_REC + (tile * 32 + 16 * (j >> 3) + 8 * hh + (j & 7)) * 16 + 12);
            const u32 mk = (dn < base || dn >= base + 8) ? ((j & 1) ? 0x0000ffffu : 0xffff0000u) : 0xffffffffu;
#pragma unroll
            for (int i = 0; i < 8; i++) sp[i >> 2][i & 3] &= (i == (j >> 1)) ? mk : 0xffffffffu;
          }
          VMWAIT0();
#pragma unroll
          for (int t = 0; t < 4; t++) {
            const u32x4 ga_ = RDG((2 * t) & 3, t >> 1), gb_ = RDG((2 * t + 1) & 3, t >> 1);
            f32x16 xc = MFMA32(asbf(aex), asbf(wex[t]), (f32x16)(0.f)), e;
            xc = MFMA32(asbf(ga_), asbf(id0), xc);
            xc = MFMA32(asbf(gb_), asbf(id1), xc);
            u32x4 pa, pb;
            EXP_(0, 16);
            e = e + 1.0f;
            RCP_(0, 16);
            e = e * xc;
            PACK_();
            Y[t] = MFMA32(asbf(pa), asbf(sp[0]), (f32x16)(0.f));
            Y[t] = MFMA32(asbf(pb), asbf(sp[1]), Y[t]);
          }
          const int node = base + (((n32 >> 2) - base) & 7), comp = n32 & 3;
          const float pt = pdot(IC<4>{});
          if (node <= last) {
            float* mp = (float*)(lds + L_MG) + node * 132;
            if (comp == 3) {
#pragma unroll
              for (int t = 0; t < 4; t++)
#pragma unroll
                for (int r = 0; r < 16; r++) atomicAdd(mp + 4 * hh + 32 * t + 8 * (r >> 2) + (r & 3), Y[t][r]);
            } else if (hh == 0) {
              atomicAdd(mp + 128 + comp, pt);
            }
          }
        }
        LGKMWAIT0();
      }
    }
    __syncthreads();
    for (int q = tid; q < 40 * 66; q += 512) {
      const int row = q / 66, c = q - row * 66;
      const f32x2 v = *(const f32x2*)(lds + L_MG + row * 528 + c * 8);
      if (c < 64) *(u32*)(lds + L_IMG + row * 528 + 256 + c * 4) = pk2(v[0], v[1]);
      else *(f32x2*)(lds + L_IMG + row * 528 + c * 8) = v;
    }
    __syncthreads();
    const u32x4 srd_wh = mksrd(wh, 65536);
    SETM0(m0a); DMA1(0, vsrc, srd_wh, 0); DMA1(1024, vsrc, srd_wh, 7168); DMA1(2048, vsrc, srd_wh, 14336); DMA1(3072, vsrc, srd_wh, 21504);
    SETM0(m0b); DMA1(0, vsrc, srd_wh, 32768); DMA1(1024, vsrc, srd_wh, 39936); DMA1(2048, vsrc, srd_wh, 47104); DMA1(3072, vsrc, srd_wh, 54272);
    VMWAIT0();
  }

  int tid2 = threadIdx.x;
  asm volatile("" : "+v"(tid2));
  const int c16e = tid2 & 15, gqe = (tid2 >> 4) & 3;
  const char* const xbe = lds + L_IMG + c16e * 528 + 16 * gqe;
  f32x4 acc[3], acm[3];
  const char* const wfp = lds + L_STG + w * 8192 + (8 * gqe + (c16e >> 2)) * 32 + 8 * (c16e & 3);
  VMWAIT(4);
  u32x4 wf2[4];
#pragma unroll
  for (int rt = 0; rt < 3; rt++) acc[rt] = acm[rt] = (f32x4)(0.f);
  {
    u32x4 wf1[4], xf[4][3];
#pragma unroll
    for (int s = 0; s < 4; s++) {
      const u32x2 x0 = trread(wfp + s * 1024), x1 = trread(wfp + s * 1024 + 128);
      const u32x2 y0 = trread(wfp + 4096 + s * 1024), y1 = trread(wfp + 4096 + s * 1024 + 128);
      wf1[s] = (u32x4){x0[0], x0[1], x1[0], x1[1]};
      wf2[s] = (u32x4){y0[0], y0[1], y1[0], y1[1]};
#pragma unroll
      for (int rt = 0; rt < 3; rt++) xf[s][rt] = *(const u32x4*)(xbe + rt * 8448 + 64 * s);
    }
    SB();
#pragma unroll
    for (int s = 0; s < 4; s++)
#pragma unroll
      for (int rt = 0; rt < 3; rt++) acc[rt] = MFMA16(asbf(wf1[s]), asbf(xf[s][rt]), acc[rt]);
  }
  u32x2 hr[3];
  u32 hm[3], hl[3];
#pragma unroll
  for (int rt = 0; rt < 3; rt++) {
    hr[rt] = *(const u32x2*)(lds + L_IMG + c16e * 528 + 8 * gqe + rt * 8448 + 32 * w);
    hm[rt] = 16 * rt + c16e < 40 ? ((const u32*)(lds + L_HM))[16 * rt + c16e] : 0u;
    hl[rt] = 64 * gqe + 28 + 4 * __builtin_ctz(hm[rt] | 256u);
    hm[rt] &= hm[rt] - 1;
  }
  STAMP(1, 12);
  __syncthreads();
  STAMP(1, 13);
  {
    u32x4 xf[3][4];
#pragma unroll
    for (int s = 0; s < 4; s++)
#pragma unroll
      for (int rt = 2; rt >= 0; rt--) xf[rt][s] = *(const u32x4*)(xbe + 256 + rt * 8448 + 64 * s);
    SB();
#pragma unroll
    for (int s = 0; s < 4; s++)
#pragma unroll
      for (int rt = 2; rt >= 0; rt--) acm[rt] = MFMA16(asbf(wf2[s]), asbf(xf[rt][s]), acm[rt]);
  }
  const f32x4 hd = acm[2];
  for (;;) {
#pragma unroll
    for (int rt = 0; rt < 3; rt++)
#pragma unroll
      for (int r = 0; r < 4; r++) {
        const float hv_ = hd[r];
        acm[rt][r] += __uint_as_float(__builtin_amdgcn_ds_bpermute(hl[rt], __float_as_uint(hv_)));
      }
    if (__builtin_expect(__builtin_amdgcn_ballot_w64((hm[0] | hm[1] | hm[2]) != 0) == 0, 1)) break;
#pragma unroll
    for (int rt = 0; rt < 3; rt++) {
      hl[rt] = 64 * gqe + 28 + 4 * __builtin_ctz(hm[rt] | 256u);
      hm[rt] &= hm[rt] - 1;
    }
  }
  STAMP(1, 14);
  {
    const rsrc_t rs_o = mkrs(out + (size_t)n0 * 128, nn * 256);
#pragma unroll
    for (int rt = 0; rt < 3; rt++) {
      u32 o[2];
#pragma unroll
      for (int r = 0; r < 2; r++) {
        const f32x2 z = (f32x2){acc[rt][2 * r], acc[rt][2 * r + 1]} + (f32x2){acm[rt][2 * r], acm[rt][2 * r + 1]} * NLN2;
        const f32x2 t = z * NLOG2E;
        const f32x2 d = (f32x2){__builtin_amdgcn_exp2f(t[0]), __builtin_amdgcn_exp2f(t[1])} + 1.0f;
        const f32x2 v = z * (f32x2){__builtin_amdgcn_rcpf(d[0]), __builtin_amdgcn_rcpf(d[1])} + (f32x2){bf2f(hr[rt][r]), bfhi(hr[rt][r])};
        o[r] = pk2(v[0], v[1]);
      }
      __builtin_amdgcn_raw_buffer_store_b64((u32x2){o[0], o[1]}, rs_o, c16e * 256 + 8 * gqe, rt * 4096 + 32 * w, 0);
    }
  }
  if (w < 2) {
    const int nl = __umul24(tid2, 21846) >> 16, k = tid2 - 3 * nl;
    if (tid2 < 3 * nn) {
      const char* mp = lds + L_IMG + 512 + 4 * k;
      u32 hq = ((const u32*)(lds + L_HM))[nl];
      float pv = *(const float*)(mp + nl * 528) + bf2f(*(const u16*)(lds + L_NODE + nl * 2 + tid2 * 2));
      while (hq) {
        pv += *(const float*)(mp + (39 + __builtin_ctz(hq)) * 528);
        hq &= hq - 1;
      }
      out[(size_t)NN * 128 + n0 * 3 + tid2] = (u16)pk2(pv, 0.f);
    }
  }
  VMWAIT0();
  STAMP(1, 15); RSTAMP(1, 31);
}

extern "C" void kernel_launch(void* const* d_in, const int* in_sizes, int n_in, void* d_out, int out_size,
                              void* d_ws, size_t ws_size, hipStream_t stream) {
  const u16* h = (const u16*)d_in[0];
  const u16* pos = (const u16*)d_in[1];
  const u16* ea = (const u16*)d_in[2];
  const int* src = (const int*)d_in[3];
  const int* dst = (const int*)d_in[4];
  const u16* we = (const u16*)d_in[5];
  const u16* wx = (const u16*)d_in[6];
  const u16* wh = (const u16*)d_in[7];
  egnn_k1<<<NB, 512, 0, stream>>>(h, ea, src, dst, we, (char*)d_ws);
  egnn_k2<<<NB, 512, 0, stream>>>(h, pos, we, wx, wh, (const char*)d_ws, (u16*)d_out);
}
```

```cpp
#include <hip/hip_runtime.h>
#include <hip/hip_bf16.h>

typedef unsigned short u16;
typedef unsigned int u32;
typedef float f32x2 __attribute__((ext_vector_type(2)));
typedef float f32x4 __attribute__((ext_vector_type(4)));
typedef float f32x16 __attribute__((ext_vector_type(16)));
typedef __bf16 bf16x8 __attribute__((ext_vector_type(8)));
typedef __bf16 bf16x2 __attribute__((ext_vector_type(2)));
typedef u32 u32x2 __attribute__((ext_vector_type(2)));
typedef u32 u32x3 __attribute__((ext_vector_type(3)));
typedef u32 u32x4 __attribute__((ext_vector_type(4)));
typedef short s16x4 __attribute__((ext_vector_type(4)));
typedef u16 u16x2 __attribute__((ext_vector_type(2)));
#define LAS __attribute__((address_space(3)))
typedef __amdgpu_buffer_rsrc_t rsrc_t;

constexpr int NN = 10000;
constexpr int NE = 320000;
constexpr int NCH = 250;
constexpr int CH = 1280;
constexpr int NB = 256;
constexpr int CAP = 1408;
constexpr float NLOG2E = -1.4426950408889634f;
constexpr float NLN2 = -0.6931471805599453f;
constexpr u32 OH = 0x3F81u;
constexpr u32 OHINV = 0x81u;

constexpr size_t WS_T = 0;
constexpr size_t WS_R = WS_T + (size_t)NB * 256 * 4;
constexpr size_t WS_B = WS_R + (size_t)NE * 12;

__device__ __forceinline__ float bf2f(u32 v) { return __uint_as_float(v << 16); }
__device__ __forceinline__ float bfhi(u32 v) { return __uint_as_float(v & 0xffff0000u); }
__device__ __forceinline__ u32 pk2(float a, float b) {
  f32x2 v = {a, b};
  return __builtin_bit_cast(u32, __builtin_convertvector(v, bf16x2));
}
__device__ __forceinline__ u32 mul16x2(u32 a, u32 b) {
  return __builtin_bit_cast(u32, __builtin_bit_cast(u16x2, a) * __builtin_bit_cast(u16x2, b));
}
__device__ __forceinline__ int wave_incl_scan(int x) {
  x += __builtin_amdgcn_update_dpp(0, x, 0x111, 0xf, 0xf, false);
  x += __builtin_amdgcn_update_dpp(0, x, 0x112, 0xf, 0xf, false);
  x += __builtin_amdgcn_update_dpp(0, x, 0x114, 0xf, 0xf, false);
  x += __builtin_amdgcn_update_dpp(0, x, 0x118, 0xf, 0xf, false);
  x += __builtin_amdgcn_update_dpp(0, x, 0x142, 0xa, 0xf, false);
  x += __builtin_amdgcn_update_dpp(0, x, 0x143, 0xc, 0xf, false);
  return x;
}
__device__ __forceinline__ bf16x8 asbf(u32x4 v) { return __builtin_bit_cast(bf16x8, v); }
__device__ __forceinline__ u32x2 trread(const char* p) {
  return __builtin_bit_cast(u32x2, __builtin_amdgcn_ds_read_tr16_b64_v4i16((LAS s16x4*)p));
}
__device__ __forceinline__ rsrc_t mkrs(const void* p, u32 bytes) { return __builtin_amdgcn_make_buffer_rsrc((void*)p, 0, bytes, 0x00020000); }
#define MFMA32(a, b, c) __builtin_amdgcn_mfma_f32_32x32x16_bf16(a, b, c, 0, 0, 0)
#define MFMA16(a, b, c) __builtin_amdgcn_mfma_f32_16x16x32_bf16(a, b, c, 0, 0, 0)
#define SB() __builtin_amdgcn_sched_barrier(0)
#define VMWAIT0() asm volatile("s_waitcnt vmcnt(0)" ::: "memory")

template <int V> struct IC { static constexpr int value = V; };
__device__ __forceinline__ u32x4 mksrd(const void* p, u32 bytes) {
  const unsigned long long a = (unsigned long long)p;
  return (u32x4){(u32)a, (u32)(a >> 32) & 0xffffu, bytes, 0x00020000u};
}
#define DMA1(J, A, SRD, SOFF) asm volatile("buffer_load_dwordx4 %0, %1, %2 offen offset:" #J " lds" :: "v"(A), "s"(SRD), "s"(SOFF) : "memory")
#define LDB(T, DST, VO, SRD, SO) asm volatile("buffer_load_" T " %0, %1, %2, %3 offen" : "=v"(DST) : "v"(VO), "s"(SRD), "s"(SO) : "memory")
#define SETM0(V) asm volatile("s_mov_b32 m0, %0\n\ts_nop 0" :: "s"(V) : "memory")
#define VMWAIT(N) asm volatile("s_waitcnt vmcnt(" #N ")" ::: "memory")
#define LGKMWAIT0() asm volatile("s_waitcnt lgkmcnt(0)" ::: "memory")
#define PIN(x) do { asm volatile("" : "+v"(x)); SB(); } while (0)

constexpr int K1_H = 32768;
constexpr int K1_IMG = K1_H + 48 * 256;
constexpr int K1_HIST = K1_IMG + 16384;
constexpr int K1_LDS = K1_HIST + 2 * NB * 4 + 32;

extern "C" __global__ __launch_bounds__(512) void egnn_k1(const u16* __restrict__ h, const u16* __restrict__ ea,
                                               const int* __restrict__ src, const int* __restrict__ dst,
                                               const u16* __restrict__ we, char* __restrict__ ws) {
  __shared__ __attribute__((aligned(128))) char lds[K1_LDS];
  const int tid = threadIdx.x, lane = tid & 63;
  const int w = __builtin_amdgcn_readfirstlane(tid >> 6);
  const int c = (blockIdx.x & 7) * 32 + (blockIdx.x >> 3);
  const int gq = lane >> 4, c16 = lane & 15;
  u32* const hist = (u32*)(lds + K1_HIST);
  asm volatile("" :: "s"(h), "s"(ea), "s"(src), "s"(dst), "s"(we), "s"(ws));
  if (tid < NB) hist[tid] = 0;
  else if (tid < NB + 8) hist[NB + tid] = 0;
  __syncthreads();
  auto meet = [&](int k) {
    LGKMWAIT0();
    if (lane == 0) __hip_atomic_fetch_add(&hist[2 * NB + k], 1u, __ATOMIC_RELAXED, __HIP_MEMORY_SCOPE_WORKGROUP);
    while (__hip_atomic_load(&hist[2 * NB + k], __ATOMIC_RELAXED, __HIP_MEMORY_SCOPE_WORKGROUP) < 4u) __builtin_amdgcn_s_sleep(1);
    asm volatile("" ::: "memory");
  };
  if (w < 4) {
    const u32 eo = tid * 20, nb = c < NCH ? CH * 4 : 0;
    const rsrc_t rs_d = mkrs(dst + c * CH, nb), rs_s = mkrs(src + c * CH, nb), rs_a = mkrs(ea + (size_t)c * CH * 4, 2 * nb);
    const u32x4 d4 = __builtin_amdgcn_raw_buffer_load_b128(rs_d, eo, 0, 0);
    const u32 d1 = __builtin_amdgcn_raw_buffer_load_b32(rs_d, eo + 16, 0, 0);
    const u32x4 s4 = __builtin_amdgcn_raw_buffer_load_b128(rs_s, eo, 0, 0);
    const u32 s1 = __builtin_amdgcn_raw_buffer_load_b32(rs_s, eo + 16, 0, 0);
    const u32x4 a0 = __builtin_amdgcn_raw_buffer_load_b128(rs_a, 2 * eo, 0, 0);
    const u32x4 a1 = __builtin_amdgcn_raw_buffer_load_b128(rs_a, 2 * eo + 16, 0, 0);
    const u32x2 a2 = __builtin_amdgcn_raw_buffer_load_b64(rs_a, 2 * eo + 32, 0, 0);
    __builtin_amdgcn_s_setprio(1);
    if (c < NCH) {
      u32* const img = (u32*)(lds + K1_IMG);
      u32* const base = hist + NB;
      u32 bk[5], rk[5];
#pragma unroll
      for (int i = 0; i < 5; i++) {
        bk[i] = __umul24(i < 4 ? d4[i & 3] : d1, 214749u) >> 23;
        rk[i] = atomicAdd(&hist[bk[i]], 1u);
      }
      meet(1);
      {
        const u32x4 v = *(const u32x4*)(hist + 4 * lane);
        const int s = v[0] + v[1] + v[2] + v[3];
        const int ex = wave_incl_scan(s) - s;
        const u32x4 bs = {(u32)ex, ex + v[0], ex + v[0] + v[1], ex + v[0] + v[1] + v[2]};
        *(u32x4*)(base + 4 * lane) = bs;
        const u32 bb = w == 0 ? bs[0] : w == 1 ? bs[1] : w == 2 ? bs[2] : bs[3];
        const u32 vv = w == 0 ? v[0] : w == 1 ? v[1] : w == 2 ? v[2] : v[3];
        ((u32*)(ws + WS_T))[(4 * lane + w) * 256 + c] = bb | (vv << 16);
      }
#pragma unroll
      for (int i = 0; i < 5; i++) {
        u32* p = img + 3 * (base[bk[i]] + rk[i]);
        p[0] = i < 2 ? a0[2 * (i & 1)] : i < 4 ? a1[2 * (i & 1)] : a2[0];
        p[1] = i < 2 ? a0[2 * (i & 1) + 1] : i < 4 ? a1[2 * (i & 1) + 1] : a2[1];
        p[2] = (i < 4 ? s4[i & 3] : s1) | ((i < 4 ? d4[i & 3] : d1) << 16);
      }
      meet(2);
      const rsrc_t rs_r = mkrs(ws + WS_R + (size_t)c * (CH * 12), CH * 12);
#pragma unroll
      for (int i = 0; i < 4; i++)
        __builtin_amdgcn_raw_buffer_store_b128(((const u32x4*)img)[tid + 256 * i], rs_r, tid * 16 + i * 4096, 0, 0);
    }
  } else {
    const int u = w - 4, n0 = (c * 625 + 15) >> 4, nn = (((c + 1) * 625 + 15) >> 4) - n0;
    const u32 ldsb = (u32)(size_t)(LAS char*)lds;
    {
      const u32x4 srd_h = mksrd(h + (size_t)n0 * 128, nn * 256), srd_w = mksrd(we + 128 * 128, 32768);
      const u32 vw = gq * 256 + 16 * (c16 ^ (4 * gq));
      SETM0(ldsb + u * 8192);
      DMA1(0, vw, srd_w, u * 8192); DMA1(1024, vw, srd_w, u * 8192); DMA1(2048, vw, srd_w, u * 8192); DMA1(3072, vw, srd_w, u * 8192);
      SETM0(ldsb + u * 8192 + 4096);
      DMA1(0, vw, srd_w, u * 8192 + 4096); DMA1(1024, vw, srd_w, u * 8192 + 4096); DMA1(2048, vw, srd_w, u * 8192 + 4096); DMA1(3072, vw, srd_w, u * 8192 + 4096);
      SETM0(ldsb + K1_H + u * 3072);
      DMA1(0, gq * 256 + 16 * (c16 ^ ((12 * u + gq) & 15)), srd_h, u * 3072);
      DMA1(1024, gq * 256 + 16 * (c16 ^ ((12 * u + 4 + gq) & 15)), srd_h, u * 3072);
      DMA1(2048, gq * 256 + 16 * (c16 ^ ((12 * u + 8 + gq) & 15)), srd_h, u * 3072);
    }
    VMWAIT0();
    u32 pfv = 0;
    const bool pf = blockIdx.x < 64 && u < 3;
    if (pf) {
      unsigned long long pc;
      u32 off;
      asm volatile("s_getpc_b64 %0\n.Lpc%=:\n\ts_mov_b32 %1, egnn_k2-.Lpc%=" : "=s"(pc), "=s"(off));
      asm volatile("global_load_dword %0, %1, %2" : "=v"(pfv) : "v"(min(u * 64 + lane, 147) * 128), "s"(pc + off) : "memory");
    }
    meet(0);
    f32x4 acc[3][2];
#pragma unroll
    for (int rt = 0; rt < 3; rt++) acc[rt][0] = acc[rt][1] = (f32x4)(0.f);
    const char* wb = lds + (8 * gq + (c16 >> 2)) * 256 + 16 * ((4 * u + (c16 & 3)) ^ (c16 & 12));
    const int hb = K1_H + c16 * 256 + 16 * (gq ^ c16);
    u32x4 fa[4], fb[4], hf[4][3];
#pragma unroll
    for (int s = 0; s < 4; s++) {
      const u32x2 x0 = trread(wb + (32 * s) * 256), x1 = trread(wb + (32 * s + 4) * 256);
      const u32x2 y0 = trread(wb + (32 * s) * 256 + 8), y1 = trread(wb + (32 * s + 4) * 256 + 8);
      fa[s] = (u32x4){x0[0], x0[1], x1[0], x1[1]};
      fb[s] = (u32x4){y0[0], y0[1], y1[0], y1[1]};
#pragma unroll
      for (int rt = 0; rt < 3; rt++) hf[s][rt] = *(const u32x4*)(lds + (hb ^ (64 * s)) + rt * 4096);
    }
    SB();
#pragma unroll
    for (int s = 0; s < 4; s++)
#pragma unroll
      for (int rt = 0; rt < 3; rt++) {
        acc[rt][0] = MFMA16(asbf(fa[s]), asbf(hf[s][rt]), acc[rt][0]);
        acc[rt][1] = MFMA16(asbf(fb[s]), asbf(hf[s][rt]), acc[rt][1]);
      }
    const rsrc_t rs_b = mkrs(ws + WS_B + (size_t)n0 * 256, nn * 256);
#pragma unroll
    for (int rt = 0; rt < 3; rt++) {
      const f32x4 a = acc[rt][0] * NLOG2E, b = acc[rt][1] * NLOG2E;
      const u32x4 o = {pk2(a[0], a[1]), pk2(a[2], a[3]), pk2(b[0], b[1]), pk2(b[2], b[3])};
      __builtin_amdgcn_raw_buffer_store_b128(o, rs_b, c16 * 256 + 16 * gq + 64 * u + rt * 4096, 0, 0);
    }
    if (pf) asm volatile("s_waitcnt vmcnt(0)" :: "v"(pfv) : "memory");
  }
}

constexpr int OHS = 2 * CAP + 16;
constexpr int L_NODE = 0;
constexpr int L_WX = L_NODE + 320;
constexpr int L_WS = L_WX + 512;
constexpr int L_REC = L_WS + 16;
constexpr int L_CV = L_REC + 16 * (CAP + 1);
constexpr int L_SRC = L_CV + 8 * (CAP + 2);
constexpr int L_OH = L_SRC + 2 * (CAP + 8);
constexpr int L_CNT = L_OH + 8 * OHS;
constexpr int L_HM = L_CNT + 256;
constexpr int L_IMG = L_HM + 160;
constexpr int L_AT = L_IMG + 48 * 528;
constexpr int L_STG = (L_AT + 128 * 96 + 127) & ~127;
constexpr int K2_LDS = L_STG + 65536;
constexpr int L_MG = L_STG + 32768;
static_assert(K2_LDS <= 163840, "LDS");
static_assert(L_CV % 16 == 0 && L_OH % 16 == 0 && L_SRC % 16 == 0 && L_IMG % 16 == 0 && L_AT % 16 == 0, "align");
static_assert(L_IMG - L_OH > 5 * 4096 && L_IMG - L_OH <= 6 * 4096, "zero sweep");

extern "C" __global__ __launch_bounds__(512, 2) void egnn_k2(const u16* __restrict__ h, const u16* __restrict__ pos,
                                                  const u16* __restrict__ we, const u16* __restrict__ wx,
                                                  const u16* __restrict__ wh, const char* __restrict__ ws,
                                                  u16* __restrict__ out) {
  __shared__ __attribute__((aligned(128))) char lds[K2_LDS];
  int tid = threadIdx.x, lane = tid & 63;
  const int w = __builtin_amdgcn_readfirstlane(tid >> 6);
  const int b = (blockIdx.x & 7) * 32 + (blockIdx.x >> 3);
  const int n0 = (b * 625 + 15) >> 4, nn = (((b + 1) * 625 + 15) >> 4) - n0;
  int gq = lane >> 4, c16 = lane & 15, hh = lane >> 5, n32 = lane & 31;
  const u32 ldsb = (u32)(size_t)(LAS char*)lds;
  const u32x4 srd_g = mksrd(ws + WS_B - 4096, 4096 + NN * 256), srd_pos = mksrd(pos, NN * 6);

  asm volatile("" :: "s"(h), "s"(pos), "s"(we), "s"(wx), "s"(wh), "s"(ws), "s"(out));
  u32 cwv[6];
  u32x4 hv[3], t4;
  if (w < 4) {
    LDB("dwordx4", t4, lane * 16, mksrd(ws + WS_T + b * 1024, 1024), 0);
  } else {
    const int ty = tid - 256;
    const u32x4 srd_h = mksrd(h + (size_t)n0 * 128, nn * 256), srd_we = mksrd(we, 261 * 256);
#pragma unroll
    for (int i = 0; i < 3; i++) LDB("dwordx4", hv[i], ty * 16, srd_h, i * 4096);
    if (w >= 6) {
#pragma unroll
      for (int k = 0; k < 5; k++) LDB("ushort", cwv[k], tid * 2, srd_we, (256 + k) * 256 - 768);
      LDB("ushort", cwv[5], tid * 2 - 768, mksrd(wx, 256), 0);
    } else {
      LDB("ushort", cwv[0], ty * 2, srd_pos, n0 * 6);
    }
    const u32 vw = gq * 256 + 16 * (c16 ^ (4 * gq)), so = (w - 4) * 8192;
    SETM0(ldsb + L_STG + so);
    DMA1(0, vw, srd_we, so); DMA1(1024, vw, srd_we, so); DMA1(2048, vw, srd_we, so); DMA1(3072, vw, srd_we, so);
    SETM0(ldsb + L_STG + so + 4096);
    DMA1(0, vw, srd_we, so + 4096); DMA1(1024, vw, srd_we, so + 4096); DMA1(2048, vw, srd_we, so + 4096); DMA1(3072, vw, srd_we, so + 4096);
    SB();
#pragma unroll
    for (int i = 0; i < 5; i++) *(u32x4*)(lds + L_OH + ty * 16 + i * 4096) = (u32x4)(0u);
    if (ty < (L_IMG - L_OH - 5 * 4096) / 16) *(u32x4*)(lds + L_OH + ty * 16 + 5 * 4096) = (u32x4)(0u);
    char* ip = lds + L_IMG + (ty >> 4) * 528 + 256 + c16 * 16;
#pragma unroll
    for (int i = 0; i < 3; i++) *(u32x4*)(ip + i * 16 * 528) = (u32x4)(0u);
    if (ty < 48) *(u32x4*)(lds + L_IMG + ty * 528 + 512) = (u32x4)(0u);
  }
  SB();
  u32x4 id0, id1, one4 = {hh ? 0u : 0x3F80u, 0u, 0u, 0u};
  int ga[4], recoff, ohoff, cvoff, axoff, srcoff;
  u32 go[4], vsrc;
  auto lane_consts = [&]() {
    const int k0 = n32 - 8 * hh;
    const u32 one = 0x3F80u << ((k0 & 1) * 16);
#pragma unroll
    for (int i = 0; i < 4; i++) {
      id0[i] = (k0 >> 1) == i ? one : 0u;
      id1[i] = (k0 >> 1) == 8 + i ? one : 0u;
    }
    const int prow = (n32 & 19) | ((n32 & 4) << 1) | ((n32 & 8) >> 1);
    const int sa = prow & 3, sb = prow >> 2;
#pragma unroll
    for (int s = 0; s < 4; s++) ga[s] = (L_STG + w * 8192 + (8 * sa + sb) * 128 + 16 * (hh ^ (2 * sa + ((sb >> 1) & 1)))) ^ (32 * s);
#pragma unroll
    for (int j = 0; j < 4; j++) go[j] = 4096 - 1024 * j + 16 * ((lane & 7) ^ (2 * j + ((lane >> 4) & 1)));
    recoff = L_REC + prow * 16;
    ohoff = L_OH + (n32 >> 2) * OHS + 16 * hh;
    cvoff = L_CV + (8 * hh + (c16 >> 2)) * 8;
    axoff = L_OH + (c16 >> 2) * OHS + 32 * (gq & 1) + 16 * (c16 & 1) + 8 * ((c16 >> 1) & 1);
    srcoff = L_SRC + (lane >> 3) * 8;
    vsrc = (lane >> 1) * 256 + 32 * w + 16 * (lane & 1);
  };
  lane_consts();
  const u32 m0a = ldsb + L_STG + w * 8192, m0b = m0a + 4096;
  asm volatile("" : "+v"(one4), "+v"(id0), "+v"(id1), "+v"(ga[0]), "+v"(ga[1]), "+v"(ga[2]), "+v"(ga[3]), "+v"(go[0]), "+v"(go[1]), "+v"(go[2]), "+v"(go[3]));
  asm volatile("" : "+v"(recoff), "+v"(ohoff), "+v"(cvoff), "+v"(axoff), "+v"(srcoff), "+v"(vsrc));
  SB();
  int mtot, scnt, sexc;
  u32 sbase;
  auto decode = [&]() {
    if (lane == 62) t4[2] = t4[3] = 0;
    if (lane == 63) t4 = (u32x4)(0u);
    const int c0 = t4[0] >> 16, c1 = c0 + (t4[1] >> 16), c2 = c1 + (t4[2] >> 16), c3 = c2 + (t4[3] >> 16);
    const int inc = wave_incl_scan(c3);
    mtot = __builtin_amdgcn_readlane(inc, 63);
    const int wq = w & 3;
    const u32 tv = wq == 0 ? t4[0] : wq == 1 ? t4[1] : wq == 2 ? t4[2] : t4[3];
    scnt = tv >> 16;
    sexc = inc - c3 + (wq == 0 ? 0 : wq == 1 ? c0 : wq == 2 ? c1 : c2);
    sbase = ((4 * lane + wq) * CH + (tv & 0xffffu)) * 12;
  };
  if (w < 4) {
    VMWAIT0(); SB();
    decode();
    if (tid == 0) *(int*)(lds + L_WS) = mtot;
    if (mtot <= CAP) {
      u32* p = (u32*)(lds + L_CV) + sexc;
      u32 v = sbase;
      if (scnt & 1) p[scnt - 1] = v + 12 * (scnt - 1);
#pragma clang loop vectorize(disable) unroll(disable)
      for (int i = 1; i < scnt; i += 2, p += 2, v += 24) { p[0] = v; p[1] = v + 12; }
    }
  } else {
    VMWAIT0(); SB();
    asm volatile("" : "+v"(cwv[0]), "+v"(cwv[1]), "+v"(cwv[2]), "+v"(cwv[3]), "+v"(cwv[4]), "+v"(cwv[5]));
    char* hp = lds + L_IMG + ((tid - 256) >> 4) * 528 + c16 * 16;
#pragma unroll
    for (int i = 0; i < 3; i++) *(u32x4*)(hp + i * 16 * 528) = hv[i];
    if (w < 6) {
      const int t3 = tid - 256, nl = __umul24(t3, 21846) >> 16;
      if (t3 < 120) *(u16*)(lds + L_NODE + nl * 2 + t3 * 2) = (u16)cwv[0];
    }
  }
  __syncthreads();
  u32x3 rec[3];
  bool general;
  {
    const u32x4 srd_rec = mksrd(ws + WS_R, NE * 12);
    u32 ro[3];
#pragma unroll
    for (int k = 0; k < 3; k++) ro[k] = ((const u32*)(lds + L_CV))[tid + 512 * k];
    mtot = __builtin_amdgcn_readfirstlane(*(const int*)(lds + L_WS));
    general = mtot > CAP;
    const int lim = general ? 0 : mtot;
#pragma unroll
    for (int k = 0; k < 3; k++) LDB("dwordx3", rec[k], tid + 512 * k < lim ? ro[k] : 0x7fffff00u, srd_rec, 0);
  }
  if (general) asm volatile("s_waitcnt vmcnt(0)" :: "v"(rec[0]), "v"(rec[1]), "v"(rec[2]) : "memory");
  SB();
  if (w >= 6) {
    const float c0 = bf2f(cwv[0]) * NLOG2E;
    const u32 d_hi = pk2(c0, 0.f);
    const u32x4 o = {pk2(bf2f(cwv[1]) * NLOG2E, bf2f(cwv[2]) * NLOG2E), pk2(bf2f(cwv[3]) * NLOG2E, bf2f(cwv[4]) * NLOG2E),
                     pk2(c0, c0 - bf2f(d_hi)), 0u};
    *(u32x4*)(lds + L_AT + (tid - 384) * 96 + 80) = o;
    ((float*)(lds + L_WX))[tid - 384] = bf2f(cwv[5]) * NLN2;
  }
  const char* const xb = lds + L_IMG + c16 * 528 + 16 * gq;
  if (w < 4) {
    f32x4 acc[3][2];
#pragma unroll
    for (int rt = 0; rt < 3; rt++) acc[rt][0] = acc[rt][1] = (f32x4)(0.f);
    const char* wb = lds + L_STG + (8 * gq + (c16 >> 2)) * 256 + 16 * ((4 * w + (c16 & 3)) ^ (c16 & 12));
    u32x4 fa[4], fb[4], hf[4][3];
#pragma unroll
    for (int s = 0; s < 4; s++) {
      const u32x2 x0 = trread(wb + (32 * s) * 256), x1 = trread(wb + (32 * s + 4) * 256);
      const u32x2 y0 = trread(wb + (32 * s) * 256 + 8), y1 = trread(wb + (32 * s + 4) * 256 + 8);
      fa[s] = (u32x4){x0[0], x0[1], x1[0], x1[1]};
      fb[s] = (u32x4){y0[0], y0[1], y1[0], y1[1]};
#pragma unroll
      for (int rt = 0; rt < 3; rt++) hf[s][rt] = *(const u32x4*)(xb + rt * 8448 + 64 * s);
    }
    SB();
#pragma unroll
    for (int s = 0; s < 4; s++)
#pragma unroll
      for (int rt = 0; rt < 3; rt++) {
        acc[rt][0] = MFMA16(asbf(hf[s][rt]), asbf(fa[s]), acc[rt][0]);
        acc[rt][1] = MFMA16(asbf(hf[s][rt]), asbf(fb[s]), acc[rt][1]);
      }
    char* ap = lds + L_AT + (32 * w + 8 * (c16 >> 2) + (c16 & 3)) * 96 + 8 * gq;
#pragma unroll
    for (int rt = 0; rt < 3; rt++)
      if (16 * rt + 4 * gq < 40)
#pragma unroll
        for (int j = 0; j < 2; j++) {
          const f32x4 a = acc[rt][j] * (NLOG2E / 1.0078125f);
          *(u32x2*)(ap + j * 384 + rt * 32) = (u32x2){pk2(a[0], a[1]), pk2(a[2], a[3])};
        }
  }
  f32x16 Y[4], XA, XB, e;
  u32x4 sp0 = (u32x4)(0u), sp1 = (u32x4)(0u), pa = (u32x4)(0u), pb = (u32x4)(0u);
  asm volatile("" : "+v"(sp0), "+v"(sp1), "+v"(pa), "+v"(pb));
  Y[0] = MFMA32(asbf(pa), asbf(pa), (f32x16)(0.f));
  Y[1] = MFMA32(asbf(pb), asbf(pb), (f32x16)(0.f));
  Y[2] = MFMA32(asbf(sp0), asbf(sp0), (f32x16)(0.f));
  Y[3] = MFMA32(asbf(sp1), asbf(sp1), (f32x16)(0.f));
  XB = MFMA32(asbf(pa), asbf(pb), (f32x16)(0.f));
  asm volatile("" : "+v"(Y[0]), "+v"(Y[1]), "+v"(Y[2]), "+v"(Y[3]), "+v"(XB));
  SB();
  u32x4 wex[4];
  u32 ad[4];
  int ninc;
  u32x4 srd = srd_g;
  u32 sfb = 128;
  auto prep = [&](u32x2 sv, bool real) {
    if (real) {
      asm("v_mad_u32_u16 %0, %1, %2, %3" : "=v"(ad[0]) : "v"(sv[0]), "s"(256), "v"(go[0]));
      asm("v_mad_u32_u16 %0, %1, %2, %3 op_sel:[1,0,0,0]" : "=v"(ad[1]) : "v"(sv[0]), "s"(256), "v"(go[1]));
      asm("v_mad_u32_u16 %0, %1, %2, %3" : "=v"(ad[2]) : "v"(sv[1]), "s"(256), "v"(go[2]));
      asm("v_mad_u32_u16 %0, %1, %2, %3 op_sel:[1,0,0,0]" : "=v"(ad[3]) : "v"(sv[1]), "s"(256), "v"(go[3]));
    } else {
#pragma unroll
      for (int j = 0; j < 4; j++) ad[j] = vsrc + 7168 * j;
      srd = mksrd(wh, 65536);
      sfb = 32768;
    }
  };
  auto issue_a = [&]() { SETM0(m0a); DMA1(0, ad[0], srd, 0); DMA1(1024, ad[1], srd, 0); DMA1(2048, ad[2], srd, 0); DMA1(3072, ad[3], srd, 0); };
  auto issue_b = [&]() { SETM0(m0b); DMA1(0, ad[0], srd, sfb); DMA1(1024, ad[1], srd, sfb); DMA1(2048, ad[2], srd, sfb); DMA1(3072, ad[3], srd, sfb); };
  auto node_at = [&](int x) { return (int)__builtin_popcountll(__builtin_amdgcn_ballot_w64(ninc <= x)); };
  auto loadw = [&](int base) {
    const int g0 = base >> 3, bm = base & 7;
    const char* pa = lds + L_AT + n32 * 96 + (hh ? g0 * 16 : 80);
    const char* pb = lds + L_AT + n32 * 96 + (hh ? g0 * 16 + 16 : 80);
    const unsigned long long lo = ~0ull << (16 * min(bm, 3)) << (bm > 3 ? 16 : 0), hi = bm > 4 ? ~0ull << (16 * (bm - 4)) : ~0ull;
    const u32 mk[4] = {(u32)lo, (u32)(lo >> 32), (u32)hi, (u32)(hi >> 32)};
#pragma unroll
    for (int t = 0; t < 4; t++) {
      const u32x4 a = *(const u32x4*)(pa + t * 3072), bb = *(const u32x4*)(pb + t * 3072);
#pragma unroll
      for (int i = 0; i < 4; i++) wex[t][i] = (a[i] & mk[i]) | (bb[i] & ~mk[i]);
    }
  };
  auto pdot = [&](auto nbc) {
    constexpr int nb = decltype(nbc)::value;
    f32x4 p4[2] = {(f32x4)(0.f), (f32x4)(0.f)};
#pragma unroll
    for (int q0 = 0; q0 < 16; q0 += nb) {
      f32x4 wv[16];
#pragma unroll
      for (int q = q0; q < q0 + nb; q++) wv[q] = *(const f32x4*)(lds + L_WX + (8 * q + 4 * hh) * 4);
      SB();
#pragma unroll
      for (int q = q0; q < q0 + nb; q++)
        p4[q & 1] += (f32x4){Y[q >> 2][4 * (q & 3)], Y[q >> 2][4 * (q & 3) + 1], Y[q >> 2][4 * (q & 3) + 2], Y[q >> 2][4 * (q & 3) + 3]} * wv[q];
      SB();
    }
    p4[0] += p4[1];
    const f32x2 pa = (f32x2){p4[0][0], p4[0][1]} + (f32x2){p4[0][2], p4[0][3]};
    const u32 ph = __float_as_uint(pa[0] + pa[1]);
    const auto sw = __builtin_amdgcn_permlane32_swap(ph, ph, false, false);
    return __uint_as_float(sw[0]) + __uint_as_float(sw[1]);
  };
  auto place = [&](u32x3 rec, u32x2 ps, u32x2 pd, u32 dl, u32 p) {
    const u32 sh = (rec[2] & 1) * 16;
    const u32 sxy = __builtin_amdgcn_alignbit(ps[1], ps[0], sh), sz = ps[1] >> sh;
    const u32 dxy = pk2(bf2f(pd[0]) - bf2f(sxy), bfhi(pd[0]) - bfhi(sxy));
    const u32 dz1 = pk2(bf2f(pd[1]) - bf2f(sz), 1.0f);
    const float fx = bf2f(dxy), fy = bfhi(dxy), fz = bf2f(dz1);
    const float d2 = fx * fx + fy * fy + fz * fz;
    *(u32x4*)(lds + L_REC + p * 16) = (u32x4){rec[0], rec[1], pk2(d2, d2), dl};
    *(u32x2*)(lds + L_CV + p * 8) = (u32x2){mul16x2(dxy, OHINV * 0x10001u), mul16x2(dz1, OHINV * 0x10001u)};
    *(u16*)(lds + L_OH + (dl & 7) * OHS + 2 * p) = (u16)OH;
  };
  auto count_nodes = [&](int m) {
    const int cn = ((const u32*)(lds + L_CNT))[lane];
    ninc = wave_incl_scan(cn);
    if (w == 7) {
      const int p = m + n32 < ((m + 31) & ~31) ? m + n32 : CAP;
      *(u32x4*)(lds + L_REC + p * 16) = (u32x4)(0u);
      ((u16*)(lds + L_SRC))[p] = 0;
    }
    return ninc - cn;
  };
#define RDG(s, half) (*(const u32x4*)(lds + ga[s] + (half) * 4096))
#define EXP_(lo, hi) _Pragma("unroll") for (int r = lo; r < hi; r++) e[r] = __builtin_amdgcn_exp2f(xc[r]); SB()
#define RCP_(lo, hi) _Pragma("unroll") for (int r = lo; r < hi; r++) e[r] = __builtin_amdgcn_rcpf(e[r]); SB()
#define PACK_() _Pragma("unroll") for (int i = 0; i < 4; i++) { pa[i] = pk2(e[2 * i], e[2 * i + 1]); pb[i] = pk2(e[8 + 2 * i], e[8 + 2 * i + 1]); } \
  asm volatile("" : "+v"(pa), "+v"(pb)); SB()
#define SPROD(o, ca, cb) (u32x4){mul16x2(o[0], ca[0]), mul16x2(o[1], ca[1]), mul16x2(o[2], cb[0]), mul16x2(o[3], cb[1])}

  const int m = mtot;
  const bool third = w * 64 + 1024 < m;
  u32 rk[3], dl[3];
  u32x2 ps[3], pd[3];
  if (!general) {
    VMWAIT0(); SB();
#pragma unroll
    for (int k = 0; k < 3; k++)
      if (k < 2 || third) {
        LDB("dwordx2", ps[k], ((rec[k][2] & 0xffffu) * 6) & ~3u, srd_pos, 0);
        dl[k] = tid + 512 * k < m ? (rec[k][2] >> 16) - n0 : 48u + c16;
        rk[k] = atomicAdd((u32*)(lds + L_CNT) + dl[k], 1u);
        pd[k] = *(const u32x2*)(lds + L_NODE + dl[k] * 8);
      }
    __syncthreads();
    const int nst = count_nodes(m);
    const int nprev = __builtin_amdgcn_ds_bpermute(lane * 4 - 28, ninc);
#pragma unroll
    for (int k = 0; k < 3; k++)
      if (k < 2 || third) {
        rk[k] += __builtin_amdgcn_ds_bpermute(dl[k] * 4, nst);
        rk[k] = dl[k] < 48 ? rk[k] : CAP;
        *(u16*)(lds + L_SRC + 2 * rk[k]) = (u16)rec[k][2];
      }
    general = m < 256 || __builtin_amdgcn_ballot_w64(lane >= 7 && lane < nn && ninc - nprev <= 30) != 0;
    if (general) asm volatile("s_waitcnt vmcnt(0)" :: "v"(ps[0]), "v"(ps[1]), "v"(ps[2]) : "memory");
  }
  if (!general) {
    {
      const int nt = (m + 31) >> 5, tq = nt >> 3, trm = nt & 7;
      const int tbeg = w * tq + min(w, trm);
      const int tend = tbeg + tq + (w < trm ? 1 : 0);
      srcoff += tbeg * 64;
      __syncthreads();
      const u32x2 sv0 = *(const u32x2*)(lds + srcoff), sv1 = *(const u32x2*)(lds + srcoff + 64);
      prep(sv0, true);
      if (w < 4) {
        issue_a();
        issue_b();
        VMWAIT(8);
      } else {
        VMWAIT(0);
      }
      SB();
      int base = node_at(tbeg * 32);
      loadw(base);
      const int lnode = node_at(min(tend * 32, m) - 1);
      const int hnode = (tbeg > 0 && node_at(tbeg * 32 - 1) == base) ? base : -1;
      if (hnode >= 0 && lane == 0) __hip_atomic_fetch_or((u32*)(lds + L_HM) + hnode, 1u << w, __ATOMIC_RELAXED, __HIP_MEMORY_SCOPE_WORKGROUP);
      recoff += tbeg * 512; cvoff += tbeg * 256 - 256; ohoff += tbeg * 64 - 64; axoff += tbeg * 64;
#pragma unroll
      for (int k = 0; k < 3; k++)
        if (k < 2 || third) place(rec[k], ps[k], pd[k], dl[k], rk[k]);
      if (w >= 4) {
        issue_a();
        issue_b();
      }
      prep(sv1, tbeg + 1 < tend);
#pragma unroll
      for (int t = 0; t < 4; t++) asm volatile("" : "+v"(wex[t]));
      auto flush = [&](int base, int lim, auto clearc) {
        const int node = base + (((n32 >> 2) - base) & 7), comp = n32 & 3;
        constexpr bool clear = decltype(clearc)::value != 0;
        const float pt = pdot(IC<(clear ? 2 : 16)>{});
        if (node < lim) {
          char* mp = lds + L_IMG + (node == hnode ? 39 + w : node) * 528;
          if (comp == 3) {
#pragma unroll
            for (int q = 0; q < 16; q++)
              *(u32x2*)(mp + 256 + 8 * hh + 16 * q) = (u32x2){pk2(Y[q >> 2][4 * (q & 3)], Y[q >> 2][4 * (q & 3) + 1]),
                                                              pk2(Y[q >> 2][4 * (q & 3) + 2], Y[q >> 2][4 * (q & 3) + 3])};
          } else {
            *(float*)(mp + 512 + 4 * comp) = pt;
          }
        }
        if (clear) {
          const float keep = node < lim ? 0.f : 1.f;
#pragma unroll
          for (int t = 0; t < 4; t++) Y[t] *= keep;
        }
      };
      __syncthreads();
      u32x4 aex;
      {
        const u32x2 t0 = trread(lds + axoff), t1 = trread(lds + axoff + 4 * OHS);
        const u32x4 ra = *(const u32x4*)(lds + recoff);
        aex = hh ? (u32x4){t0[0], t0[1], t1[0], t1[1]} : ra;
      }
      XA = MFMA32(asbf(aex), asbf(wex[0]), (f32x16)(0.f)); PIN(XA);
      VMWAIT(4);
      u32x4 g0 = RDG(0, 0), g1 = RDG(1, 0), g2 = RDG(2, 0), g3 = RDG(3, 0);
      LGKMWAIT0();
      SB();
      XA = MFMA32(asbf(g0), asbf(id0), XA); PIN(XA);
      issue_a();
      XA = MFMA32(asbf(g1), asbf(id1), XA); PIN(XA);
      int fbase = base;
      __builtin_amdgcn_s_setprio(3);
#define EXPX(X, lo, hi) _Pragma("unroll") for (int r = lo; r < hi; r++) e[r] = __builtin_amdgcn_exp2f(X[r]); SB()
#define PACKX(X) _Pragma("unroll") for (int i = 0; i < 4; i++) { pa[i] = pk2(X[2 * i], X[2 * i + 1]); pb[i] = pk2(X[8 + 2 * i], X[8 + 2 * i + 1]); } \
  asm volatile("" : "+v"(pa), "+v"(pb)); SB()
#define SEG(t, p, sp) Y[t] = MFMA32(asbf(p), asbf(sp), Y[t]); PIN(Y[t])
#define PLUS1() e = MFMA32(asbf(one4), asbf(one4), e); PIN(e)
      for (int left = tend - tbeg, x = tbeg * 32 + 32; left > 0; left--, x += 32) {
        if (__builtin_expect(left == 1, 0)) __builtin_amdgcn_s_setprio(1);
        SB();
        SEG(2, pa, sp0);
        EXPX(XA, 0, 8);
        SEG(2, pb, sp1);
        EXPX(XA, 8, 16);
        PLUS1();
        PACKX(XB);
        srcoff += 64; recoff += 512; cvoff += 256; ohoff += 64; axoff += 64;
        asm volatile("" : "+v"(srcoff), "+v"(recoff), "+v"(cvoff), "+v"(ohoff), "+v"(axoff)); SB();
        RCP_(0, 4);
        XB = MFMA32(asbf(aex), asbf(wex[1]), (f32x16)(0.f)); PIN(XB);
        RCP_(4, 8);
        XB = MFMA32(asbf(g2), asbf(id0), XB); PIN(XB);
        RCP_(8, 12);
        XB = MFMA32(asbf(g3), asbf(id1), XB); PIN(XB);
        RCP_(12, 16);
        XA = XA * e; PIN(XA);
        SEG(3, pa, sp0);
        VMWAIT(4);
        g0 = RDG(0, 1); g1 = RDG(1, 1); g2 = RDG(2, 1); g3 = RDG(3, 1);
        const u32x2 sv = *(const u32x2*)(lds + srcoff + 64);
        SB();
        EXPX(XB, 0, 8);
        SEG(3, pb, sp1);
        const u32x2 c0 = trread(lds + cvoff), c1 = trread(lds + cvoff + 32), c2 = trread(lds + cvoff + 128), c3 = trread(lds + cvoff + 160);
        const u32x4 o0 = *(const u32x4*)(lds + ohoff), o1 = *(const u32x4*)(lds + ohoff + 32);
        SB();
        EXPX(XB, 8, 16);
        PLUS1();
        PACKX(XA);
        sp0 = SPROD(o0, c0, c1);
        sp1 = SPROD(o1, c2, c3);
        asm volatile("" : "+v"(sp0), "+v"(sp1));
        LGKMWAIT0();
        SB();
        SETM0(m0b); DMA1(0, ad[0], srd, sfb); SB();
        RCP_(0, 4);
        XA = MFMA32(asbf(aex), asbf(wex[2]), (f32x16)(0.f)); PIN(XA);
        DMA1(1024, ad[1], srd, sfb); SB();
        RCP_(4, 8);
        XA = MFMA32(asbf(g0), asbf(id0), XA); PIN(XA);
        DMA1(2048, ad[2], srd, sfb); SB();
        RCP_(8, 12);
        XA = MFMA32(asbf(g1), asbf(id1), XA); PIN(XA);
        DMA1(3072, ad[3], srd, sfb); SB();
        RCP_(12, 16);
        XB = XB * e; PIN(XB);
        if (__builtin_expect(fbase != base, 0)) {
          flush(fbase, base, IC<1>{});
          fbase = base;
        }
        if (__builtin_expect(left <= 2, 0)) {
          if (left == 2) __builtin_amdgcn_s_setprio(2);
          else __builtin_amdgcn_s_setprio(0);
          prep(sv, false);
        } else {
          prep(sv, true);
        }
        asm volatile("" : "+v"(ad[0]), "+v"(ad[1]), "+v"(ad[2]), "+v"(ad[3]));
        SB();
        SEG(0, pa, sp0);
        const int nlast = node_at(min(x + 31, m - 1));
        SB();
        EXPX(XA, 0, 8);
        SEG(0, pb, sp1);
        EXPX(XA, 8, 16);
        PLUS1();
        PACKX(XB);
        RCP_(0, 4);
        XB = MFMA32(asbf(aex), asbf(wex[3]), (f32x16)(0.f)); PIN(XB);
        RCP_(4, 8);
        XB = MFMA32(asbf(g2), asbf(id0), XB); PIN(XB);
        RCP_(8, 12);
        XB = MFMA32(asbf(g3), asbf(id1), XB); PIN(XB);
        RCP_(12, 16);
        XA = XA * e; PIN(XA);
        if (__builtin_expect(left > 1 && nlast >= base + 8, 0)) {
          base = node_at(x);
          loadw(base);
        }
        SB();
        SEG(1, pa, sp0);
        VMWAIT(4);
        const u32x2 t0 = trread(lds + axoff), t1 = trread(lds + axoff + 4 * OHS);
        const u32x4 ra = *(const u32x4*)(lds + recoff);
        g0 = RDG(0, 0); g1 = RDG(1, 0); g2 = RDG(2, 0); g3 = RDG(3, 0);
        SB();
        EXPX(XB, 0, 8);
        SEG(1, pb, sp1);
        EXPX(XB, 8, 16);
        PLUS1();
        PACKX(XA);
        LGKMWAIT0();
        SB();
        aex = hh ? (u32x4){t0[0], t0[1], t1[0], t1[1]} : ra;
        PIN(aex);
        SETM0(m0a); DMA1(0, ad[0], srd, 0); SB();
        RCP_(0, 4);
        XA = MFMA32(asbf(aex), asbf(wex[0]), (f32x16)(0.f)); PIN(XA);
        DMA1(1024, ad[1], srd, 0); SB();
        RCP_(4, 8);
        XA = MFMA32(asbf(g0), asbf(id0), XA); PIN(XA);
        DMA1(2048, ad[2], srd, 0); SB();
        RCP_(8, 12);
        XA = MFMA32(asbf(g1), asbf(id1), XA); PIN(XA);
        DMA1(3072, ad[3], srd, 0); SB();
        RCP_(12, 16);
        XB = XB * e; PIN(XB);
      }
      SEG(2, pa, sp0);
      SEG(2, pb, sp1);
      PACKX(XB);
      SEG(3, pa, sp0);
      SEG(3, pb, sp1);
      flush(base, lnode + 1, IC<0>{});
    }
  } else {
    tid = threadIdx.x;
    asm volatile("" : "+v"(tid));
    lane = tid & 63; gq = lane >> 4; c16 = lane & 15; hh = lane >> 5; n32 = lane & 31;
    lane_consts();
    t4 = *(const u32x4*)(ws + WS_T + b * 1024 + lane * 16);
    decode();
    const rsrc_t rs_rec = mkrs(ws + WS_R, NE * 12), rs_pos = mkrs(pos, NN * 6);
    for (int q = tid; q < 40 * 33; q += 512) *(u32x4*)(lds + L_MG + q * 16) = (u32x4)(0u);
    for (int r0 = 0; r0 < mtot; r0 += CAP) {
      const int m = min(mtot - r0, CAP);
      __syncthreads();
      for (int q = tid; q < (L_HM - L_OH) / 16; q += 512) *(u32x4*)(lds + L_OH + q * 16) = (u32x4)(0u);
      const int lim = min(scnt, r0 + CAP - sexc);
      for (int i = max(r0 - sexc, 0) + (w >> 2); i < lim; i += 2)
        *(u32x3*)(lds + L_STG + (sexc + i - r0) * 16) = __builtin_amdgcn_raw_buffer_load_b96(rs_rec, sbase + i * 12, 0, 0);
      __syncthreads();
      for (int f = tid; f < m; f += 512) {
        u32* rp = (u32*)(lds + L_STG + f * 16);
        rp[3] = atomicAdd((u32*)(lds + L_CNT) + ((rp[2] >> 16) - n0), 1u);
      }
      __syncthreads();
      const int nst = count_nodes(m);
      for (int f0 = w * 64; f0 < m; f0 += 512) {
        const bool ok = f0 + lane < m;
        const u32x4 rc = *(const u32x4*)(lds + L_STG + (f0 + lane) * 16);
        const u32 dl = ok ? (rc[2] >> 16) - n0 : 0u;
        const u32 pp = rc[3] + __builtin_amdgcn_ds_bpermute(dl * 4, nst), p = ok ? pp : CAP;
        const u32x2 ps = __builtin_amdgcn_raw_buffer_load_b64(rs_pos, ((rc[2] & 0xffffu) * 6) & ~3u, 0, 0);
        *(u16*)(lds + L_SRC + 2 * p) = (u16)rc[2];
        place((u32x3){rc[0], rc[1], rc[2]}, ps, *(const u32x2*)(lds + L_NODE + dl * 8), dl, p);
      }
      __syncthreads();
      const int nt = w < 4 ? (m + 31) >> 5 : 0;
      for (int tile = w; tile < nt; tile += 4) {
        const int last = node_at(min(tile * 32 + 31, m - 1));
        prep(*(const u32x2*)(lds + srcoff + tile * 64), true);
        SETM0(m0a); DMA1(0, ad[0], srd_g, 0); DMA1(1024, ad[1], srd_g, 0); DMA1(2048, ad[2], srd_g, 0); DMA1(3072, ad[3], srd_g, 0);
        SETM0(m0b); DMA1(0, ad[0], srd_g, 128); DMA1(1024, ad[1], srd_g, 128); DMA1(2048, ad[2], srd_g, 128); DMA1(3072, ad[3], srd_g, 128);
        const u32x2 t0 = trread(lds + axoff + tile * 64), t1 = trread(lds + axoff + tile * 64 + 4 * OHS);
        const u32x4 ra = *(const u32x4*)(lds + recoff + tile * 512);
        const u32x4 aex = hh ? (u32x4){t0[0], t0[1], t1[0], t1[1]} : ra;
        for (int base = node_at(tile * 32); base <= last; base += 8) {
          loadw(base);
          u32x4 sp[2];
#pragma unroll
          for (int s = 0; s < 2; s++) {
            const u32x2 c0 = trread(lds + cvoff + tile * 256 + s * 128), c1 = trread(lds + cvoff + tile * 256 + s * 128 + 32);
            const u32x4 oh = *(const u32x4*)(lds + ohoff + tile * 64 + 32 * s);
            sp[s] = SPROD(oh, c0, c1);
          }
#pragma unroll 1
          for (int j = 0; j < 16; j++) {
            const int dn = *(const u32*)(lds + L_REC + (tile * 32 + 16 * (j >> 3) + 8 * hh + (j & 7)) * 16 + 12);
            const u32 mk = (dn < base || dn >= base + 8) ? ((j & 1) ? 0x0000ffffu : 0xffff0000u) : 0xffffffffu;
#pragma unroll
            for (int i = 0; i < 8; i++) sp[i >> 2][i & 3] &= (i == (j >> 1)) ? mk : 0xffffffffu;
          }
          VMWAIT0();
#pragma unroll
          for (int t = 0; t < 4; t++) {
            const u32x4 ga_ = RDG((2 * t) & 3, t >> 1), gb_ = RDG((2 * t + 1) & 3, t >> 1);
            f32x16 xc = MFMA32(asbf(aex), asbf(wex[t]), (f32x16)(0.f)), e;
            xc = MFMA32(asbf(ga_), asbf(id0), xc);
            xc = MFMA32(asbf(gb_), asbf(id1), xc);
            u32x4 pa, pb;
            EXP_(0, 16);
            e = e + 1.0f;
            RCP_(0, 16);
            e = e * xc;
            PACK_();
            Y[t] = MFMA32(asbf(pa), asbf(sp[0]), (f32x16)(0.f));
            Y[t] = MFMA32(asbf(pb), asbf(sp[1]), Y[t]);
          }
          const int node = base + (((n32 >> 2) - base) & 7), comp = n32 & 3;
          const float pt = pdot(IC<4>{});
          if (node <= last) {
            float* mp = (float*)(lds + L_MG) + node * 132;
            if (comp == 3) {
#pragma unroll
              for (int t = 0; t < 4; t++)
#pragma unroll
                for (int r = 0; r < 16; r++) atomicAdd(mp + 4 * hh + 32 * t + 8 * (r >> 2) + (r & 3), Y[t][r]);
            } else if (hh == 0) {
              atomicAdd(mp + 128 + comp, pt);
            }
          }
        }
        LGKMWAIT0();
      }
    }
    __syncthreads();
    for (int q = tid; q < 40 * 66; q += 512) {
      const int row = q / 66, c = q - row * 66;
      const f32x2 v = *(const f32x2*)(lds + L_MG + row * 528 + c * 8);
      if (c < 64) *(u32*)(lds + L_IMG + row * 528 + 256 + c * 4) = pk2(v[0], v[1]);
      else *(f32x2*)(lds + L_IMG + row * 528 + c * 8) = v;
    }
    __syncthreads();
    const u32x4 srd_wh = mksrd(wh, 65536);
    SETM0(m0a); DMA1(0, vsrc, srd_wh, 0); DMA1(1024, vsrc, srd_wh, 7168); DMA1(2048, vsrc, srd_wh, 14336); DMA1(3072, vsrc, srd_wh, 21504);
    SETM0(m0b); DMA1(0, vsrc, srd_wh, 32768); DMA1(1024, vsrc, srd_wh, 39936); DMA1(2048, vsrc, srd_wh, 47104); DMA1(3072, vsrc, srd_wh, 54272);
    VMWAIT0();
  }

  int tid2 = threadIdx.x;
  asm volatile("" : "+v"(tid2));
  const int c16e = tid2 & 15, gqe = (tid2 >> 4) & 3;
  const char* const xbe = lds + L_IMG + c16e * 528 + 16 * gqe;
  f32x4 acc[3], acm[3];
  const char* const wfp = lds + L_STG + w * 8192 + (8 * gqe + (c16e >> 2)) * 32 + 8 * (c16e & 3);
  VMWAIT(4);
  u32x4 wf2[4];
#pragma unroll
  for (int rt = 0; rt < 3; rt++) acc[rt] = acm[rt] = (f32x4)(0.f);
  {
    u32x4 wf1[4], xf[4][3];
#pragma unroll
    for (int s = 0; s < 4; s++) {
      const u32x2 x0 = trread(wfp + s * 1024), x1 = trread(wfp + s * 1024 + 128);
      const u32x2 y0 = trread(wfp + 4096 + s * 1024), y1 = trread(wfp + 4096 + s * 1024 + 128);
      wf1[s] = (u32x4){x0[0], x0[1], x1[0], x1[1]};
      wf2[s] = (u32x4){y0[0], y0[1], y1[0], y1[1]};
#pragma unroll
      for (int rt = 0; rt < 3; rt++) xf[s][rt] = *(const u32x4*)(xbe + rt * 8448 + 64 * s);
    }
    SB();
#pragma unroll
    for (int s = 0; s < 4; s++)
#pragma unroll
      for (int rt = 0; rt < 3; rt++) acc[rt] = MFMA16(asbf(wf1[s]), asbf(xf[s][rt]), acc[rt]);
  }
  u32x2 hr[3];
  u32 hm[3], hl[3];
#pragma unroll
  for (int rt = 0; rt < 3; rt++) {
    hr[rt] = *(const u32x2*)(lds + L_IMG + c16e * 528 + 8 * gqe + rt * 8448 + 32 * w);
    hm[rt] = 16 * rt + c16e < 40 ? ((const u32*)(lds + L_HM))[16 * rt + c16e] : 0u;
    hl[rt] = 64 * gqe + 28 + 4 * __builtin_ctz(hm[rt] | 256u);
    hm[rt] &= hm[rt] - 1;
  }
  __syncthreads();
  {
    u32x4 xf[3][4];
#pragma unroll
    for (int s = 0; s < 4; s++)
#pragma unroll
      for (int rt = 2; rt >= 0; rt--) xf[rt][s] = *(const u32x4*)(xbe + 256 + rt * 8448 + 64 * s);
    SB();
#pragma unroll
    for (int s = 0; s < 4; s++)
#pragma unroll
      for (int rt = 2; rt >= 0; rt--) acm[rt] = MFMA16(asbf(wf2[s]), asbf(xf[rt][s]), acm[rt]);
  }
  const f32x4 hd = acm[2];
  for (;;) {
#pragma unroll
    for (int rt = 0; rt < 3; rt++)
#pragma unroll
      for (int r = 0; r < 4; r++) {
        const float hv_ = hd[r];
        acm[rt][r] += __uint_as_float(__builtin_amdgcn_ds_bpermute(hl[rt], __float_as_uint(hv_)));
      }
    if (__builtin_expect(__builtin_amdgcn_ballot_w64((hm[0] | hm[1] | hm[2]) != 0) == 0, 1)) break;
#pragma unroll
    for (int rt = 0; rt < 3; rt++) {
      hl[rt] = 64 * gqe + 28 + 4 * __builtin_ctz(hm[rt] | 256u);
      hm[rt] &= hm[rt] - 1;
    }
  }
  {
    const rsrc_t rs_o = mkrs(out + (size_t)n0 * 128, nn * 256);
#pragma unroll
    for (int rt = 0; rt < 3; rt++) {
      u32 o[2];
#pragma unroll
      for (int r = 0; r < 2; r++) {
        const f32x2 z = (f32x2){acc[rt][2 * r], acc[rt][2 * r + 1]} + (f32x2){acm[rt][2 * r], acm[rt][2 * r + 1]} * NLN2;
        const f32x2 t = z * NLOG2E;
        const f32x2 d = (f32x2){__builtin_amdgcn_exp2f(t[0]), __builtin_amdgcn_exp2f(t[1])} + 1.0f;
        const f32x2 v = z * (f32x2){__builtin_amdgcn_rcpf(d[0]), __builtin_amdgcn_rcpf(d[1])} + (f32x2){bf2f(hr[rt][r]), bfhi(hr[rt][r])};
        o[r] = pk2(v[0], v[1]);
      }
      __builtin_amdgcn_raw_buffer_store_b64((u32x2){o[0], o[1]}, rs_o, c16e * 256 + 8 * gqe, rt * 4096 + 32 * w, 0);
    }
  }
  if (w < 2) {
    const int nl = __umul24(tid2, 21846) >> 16, k = tid2 - 3 * nl;
    if (tid2 < 3 * nn) {
      const char* mp = lds + L_IMG + 512 + 4 * k;
      u32 hq = ((const u32*)(lds + L_HM))[nl];
      float pv = *(const float*)(mp + nl * 528) + bf2f(*(const u16*)(lds + L_NODE + nl * 2 + tid2 * 2));
      while (hq) {
        pv += *(const float*)(mp + (39 + __builtin_ctz(hq)) * 528);
        hq &= hq - 1;
      }
      out[(size_t)NN * 128 + n0 * 3 + tid2] = (u16)pk2(pv, 0.f);
    }
  }
  VMWAIT0();
}

extern "C" void kernel_launch(void* const* d_in, const int* in_sizes, int n_in, void* d_out, int out_size,
                              void* d_ws, size_t ws_size, hipStream_t stream) {
  const u16* h = (const u16*)d_in[0];
  const u16* pos = (const u16*)d_in[1];
  const u16* ea = (const u16*)d_in[2];
  const int* src = (const int*)d_in[3];
  const int* dst = (const int*)d_in[4];
  const u16* we = (const u16*)d_in[5];
  const u16* wx = (const u16*)d_in[6];
  const u16* wh = (const u16*)d_in[7];
  egnn_k1<<<NB, 512, 0, stream>>>(h, ea, src, dst, we, (char*)d_ws);
  egnn_k2<<<NB, 512, 0, stream>>>(h, pos, we, wx, wh, (const char*)d_ws, (u16*)d_out);
}
```

```cpp
#include <hip/hip_runtime.h>
#include <hip/hip_bf16.h>

typedef unsigned short u16;
typedef unsigned int u32;
typedef float f32x2 __attribute__((ext_vector_type(2)));
typedef float f32x4 __attribute__((ext_vector_type(4)));
typedef float f32x16 __attribute__((ext_vector_type(16)));
typedef __bf16 bf16x8 __attribute__((ext_vector_type(8)));
typedef __bf16 bf16x2 __attribute__((ext_vector_type(2)));
typedef u32 u32x2 __attribute__((ext_vector_type(2)));
typedef u32 u32x3 __attribute__((ext_vector_type(3)));
typedef u32 u32x4 __attribute__((ext_vector_type(4)));
typedef short s16x4 __attribute__((ext_vector_type(4)));
typedef u16 u16x2 __attribute__((ext_vector_type(2)));
#define LAS __attribute__((address_space(3)))
typedef __amdgpu_buffer_rsrc_t rsrc_t;

constexpr int NN = 10000;
constexpr int NE = 320000;
constexpr int NCH = 250;
constexpr int CH = 1280;
constexpr int NB = 256;
constexpr int CAP = 1408;
constexpr float NLOG2E = -1.4426950408889634f;
constexpr float NLN2 = -0.6931471805599453f;
constexpr u32 OH = 0x3F81u;
constexpr u32 OHINV = 0x81u;

constexpr size_t WS_T = 0;
constexpr size_t WS_R = WS_T + (size_t)NB * 256 * 4;
constexpr size_t WS_B = WS_R + (size_t)NE * 12;

__device__ __forceinline__ float bf2f(u32 v) { return __uint_as_float(v << 16); }
__device__ __forceinline__ float bfhi(u32 v) { return __uint_as_float(v & 0xffff0000u); }
__device__ __forceinline__ u32 pk2(float a, float b) {
  f32x2 v = {a, b};
  return __builtin_bit_cast(u32, __builtin_convertvector(v, bf16x2));
}
__device__ __forceinline__ u32 mul16x2(u32 a, u32 b) {
  return __builtin_bit_cast(u32, __builtin_bit_cast(u16x2, a) * __builtin_bit_cast(u16x2, b));
}
__device__ __forceinline__ int wave_incl_scan(int x) {
  x += __builtin_amdgcn_update_dpp(0, x, 0x111, 0xf, 0xf, false);
  x += __builtin_amdgcn_update_dpp(0, x, 0x112, 0xf, 0xf, false);
  x += __builtin_amdgcn_update_dpp(0, x, 0x114, 0xf, 0xf, false);
  x += __builtin_amdgcn_update_dpp(0, x, 0x118, 0xf, 0xf, false);
  x += __builtin_amdgcn_update_dpp(0, x, 0x142, 0xa, 0xf, false);
  x += __builtin_amdgcn_update_dpp(0, x, 0x143, 0xc, 0xf, false);
  return x;
}
__device__ __forceinline__ bf16x8 asbf(u32x4 v) { return __builtin_bit_cast(bf16x8, v); }
__device__ __forceinline__ u32x2 trread(const char* p) {
  return __builtin_bit_cast(u32x2, __builtin_amdgcn_ds_read_tr16_b64_v4i16((LAS s16x4*)p));
}
__device__ __forceinline__ rsrc_t mkrs(const void* p, u32 bytes) { return __builtin_amdgcn_make_buffer_rsrc((void*)p, 0, bytes, 0x00020000); }
#define MFMA32(a, b, c) __builtin_amdgcn_mfma_f32_32x32x16_bf16(a, b, c, 0, 0, 0)
#define MFMA16(a, b, c) __builtin_amdgcn_mfma_f32_16x16x32_bf16(a, b, c, 0, 0, 0)
#define SB() __builtin_amdgcn_sched_barrier(0)
#define VMWAIT0() asm volatile("s_waitcnt vmcnt(0)" ::: "memory")

template <int V> struct IC { static constexpr int value = V; };
__device__ __forceinline__ u32x4 mksrd(const void* p, u32 bytes) {
  const unsigned long long a = (unsigned long long)p;
  return (u32x4){(u32)a, (u32)(a >> 32) & 0xffffu, bytes, 0x00020000u};
}
#define DMA1(J, A, SRD, SOFF) asm volatile("buffer_load_dwordx4 %0, %1, %2 offen offset:" #J " lds" :: "v"(A), "s"(SRD), "s"(SOFF) : "memory")
#define LDB(T, DST, VO, SRD, SO) asm volatile("buffer_load_" T " %0, %1, %2, %3 offen" : "=v"(DST) : "v"(VO), "s"(SRD), "s"(SO) : "memory")
#define SETM0(V) asm volatile("s_mov_b32 m0, %0\n\ts_nop 0" :: "s"(V) : "memory")
#define VMWAIT(N) asm volatile("s_waitcnt vmcnt(" #N ")" ::: "memory")
#define LGKMWAIT0() asm volatile("s_waitcnt lgkmcnt(0)" ::: "memory")
#define PIN(x) do { asm volatile("" : "+v"(x)); SB(); } while (0)

constexpr int K1_H = 32768;
constexpr int K1_IMG = K1_H + 48 * 256;
constexpr int K1_HIST = K1_IMG + 16384;
constexpr int K1_LDS = K1_HIST + 8 * NB * 4 + 32;

extern "C" __global__ __launch_bounds__(512) void egnn_k1(const u16* __restrict__ h, const u16* __restrict__ ea,
                                               const int* __restrict__ src, const int* __restrict__ dst,
                                               const u16* __restrict__ we, char* __restrict__ ws) {
  __shared__ __attribute__((aligned(128))) char lds[K1_LDS];
  const int tid = threadIdx.x, lane = tid & 63;
  const int w = __builtin_amdgcn_readfirstlane(tid >> 6);
  const int c = (blockIdx.x & 7) * 32 + (blockIdx.x >> 3);
  const int gq = lane >> 4, c16 = lane & 15;
  u32* const hist = (u32*)(lds + K1_HIST);
  asm volatile("" :: "s"(h), "s"(ea), "s"(src), "s"(dst), "s"(we), "s"(ws));
  if (tid < NB) *(u32x4*)(hist + 4 * tid) = (u32x4)(0u);
  else if (tid < NB + 8) hist[7 * NB + tid] = 0;
  __syncthreads();
  auto meet = [&](int k) {
    LGKMWAIT0();
    if (lane == 0) __hip_atomic_fetch_add(&hist[8 * NB + k], 1u, __ATOMIC_RELAXED, __HIP_MEMORY_SCOPE_WORKGROUP);
    while (__hip_atomic_load(&hist[8 * NB + k], __ATOMIC_RELAXED, __HIP_MEMORY_SCOPE_WORKGROUP) < 4u) __builtin_amdgcn_s_sleep(1);
    asm volatile("" ::: "memory");
  };
  if (w < 4) {
    const u32 eo = tid * 20, nb = c < NCH ? CH * 4 : 0;
    const rsrc_t rs_d = mkrs(dst + c * CH, nb), rs_s = mkrs(src + c * CH, nb), rs_a = mkrs(ea + (size_t)c * CH * 4, 2 * nb);
    const u32x4 d4 = __builtin_amdgcn_raw_buffer_load_b128(rs_d, eo, 0, 0);
    const u32 d1 = __builtin_amdgcn_raw_buffer_load_b32(rs_d, eo + 16, 0, 0);
    const u32x4 s4 = __builtin_amdgcn_raw_buffer_load_b128(rs_s, eo, 0, 0);
    const u32 s1 = __builtin_amdgcn_raw_buffer_load_b32(rs_s, eo + 16, 0, 0);
    const u32x4 a0 = __builtin_amdgcn_raw_buffer_load_b128(rs_a, 2 * eo, 0, 0);
    const u32x4 a1 = __builtin_amdgcn_raw_buffer_load_b128(rs_a, 2 * eo + 16, 0, 0);
    const u32x2 a2 = __builtin_amdgcn_raw_buffer_load_b64(rs_a, 2 * eo + 32, 0, 0);
    __builtin_amdgcn_s_setprio(1);
    if (c < NCH) {
      u32* const img = (u32*)(lds + K1_IMG);
      u32* const base = hist + (4 + w) * NB;
      u32 bk[5], rk[5];
#pragma unroll
      for (int i = 0; i < 5; i++) {
        bk[i] = __umul24(i < 4 ? d4[i & 3] : d1, 214749u) >> 23;
        rk[i] = atomicAdd(&hist[w * NB + bk[i]], 1u);
      }
      meet(1);
      {
        const u32x4 v0 = *(const u32x4*)(hist + 4 * lane), v1 = *(const u32x4*)(hist + NB + 4 * lane);
        const u32x4 v2 = *(const u32x4*)(hist + 2 * NB + 4 * lane), v3 = *(const u32x4*)(hist + 3 * NB + 4 * lane);
        const u32x4 v = v0 + v1 + v2 + v3;
        const u32x4 pre = w == 0 ? (u32x4)(0u) : w == 1 ? v0 : w == 2 ? v0 + v1 : v - v3;
        const int s = v[0] + v[1] + v[2] + v[3];
        const int ex = wave_incl_scan(s) - s;
        const u32x4 bs = {(u32)ex, ex + v[0], ex + v[0] + v[1], ex + v[0] + v[1] + v[2]};
        *(u32x4*)(base + 4 * lane) = bs + pre;
        const u32 bb = w == 0 ? bs[0] : w == 1 ? bs[1] : w == 2 ? bs[2] : bs[3];
        const u32 vv = w == 0 ? v[0] : w == 1 ? v[1] : w == 2 ? v[2] : v[3];
        ((u32*)(ws + WS_T))[(4 * lane + w) * 256 + c] = bb | (vv << 16);
      }
#pragma unroll
      for (int i = 0; i < 5; i++) {
        u32* p = img + 3 * (base[bk[i]] + rk[i]);
        p[0] = i < 2 ? a0[2 * (i & 1)] : i < 4 ? a1[2 * (i & 1)] : a2[0];
        p[1] = i < 2 ? a0[2 * (i & 1) + 1] : i < 4 ? a1[2 * (i & 1) + 1] : a2[1];
        p[2] = (i < 4 ? s4[i & 3] : s1) | ((i < 4 ? d4[i & 3] : d1) << 16);
      }
      meet(2);
      const rsrc_t rs_r = mkrs(ws + WS_R + (size_t)c * (CH * 12), CH * 12);
#pragma unroll
      for (int i = 0; i < 4; i++)
        __builtin_amdgcn_raw_buffer_store_b128(((const u32x4*)img)[tid + 256 * i], rs_r, tid * 16 + i * 4096, 0, 0);
    }
  } else {
    const int u = w - 4, n0 = (c * 625 + 15) >> 4, nn = (((c + 1) * 625 + 15) >> 4) - n0;
    const u32 ldsb = (u32)(size_t)(LAS char*)lds;
    {
      const u32x4 srd_h = mksrd(h + (size_t)n0 * 128, nn * 256), srd_w = mksrd(we + 128 * 128, 32768);
      const u32 vw = gq * 256 + 16 * (c16 ^ (4 * gq));
      SETM0(ldsb + u * 8192);
      DMA1(0, vw, srd_w, u * 8192); DMA1(1024, vw, srd_w, u * 8192); DMA1(2048, vw, srd_w, u * 8192); DMA1(3072, vw, srd_w, u * 8192);
      SETM0(ldsb + u * 8192 + 4096);
      DMA1(0, vw, srd_w, u * 8192 + 4096); DMA1(1024, vw, srd_w, u * 8192 + 4096); DMA1(2048, vw, srd_w, u * 8192 + 4096); DMA1(3072, vw, srd_w, u * 8192 + 4096);
      SETM0(ldsb + K1_H + u * 3072);
      DMA1(0, gq * 256 + 16 * (c16 ^ ((12 * u + gq) & 15)), srd_h, u * 3072);
      DMA1(1024, gq * 256 + 16 * (c16 ^ ((12 * u + 4 + gq) & 15)), srd_h, u * 3072);
      DMA1(2048, gq * 256 + 16 * (c16 ^ ((12 * u + 8 + gq) & 15)), srd_h, u * 3072);
    }
    VMWAIT0();
    u32 pfv = 0;
    const bool pf = blockIdx.x < 64 && u < 3;
    if (pf) {
      unsigned long long pc;
      u32 off;
      asm volatile("s_getpc_b64 %0\n.Lpc%=:\n\ts_mov_b32 %1, egnn_k2-.Lpc%=" : "=s"(pc), "=s"(off));
      asm volatile("global_load_dword %0, %1, %2" : "=v"(pfv) : "v"(min(u * 64 + lane, 147) * 128), "s"(pc + off) : "memory");
    }
    meet(0);
    f32x4 acc[3][2];
#pragma unroll
    for (int rt = 0; rt < 3; rt++) acc[rt][0] = acc[rt][1] = (f32x4)(0.f);
    const char* wb = lds + (8 * gq + (c16 >> 2)) * 256 + 16 * ((4 * u + (c16 & 3)) ^ (c16 & 12));
    const int hb = K1_H + c16 * 256 + 16 * (gq ^ c16);
    u32x4 fa[4], fb[4], hf[4][3];
#pragma unroll
    for (int s = 0; s < 4; s++) {
      const u32x2 x0 = trread(wb + (32 * s) * 256), x1 = trread(wb + (32 * s + 4) * 256);
      const u32x2 y0 = trread(wb + (32 * s) * 256 + 8), y1 = trread(wb + (32 * s + 4) * 256 + 8);
      fa[s] = (u32x4){x0[0], x0[1], x1[0], x1[1]};
      fb[s] = (u32x4){y0[0], y0[1], y1[0], y1[1]};
#pragma unroll
      for (int rt = 0; rt < 3; rt++) hf[s][rt] = *(const u32x4*)(lds + (hb ^ (64 * s)) + rt * 4096);
    }
    SB();
#pragma unroll
    for (int s = 0; s < 4; s++)
#pragma unroll
      for (int rt = 0; rt < 3; rt++) {
        acc[rt][0] = MFMA16(asbf(fa[s]), asbf(hf[s][rt]), acc[rt][0]);
        acc[rt][1] = MFMA16(asbf(fb[s]), asbf(hf[s][rt]), acc[rt][1]);
      }
    const rsrc_t rs_b = mkrs(ws + WS_B + (size_t)n0 * 256, nn * 256);
#pragma unroll
    for (int rt = 0; rt < 3; rt++) {
      const f32x4 a = acc[rt][0] * NLOG2E, b = acc[rt][1] * NLOG2E;
      const u32x4 o = {pk2(a[0], a[1]), pk2(a[2], a[3]), pk2(b[0], b[1]), pk2(b[2], b[3])};
      __builtin_amdgcn_raw_buffer_store_b128(o, rs_b, c16 * 256 + 16 * gq + 64 * u + rt * 4096, 0, 0);
    }
    if (pf) asm volatile("s_waitcnt vmcnt(0)" :: "v"(pfv) : "memory");
  }
}

constexpr int OHS = 2 * CAP + 16;
constexpr int L_NODE = 0;
constexpr int L_WX = L_NODE + 320;
constexpr int L_WS = L_WX + 512;
constexpr int L_REC = L_WS + 16;
constexpr int L_CV = L_REC + 16 * (CAP + 1);
constexpr int L_SRC = L_CV + 8 * (CAP + 2);
constexpr int L_OH = L_SRC + 2 * (CAP + 8);
constexpr int L_CNT = L_OH + 8 * OHS;
constexpr int L_HM = L_CNT + 256;
constexpr int L_IMG = L_HM + 160;
constexpr int L_AT = L_IMG + 48 * 528;
constexpr int L_STG = (L_AT + 128 * 96 + 127) & ~127;
constexpr int K2_LDS = L_STG + 65536;
constexpr int L_MG = L_STG + 32768;
static_assert(K2_LDS <= 163840, "LDS");
static_assert(L_CV % 16 == 0 && L_OH % 16 == 0 && L_SRC % 16 == 0 && L_IMG % 16 == 0 && L_AT % 16 == 0, "align");
static_assert(L_IMG - L_OH > 5 * 4096 && L_IMG - L_OH <= 6 * 4096, "zero sweep");

extern "C" __global__ __launch_bounds__(512, 2) void egnn_k2(const u16* __restrict__ h, const u16* __restrict__ pos,
                                                  const u16* __restrict__ we, const u16* __restrict__ wx,
                                                  const u16* __restrict__ wh, const char* __restrict__ ws,
                                                  u16* __restrict__ out) {
  __shared__ __attribute__((aligned(128))) char lds[K2_LDS];
  int tid = threadIdx.x, lane = tid & 63;
  const int w = __builtin_amdgcn_readfirstlane(tid >> 6);
  const int b = (blockIdx.x & 7) * 32 + (blockIdx.x >> 3);
  const int n0 = (b * 625 + 15) >> 4, nn = (((b + 1) * 625 + 15) >> 4) - n0;
  int gq = lane >> 4, c16 = lane & 15, hh = lane >> 5, n32 = lane & 31;
  const u32 ldsb = (u32)(size_t)(LAS char*)lds;
  const u32x4 srd_g = mksrd(ws + WS_B - 4096, 4096 + NN * 256), srd_pos = mksrd(pos, NN * 6);

  asm volatile("" :: "s"(h), "s"(pos), "s"(we), "s"(wx), "s"(wh), "s"(ws), "s"(out));
  u32 cwv[6];
  u32x4 hv[3], t4;
  if (w < 4) {
    LDB("dwordx4", t4, lane * 16, mksrd(ws + WS_T + b * 1024, 1024), 0);
    *(u32x2*)(lds + L_REC + tid * 8) = (u32x2)(0u);
  } else {
    const int ty = tid - 256;
    const u32x4 srd_h = mksrd(h + (size_t)n0 * 128, nn * 256), srd_we = mksrd(we, 261 * 256);
#pragma unroll
    for (int i = 0; i < 3; i++) LDB("dwordx4", hv[i], ty * 16, srd_h, i * 4096);
    if (w >= 6) {
#pragma unroll
      for (int k = 0; k < 5; k++) LDB("ushort", cwv[k], tid * 2, srd_we, (256 + k) * 256 - 768);
      LDB("ushort", cwv[5], tid * 2 - 768, mksrd(wx, 256), 0);
    } else {
      LDB("ushort", cwv[0], ty * 2, srd_pos, n0 * 6);
    }
    const u32 vw = gq * 256 + 16 * (c16 ^ (4 * gq)), so = (w - 4) * 8192;
    SETM0(ldsb + L_STG + so);
    DMA1(0, vw, srd_we, so); DMA1(1024, vw, srd_we, so); DMA1(2048, vw, srd_we, so); DMA1(3072, vw, srd_we, so);
    SETM0(ldsb + L_STG + so + 4096);
    DMA1(0, vw, srd_we, so + 4096); DMA1(1024, vw, srd_we, so + 4096); DMA1(2048, vw, srd_we, so + 4096); DMA1(3072, vw, srd_we, so + 4096);
  }
  SB();
  u32x4 id0, id1, one4 = {hh ? 0u : 0x3F80u, 0u, 0u, 0u};
  int ga[4], recoff, ohoff, cvoff, axoff, srcoff;
  u32 go[4], vsrc;
  auto lane_consts = [&]() {
    const int k0 = n32 - 8 * hh;
    const u32 one = 0x3F80u << ((k0 & 1) * 16);
#pragma unroll
    for (int i = 0; i < 4; i++) {
      id0[i] = (k0 >> 1) == i ? one : 0u;
      id1[i] = (k0 >> 1) == 8 + i ? one : 0u;
    }
    const int prow = (n32 & 19) | ((n32 & 4) << 1) | ((n32 & 8) >> 1);
    const int sa = prow & 3, sb = prow >> 2;
#pragma unroll
    for (int s = 0; s < 4; s++) ga[s] = (L_STG + w * 8192 + (8 * sa + sb) * 128 + 16 * (hh ^ (2 * sa + ((sb >> 1) & 1)))) ^ (32 * s);
#pragma unroll
    for (int j = 0; j < 4; j++) go[j] = 4096 - 1024 * j + 16 * ((lane & 7) ^ (2 * j + ((lane >> 4) & 1)));
    recoff = L_REC + prow * 16;
    ohoff = L_OH + (n32 >> 2) * OHS + 16 * hh;
    cvoff = L_CV + (8 * hh + (c16 >> 2)) * 8;
    axoff = L_OH + (c16 >> 2) * OHS + 32 * (gq & 1) + 16 * (c16 & 1) + 8 * ((c16 >> 1) & 1);
    srcoff = L_SRC + (lane >> 3) * 8;
    vsrc = (lane >> 1) * 256 + 32 * w + 16 * (lane & 1);
  };
  lane_consts();
  const u32 m0a = ldsb + L_STG + w * 8192, m0b = m0a + 4096;
  asm volatile("" : "+v"(one4), "+v"(id0), "+v"(id1), "+v"(ga[0]), "+v"(ga[1]), "+v"(ga[2]), "+v"(ga[3]), "+v"(go[0]), "+v"(go[1]), "+v"(go[2]), "+v"(go[3]));
  asm volatile("" : "+v"(recoff), "+v"(ohoff), "+v"(cvoff), "+v"(axoff), "+v"(srcoff), "+v"(vsrc));
  SB();
  int mtot, scnt, sexc;
  u32 sbase;
  auto decode = [&]() {
    if (lane == 62) t4[2] = t4[3] = 0;
    if (lane == 63) t4 = (u32x4)(0u);
    const int c0 = t4[0] >> 16, c1 = c0 + (t4[1] >> 16), c2 = c1 + (t4[2] >> 16), c3 = c2 + (t4[3] >> 16);
    const int inc = wave_incl_scan(c3);
    mtot = __builtin_amdgcn_readlane(inc, 63);
    const int wq = w & 3;
    const u32 tv = wq == 0 ? t4[0] : wq == 1 ? t4[1] : wq == 2 ? t4[2] : t4[3];
    scnt = tv >> 16;
    sexc = inc - c3 + (wq == 0 ? 0 : wq == 1 ? c0 : wq == 2 ? c1 : c2);
    sbase = ((4 * lane + wq) * CH + (tv & 0xffffu)) * 12;
  };
  if (w < 4) {
    VMWAIT0(); SB();
    decode();
    if (tid == 0) *(int*)(lds + L_WS) = mtot;
    if (mtot <= CAP) {
      u32* p = (u32*)(lds + L_CV) + sexc;
      u32 v = sbase;
      if (scnt & 1) p[scnt - 1] = v + 12 * (scnt - 1);
#pragma clang loop vectorize(disable) unroll(disable)
      for (int i = 1; i < scnt; i += 2, p += 2, v += 24) { p[0] = v; p[1] = v + 12; }
    }
  } else {
    VMWAIT0(); SB();
    asm volatile("" : "+v"(cwv[0]), "+v"(cwv[1]), "+v"(cwv[2]), "+v"(cwv[3]), "+v"(cwv[4]), "+v"(cwv[5]));
    char* hp = lds + L_IMG + ((tid - 256) >> 4) * 528 + c16 * 16;
#pragma unroll
    for (int i = 0; i < 3; i++) *(u32x4*)(hp + i * 16 * 528) = hv[i];
    if (w < 6) {
      const int t3 = tid - 256, nl = __umul24(t3, 21846) >> 16;
      if (t3 < 120) *(u16*)(lds + L_NODE + nl * 2 + t3 * 2) = (u16)cwv[0];
    }
  }
  __syncthreads();
  u32x3 rec[3];
  bool general;
  {
    const u32x4 srd_rec = mksrd(ws + WS_R, NE * 12);
    u32 ro[3];
#pragma unroll
    for (int k = 0; k < 3; k++) ro[k] = ((const u32*)(lds + L_CV))[tid + 512 * k];
    mtot = __builtin_amdgcn_readfirstlane(*(const int*)(lds + L_WS));
    general = mtot > CAP;
    const int lim = general ? 0 : mtot;
#pragma unroll
    for (int k = 0; k < 3; k++) LDB("dwordx3", rec[k], tid + 512 * k < lim ? ro[k] : 0x7fffff00u, srd_rec, 0);
  }
  if (general) asm volatile("s_waitcnt vmcnt(0)" :: "v"(rec[0]), "v"(rec[1]), "v"(rec[2]) : "memory");
  SB();
  if (w >= 6) {
    const float c0 = bf2f(cwv[0]) * NLOG2E;
    const u32 d_hi = pk2(c0, 0.f);
    const u32x4 o = {pk2(bf2f(cwv[1]) * NLOG2E, bf2f(cwv[2]) * NLOG2E), pk2(bf2f(cwv[3]) * NLOG2E, bf2f(cwv[4]) * NLOG2E),
                     pk2(c0, c0 - bf2f(d_hi)), 0u};
    *(u32x4*)(lds + L_AT + (tid - 384) * 96 + 80) = o;
    ((float*)(lds + L_WX))[tid - 384] = bf2f(cwv[5]) * NLN2;
  }
  const char* const xb = lds + L_IMG + c16 * 528 + 16 * gq;
  if (w < 4) {
    f32x4 acc[3][2];
#pragma unroll
    for (int rt = 0; rt < 3; rt++) acc[rt][0] = acc[rt][1] = (f32x4)(0.f);
    const char* wb = lds + L_STG + (8 * gq + (c16 >> 2)) * 256 + 16 * ((4 * w + (c16 & 3)) ^ (c16 & 12));
    u32x4 fa[4], fb[4], hf[4][3];
#pragma unroll
    for (int s = 0; s < 4; s++) {
      const u32x2 x0 = trread(wb + (32 * s) * 256), x1 = trread(wb + (32 * s + 4) * 256);
      const u32x2 y0 = trread(wb + (32 * s) * 256 + 8), y1 = trread(wb + (32 * s + 4) * 256 + 8);
      fa[s] = (u32x4){x0[0], x0[1], x1[0], x1[1]};
      fb[s] = (u32x4){y0[0], y0[1], y1[0], y1[1]};
#pragma unroll
      for (int rt = 0; rt < 3; rt++) hf[s][rt] = *(const u32x4*)(xb + rt * 8448 + 64 * s);
    }
    SB();
#pragma unroll
    for (int s = 0; s < 4; s++)
#pragma unroll
      for (int rt = 0; rt < 3; rt++) {
        acc[rt][0] = MFMA16(asbf(hf[s][rt]), asbf(fa[s]), acc[rt][0]);
        acc[rt][1] = MFMA16(asbf(hf[s][rt]), asbf(fb[s]), acc[rt][1]);
      }
    char* ap = lds + L_AT + (32 * w + 8 * (c16 >> 2) + (c16 & 3)) * 96 + 8 * gq;
#pragma unroll
    for (int rt = 0; rt < 3; rt++)
      if (16 * rt + 4 * gq < 40)
#pragma unroll
        for (int j = 0; j < 2; j++) {
          const f32x4 a = acc[rt][j] * (NLOG2E / 1.0078125f);
          *(u32x2*)(ap + j * 384 + rt * 32) = (u32x2){pk2(a[0], a[1]), pk2(a[2], a[3])};
        }
  }
  if (w >= 4) {
    const int ty = tid - 256;
#pragma unroll
    for (int i = 0; i < 5; i++) *(u32x4*)(lds + L_OH + ty * 16 + i * 4096) = (u32x4)(0u);
    if (ty < (L_IMG - L_OH - 5 * 4096) / 16) *(u32x4*)(lds + L_OH + ty * 16 + 5 * 4096) = (u32x4)(0u);
    char* ip = lds + L_IMG + (ty >> 4) * 528 + 256 + c16 * 16;
#pragma unroll
    for (int i = 0; i < 3; i++) *(u32x4*)(ip + i * 16 * 528) = (u32x4)(0u);
    if (ty < 48) *(u32x4*)(lds + L_IMG + ty * 528 + 512) = (u32x4)(0u);
  }
  f32x16 Y[4], XA, XB, e;
  u32x4 sp0 = (u32x4)(0u), sp1 = (u32x4)(0u), pa = (u32x4)(0u), pb = (u32x4)(0u);
  asm volatile("" : "+v"(sp0), "+v"(sp1), "+v"(pa), "+v"(pb));
  Y[0] = MFMA32(asbf(pa), asbf(pa), (f32x16)(0.f));
  Y[1] = MFMA32(asbf(pb), asbf(pb), (f32x16)(0.f));
  Y[2] = MFMA32(asbf(sp0), asbf(sp0), (f32x16)(0.f));
  Y[3] = MFMA32(asbf(sp1), asbf(sp1), (f32x16)(0.f));
  XB = MFMA32(asbf(pa), asbf(pb), (f32x16)(0.f));
  asm volatile("" : "+v"(Y[0]), "+v"(Y[1]), "+v"(Y[2]), "+v"(Y[3]), "+v"(XB));
  SB();
  u32x4 wex[4];
  u32 ad[4];
  int ninc;
  u32x4 srd = srd_g;
  u32 sfb = 128;
  auto prep = [&](u32x2 sv, bool real) {
    if (real) {
      asm("v_mad_u32_u16 %0, %1, %2, %3" : "=v"(ad[0]) : "v"(sv[0]), "s"(256), "v"(go[0]));
      asm("v_mad_u32_u16 %0, %1, %2, %3 op_sel:[1,0,0,0]" : "=v"(ad[1]) : "v"(sv[0]), "s"(256), "v"(go[1]));
      asm("v_mad_u32_u16 %0, %1, %2, %3" : "=v"(ad[2]) : "v"(sv[1]), "s"(256), "v"(go[2]));
      asm("v_mad_u32_u16 %0, %1, %2, %3 op_sel:[1,0,0,0]" : "=v"(ad[3]) : "v"(sv[1]), "s"(256), "v"(go[3]));
    } else {
#pragma unroll
      for (int j = 0; j < 4; j++) ad[j] = vsrc + 7168 * j;
      srd = mksrd(wh, 65536);
      sfb = 32768;
    }
  };
  auto issue_a = [&]() { SETM0(m0a); DMA1(0, ad[0], srd, 0); DMA1(1024, ad[1], srd, 0); DMA1(2048, ad[2], srd, 0); DMA1(3072, ad[3], srd, 0); };
  auto issue_b = [&]() { SETM0(m0b); DMA1(0, ad[0], srd, sfb); DMA1(1024, ad[1], srd, sfb); DMA1(2048, ad[2], srd, sfb); DMA1(3072, ad[3], srd, sfb); };
  auto node_at = [&](int x) { return (int)__builtin_popcountll(__builtin_amdgcn_ballot_w64(ninc <= x)); };
  auto loadw = [&](int base) {
    const int g0 = base >> 3, bm = base & 7;
    const char* pa = lds + L_AT + n32 * 96 + (hh ? g0 * 16 : 80);
    const char* pb = lds + L_AT + n32 * 96 + (hh ? g0 * 16 + 16 : 80);
    const unsigned long long lo = ~0ull << (16 * min(bm, 3)) << (bm > 3 ? 16 : 0), hi = bm > 4 ? ~0ull << (16 * (bm - 4)) : ~0ull;
    const u32 mk[4] = {(u32)lo, (u32)(lo >> 32), (u32)hi, (u32)(hi >> 32)};
#pragma unroll
    for (int t = 0; t < 4; t++) {
      const u32x4 a = *(const u32x4*)(pa + t * 3072), bb = *(const u32x4*)(pb + t * 3072);
#pragma unroll
      for (int i = 0; i < 4; i++) wex[t][i] = (a[i] & mk[i]) | (bb[i] & ~mk[i]);
    }
  };
  auto pdot = [&](auto nbc) {
    constexpr int nb = decltype(nbc)::value;
    f32x4 p4[2] = {(f32x4)(0.f), (f32x4)(0.f)};
#pragma unroll
    for (int q0 = 0; q0 < 16; q0 += nb) {
      f32x4 wv[16];
#pragma unroll
      for (int q = q0; q < q0 + nb; q++) wv[q] = *(const f32x4*)(lds + L_WX + (8 * q + 4 * hh) * 4);
      SB();
#pragma unroll
      for (int q = q0; q < q0 + nb; q++)
        p4[q & 1] += (f32x4){Y[q >> 2][4 * (q & 3)], Y[q >> 2][4 * (q & 3) + 1], Y[q >> 2][4 * (q & 3) + 2], Y[q >> 2][4 * (q & 3) + 3]} * wv[q];
      SB();
    }
    p4[0] += p4[1];
    const f32x2 pa = (f32x2){p4[0][0], p4[0][1]} + (f32x2){p4[0][2], p4[0][3]};
    const u32 ph = __float_as_uint(pa[0] + pa[1]);
    const auto sw = __builtin_amdgcn_permlane32_swap(ph, ph, false, false);
    return __uint_as_float(sw[0]) + __uint_as_float(sw[1]);
  };
  auto place = [&](u32x3 rec, u32x2 ps, u32x2 pd, u32 dl, u32 p) {
    const u32 sh = (rec[2] & 1) * 16;
    const u32 sxy = __builtin_amdgcn_alignbit(ps[1], ps[0], sh), sz = ps[1] >> sh;
    const u32 dxy = pk2(bf2f(pd[0]) - bf2f(sxy), bfhi(pd[0]) - bfhi(sxy));
    const u32 dz1 = pk2(bf2f(pd[1]) - bf2f(sz), 1.0f);
    const float fx = bf2f(dxy), fy = bfhi(dxy), fz = bf2f(dz1);
    const float d2 = fx * fx + fy * fy + fz * fz;
    *(u32x4*)(lds + L_REC + p * 16) = (u32x4){rec[0], rec[1], pk2(d2, d2), dl};
    *(u32x2*)(lds + L_CV + p * 8) = (u32x2){mul16x2(dxy, OHINV * 0x10001u), mul16x2(dz1, OHINV * 0x10001u)};
    *(u16*)(lds + L_OH + (dl & 7) * OHS + 2 * p) = (u16)OH;
  };
  auto count_nodes = [&](int m, int cn) {
    ninc = wave_incl_scan(cn);
    if (w == 7) {
      const int p = m + n32 < ((m + 31) & ~31) ? m + n32 : CAP;
      *(u32x4*)(lds + L_REC + p * 16) = (u32x4)(0u);
      ((u16*)(lds + L_SRC))[p] = 0;
    }
    return ninc - cn;
  };
#define RDG(s, half) (*(const u32x4*)(lds + ga[s] + (half) * 4096))
#define EXP_(lo, hi) _Pragma("unroll") for (int r = lo; r < hi; r++) e[r] = __builtin_amdgcn_exp2f(xc[r]); SB()
#define RCP_(lo, hi) _Pragma("unroll") for (int r = lo; r < hi; r++) e[r] = __builtin_amdgcn_rcpf(e[r]); SB()
#define PACK_() _Pragma("unroll") for (int i = 0; i < 4; i++) { pa[i] = pk2(e[2 * i], e[2 * i + 1]); pb[i] = pk2(e[8 + 2 * i], e[8 + 2 * i + 1]); } \
  asm volatile("" : "+v"(pa), "+v"(pb)); SB()
#define SPROD(o, ca, cb) (u32x4){mul16x2(o[0], ca[0]), mul16x2(o[1], ca[1]), mul16x2(o[2], cb[0]), mul16x2(o[3], cb[1])}

  const int m = mtot;
  const bool third = w * 64 + 1024 < m;
  u32 rk[3], dl[3];
  u32x2 ps[3], pd[3];
  if (!general) {
    VMWAIT0(); SB();
#pragma unroll
    for (int k = 0; k < 3; k++)
      if (k < 2 || third) {
        LDB("dwordx2", ps[k], ((rec[k][2] & 0xffffu) * 6) & ~3u, srd_pos, 0);
        dl[k] = tid + 512 * k < m ? (rec[k][2] >> 16) - n0 : 48u + c16;
        rk[k] = atomicAdd((u32*)(lds + L_REC) + w * 64 + dl[k], 1u);
        pd[k] = *(const u32x2*)(lds + L_NODE + dl[k] * 8);
      }
    __syncthreads();
    int nst;
    {
      u32 cw[8], pre = 0;
#pragma unroll
      for (int j = 0; j < 8; j++) cw[j] = ((const u32*)(lds + L_REC))[j * 64 + lane];
      nst = count_nodes(m, (cw[0] + cw[1] + cw[2]) + (cw[3] + cw[4] + cw[5]) + (cw[6] + cw[7]));
#pragma unroll
      for (int j = 0; j < 7; j++) asm("v_mad_u32_u24 %0, %1, %2, %3" : "=v"(pre) : "v"(cw[j]), "s"(j < w ? 1 : 0), "v"(pre));
      nst += pre;
    }
    const int nprev = __builtin_amdgcn_ds_bpermute(lane * 4 - 28, ninc);
#pragma unroll
    for (int k = 0; k < 3; k++)
      if (k < 2 || third) {
        rk[k] += __builtin_amdgcn_ds_bpermute(dl[k] * 4, nst);
        rk[k] = dl[k] < 48 ? rk[k] : CAP;
        *(u16*)(lds + L_SRC + 2 * rk[k]) = (u16)rec[k][2];
      }
    general = m < 256 || __builtin_amdgcn_ballot_w64(lane >= 7 && lane < nn && ninc - nprev <= 30) != 0;
    if (general) asm volatile("s_waitcnt vmcnt(0)" :: "v"(ps[0]), "v"(ps[1]), "v"(ps[2]) : "memory");
  }
  if (!general) {
    {
      const int nt = (m + 31) >> 5, tq = nt >> 3, trm = nt & 7;
      const int tbeg = w * tq + min(w, trm);
      const int tend = tbeg + tq + (w < trm ? 1 : 0);
      srcoff += tbeg * 64;
      __syncthreads();
      const u32x2 sv0 = *(const u32x2*)(lds + srcoff), sv1 = *(const u32x2*)(lds + srcoff + 64);
      prep(sv0, true);
      if (w < 4) {
        issue_a();
        issue_b();
        VMWAIT(8);
      } else {
        VMWAIT(0);
      }
      SB();
      int base = node_at(tbeg * 32);
      loadw(base);
      const int lnode = node_at(min(tend * 32, m) - 1);
      const int hnode = (tbeg > 0 && node_at(tbeg * 32 - 1) == base) ? base : -1;
      if (hnode >= 0 && lane == 0) __hip_atomic_fetch_or((u32*)(lds + L_HM) + hnode, 1u << w, __ATOMIC_RELAXED, __HIP_MEMORY_SCOPE_WORKGROUP);
      recoff += tbeg * 512; cvoff += tbeg * 256 - 256; ohoff += tbeg * 64 - 64; axoff += tbeg * 64;
#pragma unroll
      for (int k = 0; k < 3; k++)
        if (k < 2 || third) place(rec[k], ps[k], pd[k], dl[k], rk[k]);
      if (w >= 4) {
        issue_a();
        issue_b();
      }
      prep(sv1, tbeg + 1 < tend);
#pragma unroll
      for (int t = 0; t < 4; t++) asm volatile("" : "+v"(wex[t]));
      auto flush = [&](int base, int lim, auto clearc) {
        const int node = base + (((n32 >> 2) - base) & 7), comp = n32 & 3;
        constexpr bool clear = decltype(clearc)::value != 0;
        const float pt = pdot(IC<(clear ? 2 : 16)>{});
        if (node < lim) {
          char* mp = lds + L_IMG + (node == hnode ? 39 + w : node) * 528;
          if (comp == 3) {
#pragma unroll
            for (int q = 0; q < 16; q++)
              *(u32x2*)(mp + 256 + 8 * hh + 16 * q) = (u32x2){pk2(Y[q >> 2][4 * (q & 3)], Y[q >> 2][4 * (q & 3) + 1]),
                                                              pk2(Y[q >> 2][4 * (q & 3) + 2], Y[q >> 2][4 * (q & 3) + 3])};
          } else {
            *(float*)(mp + 512 + 4 * comp) = pt;
          }
        }
        if (clear) {
          const float keep = node < lim ? 0.f : 1.f;
#pragma unroll
          for (int t = 0; t < 4; t++) Y[t] *= keep;
        }
      };
      __syncthreads();
      u32x4 aex;
      {
        const u32x2 t0 = trread(lds + axoff), t1 = trread(lds + axoff + 4 * OHS);
        const u32x4 ra = *(const u32x4*)(lds + recoff);
        aex = hh ? (u32x4){t0[0], t0[1], t1[0], t1[1]} : ra;
      }
      XA = MFMA32(asbf(aex), asbf(wex[0]), (f32x16)(0.f)); PIN(XA);
      VMWAIT(4);
      u32x4 g0 = RDG(0, 0), g1 = RDG(1, 0), g2 = RDG(2, 0), g3 = RDG(3, 0);
      LGKMWAIT0();
      SB();
      XA = MFMA32(asbf(g0), asbf(id0), XA); PIN(XA);
      issue_a();
      XA = MFMA32(asbf(g1), asbf(id1), XA); PIN(XA);
      int fbase = base;
      __builtin_amdgcn_s_setprio(3);
#define EXPX(X, lo, hi) _Pragma("unroll") for (int r = lo; r < hi; r++) e[r] = __builtin_amdgcn_exp2f(X[r]); SB()
#define PACKX(X) _Pragma("unroll") for (int i = 0; i < 4; i++) { pa[i] = pk2(X[2 * i], X[2 * i + 1]); pb[i] = pk2(X[8 + 2 * i], X[8 + 2 * i + 1]); } \
  asm volatile("" : "+v"(pa), "+v"(pb)); SB()
#define SEG(t, p, sp) Y[t] = MFMA32(asbf(p), asbf(sp), Y[t]); PIN(Y[t])
#define PLUS1() e = MFMA32(asbf(one4), asbf(one4), e); PIN(e)
      for (int left = tend - tbeg, x = tbeg * 32 + 32; left > 0; left--, x += 32) {
        if (__builtin_expect(left == 1, 0)) __builtin_amdgcn_s_setprio(1);
        SB();
        SEG(2, pa, sp0);
        EXPX(XA, 0, 8);
        SEG(2, pb, sp1);
        EXPX(XA, 8, 16);
        PLUS1();
        PACKX(XB);
        srcoff += 64; recoff += 512; cvoff += 256; ohoff += 64; axoff += 64;
        asm volatile("" : "+v"(srcoff), "+v"(recoff), "+v"(cvoff), "+v"(ohoff), "+v"(axoff)); SB();
        RCP_(0, 4);
        XB = MFMA32(asbf(aex), asbf(wex[1]), (f32x16)(0.f)); PIN(XB);
        RCP_(4, 8);
        XB = MFMA32(asbf(g2), asbf(id0), XB); PIN(XB);
        RCP_(8, 12);
        XB = MFMA32(asbf(g3), asbf(id1), XB); PIN(XB);
        RCP_(12, 16);
        XA = XA * e; PIN(XA);
        SEG(3, pa, sp0);
        VMWAIT(4);
        g0 = RDG(0, 1); g1 = RDG(1, 1); g2 = RDG(2, 1); g3 = RDG(3, 1);
        const u32x2 sv = *(const u32x2*)(lds + srcoff + 64);
        SB();
        EXPX(XB, 0, 8);
        SEG(3, pb, sp1);
        const u32x2 c0 = trread(lds + cvoff), c1 = trread(lds + cvoff + 32), c2 = trread(lds + cvoff + 128), c3 = trread(lds + cvoff + 160);
        const u32x4 o0 = *(const u32x4*)(lds + ohoff), o1 = *(const u32x4*)(lds + ohoff + 32);
        SB();
        EXPX(XB, 8, 16);
        PLUS1();
        PACKX(XA);
        sp0 = SPROD(o0, c0, c1);
        sp1 = SPROD(o1, c2, c3);
        asm volatile("" : "+v"(sp0), "+v"(sp1));
        LGKMWAIT0();
        SB();
        SETM0(m0b); DMA1(0, ad[0], srd, sfb); SB();
        RCP_(0, 4);
        XA = MFMA32(asbf(aex), asbf(wex[2]), (f32x16)(0.f)); PIN(XA);
        DMA1(1024, ad[1], srd, sfb); SB();
        RCP_(4, 8);
        XA = MFMA32(asbf(g0), asbf(id0), XA); PIN(XA);
        DMA1(2048, ad[2], srd, sfb); SB();
        RCP_(8, 12);
        XA = MFMA32(asbf(g1), asbf(id1), XA); PIN(XA);
        DMA1(3072, ad[3], srd, sfb); SB();
        RCP_(12, 16);
        XB = XB * e; PIN(XB);
        if (__builtin_expect(fbase != base, 0)) {
          flush(fbase, base, IC<1>{});
          fbase = base;
        }
        if (__builtin_expect(left <= 2, 0)) {
          if (left == 2) __builtin_amdgcn_s_setprio(2);
          else __builtin_amdgcn_s_setprio(0);
          prep(sv, false);
        } else {
          prep(sv, true);
        }
        asm volatile("" : "+v"(ad[0]), "+v"(ad[1]), "+v"(ad[2]), "+v"(ad[3]));
        SB();
        SEG(0, pa, sp0);
        const int nlast = node_at(min(x + 31, m - 1));
        SB();
        EXPX(XA, 0, 8);
        SEG(0, pb, sp1);
        EXPX(XA, 8, 16);
        PLUS1();
        PACKX(XB);
        RCP_(0, 4);
        XB = MFMA32(asbf(aex), asbf(wex[3]), (f32x16)(0.f)); PIN(XB);
        RCP_(4, 8);
        XB = MFMA32(asbf(g2), asbf(id0), XB); PIN(XB);
        RCP_(8, 12);
        XB = MFMA32(asbf(g3), asbf(id1), XB); PIN(XB);
        RCP_(12, 16);
        XA = XA * e; PIN(XA);
        if (__builtin_expect(left > 1 && nlast >= base + 8, 0)) {
          base = node_at(x);
          loadw(base);
        }
        SB();
        SEG(1, pa, sp0);
        VMWAIT(4);
        const u32x2 t0 = trread(lds + axoff), t1 = trread(lds + axoff + 4 * OHS);
        const u32x4 ra = *(const u32x4*)(lds + recoff);
        g0 = RDG(0, 0); g1 = RDG(1, 0); g2 = RDG(2, 0); g3 = RDG(3, 0);
        SB();
        EXPX(XB, 0, 8);
        SEG(1, pb, sp1);
        EXPX(XB, 8, 16);
        PLUS1();
        PACKX(XA);
        LGKMWAIT0();
        SB();
        aex = hh ? (u32x4){t0[0], t0[1], t1[0], t1[1]} : ra;
        PIN(aex);
        SETM0(m0a); DMA1(0, ad[0], srd, 0); SB();
        RCP_(0, 4);
        XA = MFMA32(asbf(aex), asbf(wex[0]), (f32x16)(0.f)); PIN(XA);
        DMA1(1024, ad[1], srd, 0); SB();
        RCP_(4, 8);
        XA = MFMA32(asbf(g0), asbf(id0), XA); PIN(XA);
        DMA1(2048, ad[2], srd, 0); SB();
        RCP_(8, 12);
        XA = MFMA32(asbf(g1), asbf(id1), XA); PIN(XA);
        DMA1(3072, ad[3], srd, 0); SB();
        RCP_(12, 16);
        XB = XB * e; PIN(XB);
      }
      SEG(2, pa, sp0);
      SEG(2, pb, sp1);
      PACKX(XB);
      SEG(3, pa, sp0);
      SEG(3, pb, sp1);
      flush(base, lnode + 1, IC<0>{});
    }
  } else {
    tid = threadIdx.x;
    asm volatile("" : "+v"(tid));
    lane = tid & 63; gq = lane >> 4; c16 = lane & 15; hh = lane >> 5; n32 = lane & 31;
    lane_consts();
    t4 = *(const u32x4*)(ws + WS_T + b * 1024 + lane * 16);
    decode();
    const rsrc_t rs_rec = mkrs(ws + WS_R, NE * 12), rs_pos = mkrs(pos, NN * 6);
    for (int q = tid; q < 40 * 33; q += 512) *(u32x4*)(lds + L_MG + q * 16) = (u32x4)(0u);
    for (int r0 = 0; r0 < mtot; r0 += CAP) {
      const int m = min(mtot - r0, CAP);
      __syncthreads();
      for (int q = tid; q < (L_HM - L_OH) / 16; q += 512) *(u32x4*)(lds + L_OH + q * 16) = (u32x4)(0u);
      const int lim = min(scnt, r0 + CAP - sexc);
      for (int i = max(r0 - sexc, 0) + (w >> 2); i < lim; i += 2)
        *(u32x3*)(lds + L_STG + (sexc + i - r0) * 16) = __builtin_amdgcn_raw_buffer_load_b96(rs_rec, sbase + i * 12, 0, 0);
      __syncthreads();
      for (int f = tid; f < m; f += 512) {
        u32* rp = (u32*)(lds + L_STG + f * 16);
        rp[3] = atomicAdd((u32*)(lds + L_CNT) + ((rp[2] >> 16) - n0), 1u);
      }
      __syncthreads();
      const int nst = count_nodes(m, ((const u32*)(lds + L_CNT))[lane]);
      for (int f0 = w * 64; f0 < m; f0 += 512) {
        const bool ok = f0 + lane < m;
        const u32x4 rc = *(const u32x4*)(lds + L_STG + (f0 + lane) * 16);
        const u32 dl = ok ? (rc[2] >> 16) - n0 : 0u;
        const u32 pp = rc[3] + __builtin_amdgcn_ds_bpermute(dl * 4, nst), p = ok ? pp : CAP;
        const u32x2 ps = __builtin_amdgcn_raw_buffer_load_b64(rs_pos, ((rc[2] & 0xffffu) * 6) & ~3u, 0, 0);
        *(u16*)(lds + L_SRC + 2 * p) = (u16)rc[2];
        place((u32x3){rc[0], rc[1], rc[2]}, ps, *(const u32x2*)(lds + L_NODE + dl * 8), dl, p);
      }
      __syncthreads();
      const int nt = w < 4 ? (m + 31) >> 5 : 0;
      for (int tile = w; tile < nt; tile += 4) {
        const int last = node_at(min(tile * 32 + 31, m - 1));
        prep(*(const u32x2*)(lds + srcoff + tile * 64), true);
        SETM0(m0a); DMA1(0, ad[0], srd_g, 0); DMA1(1024, ad[1], srd_g, 0); DMA1(2048, ad[2], srd_g, 0); DMA1(3072, ad[3], srd_g, 0);
        SETM0(m0b); DMA1(0, ad[0], srd_g, 128); DMA1(1024, ad[1], srd_g, 128); DMA1(2048, ad[2], srd_g, 128); DMA1(3072, ad[3], srd_g, 128);
        const u32x2 t0 = trread(lds + axoff + tile * 64), t1 = trread(lds + axoff + tile * 64 + 4 * OHS);
        const u32x4 ra = *(const u32x4*)(lds + recoff + tile * 512);
        const u32x4 aex = hh ? (u32x4){t0[0], t0[1], t1[0], t1[1]} : ra;
        for (int base = node_at(tile * 32); base <= last; base += 8) {
          loadw(base);
          u32x4 sp[2];
#pragma unroll
          for (int s = 0; s < 2; s++) {
            const u32x2 c0 = trread(lds + cvoff + tile * 256 + s * 128), c1 = trread(lds + cvoff + tile * 256 + s * 128 + 32);
            const u32x4 oh = *(const u32x4*)(lds + ohoff + tile * 64 + 32 * s);
            sp[s] = SPROD(oh, c0, c1);
          }
#pragma unroll 1
          for (int j = 0; j < 16; j++) {
            const int dn = *(const u32*)(lds + L_REC + (tile * 32 + 16 * (j >> 3) + 8 * hh + (j & 7)) * 16 + 12);
            const u32 mk = (dn < base || dn >= base + 8) ? ((j & 1) ? 0x0000ffffu : 0xffff0000u) : 0xffffffffu;
#pragma unroll
            for (int i = 0; i < 8; i++) sp[i >> 2][i & 3] &= (i == (j >> 1)) ? mk : 0xffffffffu;
          }
          VMWAIT0();
#pragma unroll
          for (int t = 0; t < 4; t++) {
            const u32x4 ga_ = RDG((2 * t) & 3, t >> 1), gb_ = RDG((2 * t + 1) & 3, t >> 1);
            f32x16 xc = MFMA32(asbf(aex), asbf(wex[t]), (f32x16)(0.f)), e;
            xc = MFMA32(asbf(ga_), asbf(id0), xc);
            xc = MFMA32(asbf(gb_), asbf(id1), xc);
            u32x4 pa, pb;
            EXP_(0, 16);
            e = e + 1.0f;
            RCP_(0, 16);
            e = e * xc;
            PACK_();
            Y[t] = MFMA32(asbf(pa), asbf(sp[0]), (f32x16)(0.f));
            Y[t] = MFMA32(asbf(pb), asbf(sp[1]), Y[t]);
          }
          const int node = base + (((n32 >> 2) - base) & 7), comp = n32 & 3;
          const float pt = pdot(IC<4>{});
          if (node <= last) {
            float* mp = (float*)(lds + L_MG) + node * 132;
            if (comp == 3) {
#pragma unroll
              for (int t = 0; t < 4; t++)
#pragma unroll
                for (int r = 0; r < 16; r++) atomicAdd(mp + 4 * hh + 32 * t + 8 * (r >> 2) + (r & 3), Y[t][r]);
            } else if (hh == 0) {
              atomicAdd(mp + 128 + comp, pt);
            }
          }
        }
        LGKMWAIT0();
      }
    }
    __syncthreads();
    for (int q = tid; q < 40 * 66; q += 512) {
      const int row = q / 66, c = q - row * 66;
      const f32x2 v = *(const f32x2*)(lds + L_MG + row * 528 + c * 8);
      if (c < 64) *(u32*)(lds + L_IMG + row * 528 + 256 + c * 4) = pk2(v[0], v[1]);
      else *(f32x2*)(lds + L_IMG + row * 528 + c * 8) = v;
    }
    __syncthreads();
    const u32x4 srd_wh = mksrd(wh, 65536);
    SETM0(m0a); DMA1(0, vsrc, srd_wh, 0); DMA1(1024, vsrc, srd_wh, 7168); DMA1(2048, vsrc, srd_wh, 14336); DMA1(3072, vsrc, srd_wh, 21504);
    SETM0(m0b); DMA1(0, vsrc, srd_wh, 32768); DMA1(1024, vsrc, srd_wh, 39936); DMA1(2048, vsrc, srd_wh, 47104); DMA1(3072, vsrc, srd_wh, 54272);
    VMWAIT0();
  }

  int tid2 = threadIdx.x;
  asm volatile("" : "+v"(tid2));
  const int c16e = tid2 & 15, gqe = (tid2 >> 4) & 3;
  const char* const xbe = lds + L_IMG + c16e * 528 + 16 * gqe;
  f32x4 acc[3], acm[3];
  const char* const wfp = lds + L_STG + w * 8192 + (8 * gqe + (c16e >> 2)) * 32 + 8 * (c16e & 3);
  VMWAIT(4);
  u32x4 wf2[4];
#pragma unroll
  for (int rt = 0; rt < 3; rt++) acc[rt] = acm[rt] = (f32x4)(0.f);
  {
    u32x4 wf1[4], xf[4][3];
#pragma unroll
    for (int s = 0; s < 4; s++) {
      const u32x2 x0 = trread(wfp + s * 1024), x1 = trread(wfp + s * 1024 + 128);
      const u32x2 y0 = trread(wfp + 4096 + s * 1024), y1 = trread(wfp + 4096 + s * 1024 + 128);
      wf1[s] = (u32x4){x0[0], x0[1], x1[0], x1[1]};
      wf2[s] = (u32x4){y0[0], y0[1], y1[0], y1[1]};
#pragma unroll
      for (int rt = 0; rt < 3; rt++) xf[s][rt] = *(const u32x4*)(xbe + rt * 8448 + 64 * s);
    }
    SB();
#pragma unroll
    for (int s = 0; s < 4; s++)
#pragma unroll
      for (int rt = 0; rt < 3; rt++) acc[rt] = MFMA16(asbf(wf1[s]), asbf(xf[s][rt]), acc[rt]);
  }
  u32x2 hr[3];
  u32 hm[3], hl[3];
#pragma unroll
  for (int rt = 0; rt < 3; rt++) {
    hr[rt] = *(const u32x2*)(lds + L_IMG + c16e * 528 + 8 * gqe + rt * 8448 + 32 * w);
    hm[rt] = 16 * rt + c16e < 40 ? ((const u32*)(lds + L_HM))[16 * rt + c16e] : 0u;
    hl[rt] = 64 * gqe + 28 + 4 * __builtin_ctz(hm[rt] | 256u);
    hm[rt] &= hm[rt] - 1;
  }
  __syncthreads();
  {
    u32x4 xf[3][4];
#pragma unroll
    for (int s = 0; s < 4; s++)
#pragma unroll
      for (int rt = 2; rt >= 0; rt--) xf[rt][s] = *(const u32x4*)(xbe + 256 + rt * 8448 + 64 * s);
    SB();
#pragma unroll
    for (int s = 0; s < 4; s++)
#pragma unroll
      for (int rt = 2; rt >= 0; rt--) acm[rt] = MFMA16(asbf(wf2[s]), asbf(xf[rt][s]), acm[rt]);
  }
  const f32x4 hd = acm[2];
  for (;;) {
#pragma unroll
    for (int rt = 0; rt < 3; rt++)
#pragma unroll
      for (int r = 0; r < 4; r++) {
        const float hv_ = hd[r];
        acm[rt][r] += __uint_as_float(__builtin_amdgcn_ds_bpermute(hl[rt], __float_as_uint(hv_)));
      }
    if (__builtin_expect(__builtin_amdgcn_ballot_w64((hm[0] | hm[1] | hm[2]) != 0) == 0, 1)) break;
#pragma unroll
    for (int rt = 0; rt < 3; rt++) {
      hl[rt] = 64 * gqe + 28 + 4 * __builtin_ctz(hm[rt] | 256u);
      hm[rt] &= hm[rt] - 1;
    }
  }
  {
    const rsrc_t rs_o = mkrs(out + (size_t)n0 * 128, nn * 256);
#pragma unroll
    for (int rt = 0; rt < 3; rt++) {
      u32 o[2];
#pragma unroll
      for (int r = 0; r < 2; r++) {
        const f32x2 z = (f32x2){acc[rt][2 * r], acc[rt][2 * r + 1]} + (f32x2){acm[rt][2 * r], acm[rt][2 * r + 1]} * NLN2;
        const f32x2 t = z * NLOG2E;
        const f32x2 d = (f32x2){__builtin_amdgcn_exp2f(t[0]), __builtin_amdgcn_exp2f(t[1])} + 1.0f;
        const f32x2 v = z * (f32x2){__builtin_amdgcn_rcpf(d[0]), __builtin_amdgcn_rcpf(d[1])} + (f32x2){bf2f(hr[rt][r]), bfhi(hr[rt][r])};
        o[r] = pk2(v[0], v[1]);
      }
      __builtin_amdgcn_raw_buffer_store_b64((u32x2){o[0], o[1]}, rs_o, c16e * 256 + 8 * gqe, rt * 4096 + 32 * w, 0);
    }
  }
  if (w < 2) {
    const int nl = __umul24(tid2, 21846) >> 16, k = tid2 - 3 * nl;
    if (tid2 < 3 * nn) {
      const char* mp = lds + L_IMG + 512 + 4 * k;
      u32 hq = ((const u32*)(lds + L_HM))[nl];
      float pv = *(const float*)(mp + nl * 528) + bf2f(*(const u16*)(lds + L_NODE + nl * 2 + tid2 * 2));
      while (hq) {
        pv += *(const float*)(mp + (39 + __builtin_ctz(hq)) * 528);
        hq &= hq - 1;
      }
      out[(size_t)NN * 128 + n0 * 3 + tid2] = (u16)pk2(pv, 0.f);
    }
  }
  VMWAIT0();
}

extern "C" void kernel_launch(void* const* d_in, const int* in_sizes, int n_in, void* d_out, int out_size,
                              void* d_ws, size_t ws_size, hipStream_t stream) {
  const u16* h = (const u16*)d_in[0];
  const u16* pos = (const u16*)d_in[1];
  const u16* ea = (const u16*)d_in[2];
  const int* src = (const int*)d_in[3];
  const int* dst = (const int*)d_in[4];
  const u16* we = (const u16*)d_in[5];
  const u16* wx = (const u16*)d_in[6];
  const u16* wh = (const u16*)d_in[7];
  egnn_k1<<<NB, 512, 0, stream>>>(h, ea, src, dst, we, (char*)d_ws);
  egnn_k2<<<NB, 512, 0, stream>>>(h, pos, we, wx, wh, (const char*)d_ws, (u16*)d_out);
}
```

```cpp
#include <hip/hip_runtime.h>
#include <hip/hip_bf16.h>

typedef unsigned short u16;
typedef unsigned int u32;
typedef float f32x2 __attribute__((ext_vector_type(2)));
typedef float f32x4 __attribute__((ext_vector_type(4)));
typedef float f32x16 __attribute__((ext_vector_type(16)));
typedef __bf16 bf16x8 __attribute__((ext_vector_type(8)));
typedef __bf16 bf16x2 __attribute__((ext_vector_type(2)));
typedef u32 u32x2 __attribute__((ext_vector_type(2)));
typedef u32 u32x3 __attribute__((ext_vector_type(3)));
typedef u32 u32x4 __attribute__((ext_vector_type(4)));
typedef short s16x4 __attribute__((ext_vector_type(4)));
typedef u16 u16x2 __attribute__((ext_vector_type(2)));
#define LAS __attribute__((address_space(3)))
typedef __amdgpu_buffer_rsrc_t rsrc_t;

constexpr int TSTR = 32768;
constexpr int NN = 10000;
constexpr int NE = 320000;
constexpr int NCH = 250;
constexpr int CH = 1280;
constexpr int NB = 256;
constexpr int CAP = 1408;
constexpr float NLOG2E = -1.4426950408889634f;
constexpr float NLN2 = -0.6931471805599453f;
constexpr u32 OH = 0x3F81u;
constexpr u32 OHINV = 0x81u;

constexpr size_t WS_T = 0;
constexpr size_t WS_R = WS_T + (size_t)NB * 256 * 4;
constexpr size_t WS_B = WS_R + (size_t)NE * 12;

__device__ __forceinline__ float bf2f(u32 v) { return __uint_as_float(v << 16); }
__device__ __forceinline__ float bfhi(u32 v) { return __uint_as_float(v & 0xffff0000u); }
__device__ __forceinline__ u32 pk2(float a, float b) {
  f32x2 v = {a, b};
  return __builtin_bit_cast(u32, __builtin_convertvector(v, bf16x2));
}
__device__ __forceinline__ u32 mul16x2(u32 a, u32 b) {
  return __builtin_bit_cast(u32, __builtin_bit_cast(u16x2, a) * __builtin_bit_cast(u16x2, b));
}
__device__ __forceinline__ int wave_incl_scan(int x) {
  x += __builtin_amdgcn_update_dpp(0, x, 0x111, 0xf, 0xf, false);
  x += __builtin_amdgcn_update_dpp(0, x, 0x112, 0xf, 0xf, false);
  x += __builtin_amdgcn_update_dpp(0, x, 0x114, 0xf, 0xf, false);
  x += __builtin_amdgcn_update_dpp(0, x, 0x118, 0xf, 0xf, false);
  x += __builtin_amdgcn_update_dpp(0, x, 0x142, 0xa, 0xf, false);
  x += __builtin_amdgcn_update_dpp(0, x, 0x143, 0xc, 0xf, false);
  return x;
}
__device__ __forceinline__ bf16x8 asbf(u32x4 v) { return __builtin_bit_cast(bf16x8, v); }
__device__ __forceinline__ u32x2 trread(const char* p) {
  return __builtin_bit_cast(u32x2, __builtin_amdgcn_ds_read_tr16_b64_v4i16((LAS s16x4*)p));
}
__device__ __forceinline__ rsrc_t mkrs(const void* p, u32 bytes) { return __builtin_amdgcn_make_buffer_rsrc((void*)p, 0, bytes, 0x00020000); }
#define MFMA32(a, b, c) __builtin_amdgcn_mfma_f32_32x32x16_bf16(a, b, c, 0, 0, 0)
#define MFMA16(a, b, c) __builtin_amdgcn_mfma_f32_16x16x32_bf16(a, b, c, 0, 0, 0)
#define SB() __builtin_amdgcn_sched_barrier(0)
#define VMWAIT0() asm volatile("s_waitcnt vmcnt(0)" ::: "memory")

template <int V> struct IC { static constexpr int value = V; };
__device__ __forceinline__ u32x4 mksrd(const void* p, u32 bytes) {
  const unsigned long long a = (unsigned long long)p;
  return (u32x4){(u32)a, (u32)(a >> 32) & 0xffffu, bytes, 0x00020000u};
}
#define DMA1(J, A, SRD, SOFF) asm volatile("buffer_load_dwordx4 %0, %1, %2 offen offset:" #J " lds" :: "v"(A), "s"(SRD), "s"(SOFF) : "memory")
#define LDB(T, DST, VO, SRD, SO) asm volatile("buffer_load_" T " %0, %1, %2, %3 offen" : "=v"(DST) : "v"(VO), "s"(SRD), "s"(SO) : "memory")
#define SETM0(V) asm volatile("s_mov_b32 m0, %0\n\ts_nop 0" :: "s"(V) : "memory")
#define VMWAIT(N) asm volatile("s_waitcnt vmcnt(" #N ")" ::: "memory")
#define LGKMWAIT0() asm volatile("s_waitcnt lgkmcnt(0)" ::: "memory")
#define PIN(x) do { asm volatile("" : "+v"(x)); SB(); } while (0)

constexpr int K1_H = 32768;
constexpr int K1_IMG = K1_H + 48 * 256;
constexpr int K1_HIST = K1_IMG + 16384;
constexpr int K1_LDS = K1_HIST + 8 * NB * 4 + 32;

extern "C" __global__ __launch_bounds__(512) void egnn_k1(const u16* __restrict__ h, const u16* __restrict__ ea,
                                               const int* __restrict__ src, const int* __restrict__ dst,
                                               const u16* __restrict__ we, char* __restrict__ ws,
                                               const u16* __restrict__ pos, const u16* __restrict__ wh,
                                               const u16* __restrict__ out) {
  __shared__ __attribute__((aligned(128))) char lds[K1_LDS];
  const int tid = threadIdx.x, lane = tid & 63;
  const int w = __builtin_amdgcn_readfirstlane(tid >> 6);
  const int c = (blockIdx.x & 7) * 32 + (blockIdx.x >> 3);
  const int gq = lane >> 4, c16 = lane & 15;
  u32* const hist = (u32*)(lds + K1_HIST);
  asm volatile("" :: "s"(h), "s"(ea), "s"(src), "s"(dst), "s"(we), "s"(ws));
  if (tid < NB) *(u32x4*)(hist + 4 * tid) = (u32x4)(0u);
  else if (tid < NB + 8) hist[7 * NB + tid] = 0;
  __syncthreads();
  auto meet = [&](int k) {
    LGKMWAIT0();
    if (lane == 0) __hip_atomic_fetch_add(&hist[8 * NB + k], 1u, __ATOMIC_RELAXED, __HIP_MEMORY_SCOPE_WORKGROUP);
    while (__hip_atomic_load(&hist[8 * NB + k], __ATOMIC_RELAXED, __HIP_MEMORY_SCOPE_WORKGROUP) < 4u) __builtin_amdgcn_s_sleep(1);
    asm volatile("" ::: "memory");
  };
  if (w < 4) {
    const u32 eo = tid * 20, nb = c < NCH ? CH * 4 : 0;
    const rsrc_t rs_d = mkrs(dst + c * CH, nb), rs_s = mkrs(src + c * CH, nb), rs_a = mkrs(ea + (size_t)c * CH * 4, 2 * nb);
    const u32x4 d4 = __builtin_amdgcn_raw_buffer_load_b128(rs_d, eo, 0, 0);
    const u32 d1 = __builtin_amdgcn_raw_buffer_load_b32(rs_d, eo + 16, 0, 0);
    const u32x4 s4 = __builtin_amdgcn_raw_buffer_load_b128(rs_s, eo, 0, 0);
    const u32 s1 = __builtin_amdgcn_raw_buffer_load_b32(rs_s, eo + 16, 0, 0);
    const u32x4 a0 = __builtin_amdgcn_raw_buffer_load_b128(rs_a, 2 * eo, 0, 0);
    const u32x4 a1 = __builtin_amdgcn_raw_buffer_load_b128(rs_a, 2 * eo + 16, 0, 0);
    const u32x2 a2 = __builtin_amdgcn_raw_buffer_load_b64(rs_a, 2 * eo + 32, 0, 0);
    __builtin_amdgcn_s_setprio(1);
    if (c < NCH) {
      u32* const img = (u32*)(lds + K1_IMG);
      u32* const base = hist + (4 + w) * NB;
      u32 bk[5], rk[5];
#pragma unroll
      for (int i = 0; i < 5; i++) {
        bk[i] = __umul24(i < 4 ? d4[i & 3] : d1, 214749u) >> 23;
        rk[i] = atomicAdd(&hist[w * NB + bk[i]], 1u);
      }
      meet(1);
      {
        const u32x4 v0 = *(const u32x4*)(hist + 4 * lane), v1 = *(const u32x4*)(hist + NB + 4 * lane);
        const u32x4 v2 = *(const u32x4*)(hist + 2 * NB + 4 * lane), v3 = *(const u32x4*)(hist + 3 * NB + 4 * lane);
        const u32x4 v = v0 + v1 + v2 + v3;
        const u32x4 pre = w == 0 ? (u32x4)(0u) : w == 1 ? v0 : w == 2 ? v0 + v1 : v - v3;
        const int s = v[0] + v[1] + v[2] + v[3];
        const int ex = wave_incl_scan(s) - s;
        const u32x4 bs = {(u32)ex, ex + v[0], ex + v[0] + v[1], ex + v[0] + v[1] + v[2]};
        *(u32x4*)(base + 4 * lane) = bs + pre;
        const u32 bb = w == 0 ? bs[0] : w == 1 ? bs[1] : w == 2 ? bs[2] : bs[3];
        const u32 vv = w == 0 ? v[0] : w == 1 ? v[1] : w == 2 ? v[2] : v[3];
        ((u32*)(ws + WS_T))[(4 * lane + w) * 256 + c] = bb | (vv << 16);
      }
#pragma unroll
      for (int i = 0; i < 5; i++) {
        u32* p = img + 3 * (base[bk[i]] + rk[i]);
        p[0] = i < 2 ? a0[2 * (i & 1)] : i < 4 ? a1[2 * (i & 1)] : a2[0];
        p[1] = i < 2 ? a0[2 * (i & 1) + 1] : i < 4 ? a1[2 * (i & 1) + 1] : a2[1];
        p[2] = (i < 4 ? s4[i & 3] : s1) | ((i < 4 ? d4[i & 3] : d1) << 16);
      }
      meet(2);
      const rsrc_t rs_r = mkrs(ws + WS_R + (size_t)c * (CH * 12), CH * 12);
#pragma unroll
      for (int i = 0; i < 4; i++)
        __builtin_amdgcn_raw_buffer_store_b128(((const u32x4*)img)[tid + 256 * i], rs_r, tid * 16 + i * 4096, 0, 0);
    }
  } else {
    const int u = w - 4, n0 = (c * 625 + 15) >> 4, nn = (((c + 1) * 625 + 15) >> 4) - n0;
    const u32 ldsb = (u32)(size_t)(LAS char*)lds;
    {
      const u32x4 srd_h = mksrd(h + (size_t)n0 * 128, nn * 256), srd_w = mksrd(we + 128 * 128, 32768);
      const u32 vw = gq * 256 + 16 * (c16 ^ (4 * gq));
      SETM0(ldsb + u * 8192);
      DMA1(0, vw, srd_w, u * 8192); DMA1(1024, vw, srd_w, u * 8192); DMA1(2048, vw, srd_w, u * 8192); DMA1(3072, vw, srd_w, u * 8192);
      SETM0(ldsb + u * 8192 + 4096);
      DMA1(0, vw, srd_w, u * 8192 + 4096); DMA1(1024, vw, srd_w, u * 8192 + 4096); DMA1(2048, vw, srd_w, u * 8192 + 4096); DMA1(3072, vw, srd_w, u * 8192 + 4096);
      SETM0(ldsb + K1_H + u * 3072);
      DMA1(0, gq * 256 + 16 * (c16 ^ ((12 * u + gq) & 15)), srd_h, u * 3072);
      DMA1(1024, gq * 256 + 16 * (c16 ^ ((12 * u + 4 + gq) & 15)), srd_h, u * 3072);
      DMA1(2048, gq * 256 + 16 * (c16 ^ ((12 * u + 8 + gq) & 15)), srd_h, u * 3072);
    }
    VMWAIT0();
    u32 pfv = 0;
    const bool pf = blockIdx.x < 64;
    if (pf && u == 3) {
      const int i = (blockIdx.x >> 3) * 64 + lane;
      const char* p = ws + min(i, 208) * TSTR;
      if (i >= 256) p = (const char*)out + min(i - 256, 79) * TSTR;
      if (i >= 384) p = (const char*)pos + (i & 1) * TSTR;
      if (i >= 448) p = (const char*)wh + (i & 1) * TSTR;
      asm volatile("global_load_dword %0, %1, off" : "=v"(pfv) : "v"(p) : "memory");
    } else if (pf) {
      unsigned long long pc;
      u32 off;
      asm volatile("s_getpc_b64 %0\n.Lpc%=:\n\ts_mov_b32 %1, egnn_k2-.Lpc%=" : "=s"(pc), "=s"(off));
      asm volatile("global_load_dword %0, %1, %2" : "=v"(pfv) : "v"(min(u * 64 + lane, 147) * 128), "s"(pc + off) : "memory");
    }
    meet(0);
    f32x4 acc[3][2];
#pragma unroll
    for (int rt = 0; rt < 3; rt++) acc[rt][0] = acc[rt][1] = (f32x4)(0.f);
    const char* wb = lds + (8 * gq + (c16 >> 2)) * 256 + 16 * ((4 * u + (c16 & 3)) ^ (c16 & 12));
    const int hb = K1_H + c16 * 256 + 16 * (gq ^ c16);
    u32x4 fa[4], fb[4], hf[4][3];
#pragma unroll
    for (int s = 0; s < 4; s++) {
      const u32x2 x0 = trread(wb + (32 * s) * 256), x1 = trread(wb + (32 * s + 4) * 256);
      const u32x2 y0 = trread(wb + (32 * s) * 256 + 8), y1 = trread(wb + (32 * s + 4) * 256 + 8);
      fa[s] = (u32x4){x0[0], x0[1], x1[0], x1[1]};
      fb[s] = (u32x4){y0[0], y0[1], y1[0], y1[1]};
#pragma unroll
      for (int rt = 0; rt < 3; rt++) hf[s][rt] = *(const u32x4*)(lds + (hb ^ (64 * s)) + rt * 4096);
    }
    SB();
#pragma unroll
    for (int s = 0; s < 4; s++)
#pragma unroll
      for (int rt = 0; rt < 3; rt++) {
        acc[rt][0] = MFMA16(asbf(fa[s]), asbf(hf[s][rt]), acc[rt][0]);
        acc[rt][1] = MFMA16(asbf(fb[s]), asbf(hf[s][rt]), acc[rt][1]);
      }
    const rsrc_t rs_b = mkrs(ws + WS_B + (size_t)n0 * 256, nn * 256);
#pragma unroll
    for (int rt = 0; rt < 3; rt++) {
      const f32x4 a = acc[rt][0] * NLOG2E, b = acc[rt][1] * NLOG2E;
      const u32x4 o = {pk2(a[0], a[1]), pk2(a[2], a[3]), pk2(b[0], b[1]), pk2(b[2], b[3])};
      __builtin_amdgcn_raw_buffer_store_b128(o, rs_b, c16 * 256 + 16 * gq + 64 * u + rt * 4096, 0, 0);
    }
    if (pf) asm volatile("s_waitcnt vmcnt(0)" :: "v"(pfv) : "memory");
  }
}

constexpr int OHS = 2 * CAP + 16;
constexpr int L_NODE = 0;
constexpr int L_WX = L_NODE + 320;
constexpr int L_WS = L_WX + 512;
constexpr int L_REC = L_WS + 16;
constexpr int L_CV = L_REC + 16 * (CAP + 1);
constexpr int L_SRC = L_CV + 8 * (CAP + 2);
constexpr int L_OH = L_SRC + 2 * (CAP + 8);
constexpr int L_CNT = L_OH + 8 * OHS;
constexpr int L_HM = L_CNT + 256;
constexpr int L_IMG = L_HM + 160;
constexpr int L_AT = L_IMG + 48 * 528;
constexpr int L_STG = (L_AT + 128 * 96 + 127) & ~127;
constexpr int K2_LDS = L_STG + 65536;
constexpr int L_MG = L_STG + 32768;
static_assert(K2_LDS <= 163840, "LDS");
static_assert(L_CV % 16 == 0 && L_OH % 16 == 0 && L_SRC % 16 == 0 && L_IMG % 16 == 0 && L_AT % 16 == 0, "align");
static_assert(L_IMG - L_OH > 5 * 4096 && L_IMG - L_OH <= 6 * 4096, "zero sweep");

extern "C" __global__ __launch_bounds__(512, 2) void egnn_k2(const u16* __restrict__ h, const u16* __restrict__ pos,
                                                  const u16* __restrict__ we, const u16* __restrict__ wx,
                                                  const u16* __restrict__ wh, const char* __restrict__ ws,
                                                  u16* __restrict__ out) {
  __shared__ __attribute__((aligned(128))) char lds[K2_LDS];
  int tid = threadIdx.x, lane = tid & 63;
  const int w = __builtin_amdgcn_readfirstlane(tid >> 6);
  const int b = (blockIdx.x & 7) * 32 + (blockIdx.x >> 3);
  const int n0 = (b * 625 + 15) >> 4, nn = (((b + 1) * 625 + 15) >> 4) - n0;
  int gq = lane >> 4, c16 = lane & 15, hh = lane >> 5, n32 = lane & 31;
  const u32 ldsb = (u32)(size_t)(LAS char*)lds;
  const u32x4 srd_g = mksrd(ws + WS_B - 4096, 4096 + NN * 256), srd_pos = mksrd(pos, NN * 6);

  asm volatile("" :: "s"(h), "s"(pos), "s"(we), "s"(wx), "s"(wh), "s"(ws), "s"(out));
  u32 cwv[6];
  u32x4 hv[3], t4;
  if (w < 4) {
    LDB("dwordx4", t4, lane * 16, mksrd(ws + WS_T + b * 1024, 1024), 0);
    *(u32x2*)(lds + L_REC + tid * 8) = (u32x2)(0u);
  } else {
    const int ty = tid - 256;
    const u32x4 srd_h = mksrd(h + (size_t)n0 * 128, nn * 256), srd_we = mksrd(we, 261 * 256);
#pragma unroll
    for (int i = 0; i < 3; i++) LDB("dwordx4", hv[i], ty * 16, srd_h, i * 4096);
    if (w >= 6) {
#pragma unroll
      for (int k = 0; k < 5; k++) LDB("ushort", cwv[k], tid * 2, srd_we, (256 + k) * 256 - 768);
      LDB("ushort", cwv[5], tid * 2 - 768, mksrd(wx, 256), 0);
    } else {
      LDB("ushort", cwv[0], ty * 2, srd_pos, n0 * 6);
    }
    const u32 vw = gq * 256 + 16 * (c16 ^ (4 * gq)), so = (w - 4) * 8192;
    SETM0(ldsb + L_STG + so);
    DMA1(0, vw, srd_we, so); DMA1(1024, vw, srd_we, so); DMA1(2048, vw, srd_we, so); DMA1(3072, vw, srd_we, so);
    SETM0(ldsb + L_STG + so + 4096);
    DMA1(0, vw, srd_we, so + 4096); DMA1(1024, vw, srd_we, so + 4096); DMA1(2048, vw, srd_we, so + 4096); DMA1(3072, vw, srd_we, so + 4096);
  }
  SB();
  u32x4 id0, id1, one4 = {hh ? 0u : 0x3F80u, 0u, 0u, 0u};
  int ga[4], recoff, ohoff, cvoff, axoff, srcoff;
  u32 go[4], vsrc;
  auto lane_consts = [&]() {
    const int k0 = n32 - 8 * hh;
    const u32 one = 0x3F80u << ((k0 & 1) * 16);
#pragma unroll
    for (int i = 0; i < 4; i++) {
      id0[i] = (k0 >> 1) == i ? one : 0u;
      id1[i] = (k0 >> 1) == 8 + i ? one : 0u;
    }
    const int prow = (n32 & 19) | ((n32 & 4) << 1) | ((n32 & 8) >> 1);
    const int sa = prow & 3, sb = prow >> 2;
#pragma unroll
    for (int s = 0; s < 4; s++) ga[s] = (L_STG + w * 8192 + (8 * sa + sb) * 128 + 16 * (hh ^ (2 * sa + ((sb >> 1) & 1)))) ^ (32 * s);
#pragma unroll
    for (int j = 0; j < 4; j++) go[j] = 4096 - 1024 * j + 16 * ((lane & 7) ^ (2 * j + ((lane >> 4) & 1)));
    recoff = L_REC + prow * 16;
    ohoff = L_OH + (n32 >> 2) * OHS + 16 * hh;
    cvoff = L_CV + (8 * hh + (c16 >> 2)) * 8;
    axoff = L_OH + (c16 >> 2) * OHS + 32 * (gq & 1) + 16 * (c16 & 1) + 8 * ((c16 >> 1) & 1);
    srcoff = L_SRC + (lane >> 3) * 8;
    vsrc = (lane >> 1) * 256 + 32 * w + 16 * (lane & 1);
  };
  lane_consts();
  const u32 m0a = ldsb + L_STG + w * 8192, m0b = m0a + 4096;
  asm volatile("" : "+v"(one4), "+v"(id0), "+v"(id1), "+v"(ga[0]), "+v"(ga[1]), "+v"(ga[2]), "+v"(ga[3]), "+v"(go[0]), "+v"(go[1]), "+v"(go[2]), "+v"(go[3]));
  asm volatile("" : "+v"(recoff), "+v"(ohoff), "+v"(cvoff), "+v"(axoff), "+v"(srcoff), "+v"(vsrc));
  SB();
  int mtot, scnt, sexc;
  u32 sbase;
  auto decode = [&]() {
    if (lane == 62) t4[2] = t4[3] = 0;
    if (lane == 63) t4 = (u32x4)(0u);
    const int c0 = t4[0] >> 16, c1 = c0 + (t4[1] >> 16), c2 = c1 + (t4[2] >> 16), c3 = c2 + (t4[3] >> 16);
    const int inc = wave_incl_scan(c3);
    mtot = __builtin_amdgcn_readlane(inc, 63);
    const int wq = w & 3;
    const u32 tv = wq == 0 ? t4[0] : wq == 1 ? t4[1] : wq == 2 ? t4[2] : t4[3];
    scnt = tv >> 16;
    sexc = inc - c3 + (wq == 0 ? 0 : wq == 1 ? c0 : wq == 2 ? c1 : c2);
    sbase = ((4 * lane + wq) * CH + (tv & 0xffffu)) * 12;
  };
  if (w < 4) {
    VMWAIT0(); SB();
    decode();
    if (tid == 0) *(int*)(lds + L_WS) = mtot;
    if (mtot <= CAP) {
      u32* p = (u32*)(lds + L_CV) + sexc;
      u32 v = sbase;
      if (scnt & 1) p[scnt - 1] = v + 12 * (scnt - 1);
#pragma clang loop vectorize(disable) unroll(disable)
      for (int i = 1; i < scnt; i += 2, p += 2, v += 24) { p[0] = v; p[1] = v + 12; }
    }
  } else {
    VMWAIT0(); SB();
    asm volatile("" : "+v"(cwv[0]), "+v"(cwv[1]), "+v"(cwv[2]), "+v"(cwv[3]), "+v"(cwv[4]), "+v"(cwv[5]));
    char* hp = lds + L_IMG + ((tid - 256) >> 4) * 528 + c16 * 16;
#pragma unroll
    for (int i = 0; i < 3; i++) *(u32x4*)(hp + i * 16 * 528) = hv[i];
    if (w < 6) {
      const int t3 = tid - 256, nl = __umul24(t3, 21846) >> 16;
      if (t3 < 120) *(u16*)(lds + L_NODE + nl * 2 + t3 * 2) = (u16)cwv[0];
    }
  }
  __syncthreads();
  u32x3 rec[3];
  bool general;
  {
    const u32x4 srd_rec = mksrd(ws + WS_R, NE * 12);
    u32 ro[3];
#pragma unroll
    for (int k = 0; k < 3; k++) ro[k] = ((const u32*)(lds + L_CV))[tid + 512 * k];
    mtot = __builtin_amdgcn_readfirstlane(*(const int*)(lds + L_WS));
    general = mtot > CAP;
    const int lim = general ? 0 : mtot;
#pragma unroll
    for (int k = 0; k < 3; k++) LDB("dwordx3", rec[k], tid + 512 * k < lim ? ro[k] : 0x7fffff00u, srd_rec, 0);
  }
  if (general) asm volatile("s_waitcnt vmcnt(0)" :: "v"(rec[0]), "v"(rec[1]), "v"(rec[2]) : "memory");
  SB();
  if (w >= 6) {
    const float c0 = bf2f(cwv[0]) * NLOG2E;
    const u32 d_hi = pk2(c0, 0.f);
    const u32x4 o = {pk2(bf2f(cwv[1]) * NLOG2E, bf2f(cwv[2]) * NLOG2E), pk2(bf2f(cwv[3]) * NLOG2E, bf2f(cwv[4]) * NLOG2E),
                     pk2(c0, c0 - bf2f(d_hi)), 0u};
    *(u32x4*)(lds + L_AT + (tid - 384) * 96 + 80) = o;
    ((float*)(lds + L_WX))[tid - 384] = bf2f(cwv[5]) * NLN2;
  }
  const char* const xb = lds + L_IMG + c16 * 528 + 16 * gq;
  if (w < 4) {
    f32x4 acc[3][2];
#pragma unroll
    for (int rt = 0; rt < 3; rt++) acc[rt][0] = acc[rt][1] = (f32x4)(0.f);
    const char* wb = lds + L_STG + (8 * gq + (c16 >> 2)) * 256 + 16 * ((4 * w + (c16 & 3)) ^ (c16 & 12));
    u32x4 fa[4], fb[4], hf[4][3];
#pragma unroll
    for (int s = 0; s < 4; s++) {
      const u32x2 x0 = trread(wb + (32 * s) * 256), x1 = trread(wb + (32 * s + 4) * 256);
      const u32x2 y0 = trread(wb + (32 * s) * 256 + 8), y1 = trread(wb + (32 * s + 4) * 256 + 8);
      fa[s] = (u32x4){x0[0], x0[1], x1[0], x1[1]};
      fb[s] = (u32x4){y0[0], y0[1], y1[0], y1[1]};
#pragma unroll
      for (int rt = 0; rt < 3; rt++) hf[s][rt] = *(const u32x4*)(xb + rt * 8448 + 64 * s);
    }
    SB();
#pragma unroll
    for (int s = 0; s < 4; s++)
#pragma unroll
      for (int rt = 0; rt < 3; rt++) {
        acc[rt][0] = MFMA16(asbf(hf[s][rt]), asbf(fa[s]), acc[rt][0]);
        acc[rt][1] = MFMA16(asbf(hf[s][rt]), asbf(fb[s]), acc[rt][1]);
      }
    char* ap = lds + L_AT + (32 * w + 8 * (c16 >> 2) + (c16 & 3)) * 96 + 8 * gq;
#pragma unroll
    for (int rt = 0; rt < 3; rt++)
      if (16 * rt + 4 * gq < 40)
#pragma unroll
        for (int j = 0; j < 2; j++) {
          const f32x4 a = acc[rt][j] * (NLOG2E / 1.0078125f);
          *(u32x2*)(ap + j * 384 + rt * 32) = (u32x2){pk2(a[0], a[1]), pk2(a[2], a[3])};
        }
  }
  if (w >= 4) {
    const int ty = tid - 256;
#pragma unroll
    for (int i = 0; i < 5; i++) *(u32x4*)(lds + L_OH + ty * 16 + i * 4096) = (u32x4)(0u);
    if (ty < (L_IMG - L_OH - 5 * 4096) / 16) *(u32x4*)(lds + L_OH + ty * 16 + 5 * 4096) = (u32x4)(0u);
    char* ip = lds + L_IMG + (ty >> 4) * 528 + 256 + c16 * 16;
#pragma unroll
    for (int i = 0; i < 3; i++) *(u32x4*)(ip + i * 16 * 528) = (u32x4)(0u);
    if (ty < 48) *(u32x4*)(lds + L_IMG + ty * 528 + 512) = (u32x4)(0u);
  }
  f32x16 Y[4], XA, XB, e;
  u32x4 sp0 = (u32x4)(0u), sp1 = (u32x4)(0u), pa = (u32x4)(0u), pb = (u32x4)(0u);
  asm volatile("" : "+v"(sp0), "+v"(sp1), "+v"(pa), "+v"(pb));
  Y[0] = MFMA32(asbf(pa), asbf(pa), (f32x16)(0.f));
  Y[1] = MFMA32(asbf(pb), asbf(pb), (f32x16)(0.f));
  Y[2] = MFMA32(asbf(sp0), asbf(sp0), (f32x16)(0.f));
  Y[3] = MFMA32(asbf(sp1), asbf(sp1), (f32x16)(0.f));
  XB = MFMA32(asbf(pa), asbf(pb), (f32x16)(0.f));
  asm volatile("" : "+v"(Y[0]), "+v"(Y[1]), "+v"(Y[2]), "+v"(Y[3]), "+v"(XB));
  SB();
  u32x4 wex[4];
  u32 ad[4];
  int ninc;
  u32x4 srd = srd_g;
  u32 sfb = 128;
  auto prep = [&](u32x2 sv, bool real) {
    if (real) {
      asm("v_mad_u32_u16 %0, %1, %2, %3" : "=v"(ad[0]) : "v"(sv[0]), "s"(256), "v"(go[0]));
      asm("v_mad_u32_u16 %0, %1, %2, %3 op_sel:[1,0,0,0]" : "=v"(ad[1]) : "v"(sv[0]), "s"(256), "v"(go[1]));
      asm("v_mad_u32_u16 %0, %1, %2, %3" : "=v"(ad[2]) : "v"(sv[1]), "s"(256), "v"(go[2]));
      asm("v_mad_u32_u16 %0, %1, %2, %3 op_sel:[1,0,0,0]" : "=v"(ad[3]) : "v"(sv[1]), "s"(256), "v"(go[3]));
    } else {
#pragma unroll
      for (int j = 0; j < 4; j++) ad[j] = vsrc + 7168 * j;
      srd = mksrd(wh, 65536);
      sfb = 32768;
    }
  };
  auto issue_a = [&]() { SETM0(m0a); DMA1(0, ad[0], srd, 0); DMA1(1024, ad[1], srd, 0); DMA1(2048, ad[2], srd, 0); DMA1(3072, ad[3], srd, 0); };
  auto issue_b = [&]() { SETM0(m0b); DMA1(0, ad[0], srd, sfb); DMA1(1024, ad[1], srd, sfb); DMA1(2048, ad[2], srd, sfb); DMA1(3072, ad[3], srd, sfb); };
  auto node_at = [&](int x) { return (int)__builtin_popcountll(__builtin_amdgcn_ballot_w64(ninc <= x)); };
  auto loadw = [&](int base) {
    const int g0 = base >> 3, bm = base & 7;
    const char* pa = lds + L_AT + n32 * 96 + (hh ? g0 * 16 : 80);
    const char* pb = lds + L_AT + n32 * 96 + (hh ? g0 * 16 + 16 : 80);
    const unsigned long long lo = ~0ull << (16 * min(bm, 3)) << (bm > 3 ? 16 : 0), hi = bm > 4 ? ~0ull << (16 * (bm - 4)) : ~0ull;
    const u32 mk[4] = {(u32)lo, (u32)(lo >> 32), (u32)hi, (u32)(hi >> 32)};
#pragma unroll
    for (int t = 0; t < 4; t++) {
      const u32x4 a = *(const u32x4*)(pa + t * 3072), bb = *(const u32x4*)(pb + t * 3072);
#pragma unroll
      for (int i = 0; i < 4; i++) wex[t][i] = (a[i] & mk[i]) | (bb[i] & ~mk[i]);
    }
  };
  auto pdot = [&](auto nbc) {
    constexpr int nb = decltype(nbc)::value;
    f32x4 p4[2] = {(f32x4)(0.f), (f32x4)(0.f)};
#pragma unroll
    for (int q0 = 0; q0 < 16; q0 += nb) {
      f32x4 wv[16];
#pragma unroll
      for (int q = q0; q < q0 + nb; q++) wv[q] = *(const f32x4*)(lds + L_WX + (8 * q + 4 * hh) * 4);
      SB();
#pragma unroll
      for (int q = q0; q < q0 + nb; q++)
        p4[q & 1] += (f32x4){Y[q >> 2][4 * (q & 3)], Y[q >> 2][4 * (q & 3) + 1], Y[q >> 2][4 * (q & 3) + 2], Y[q >> 2][4 * (q & 3) + 3]} * wv[q];
      SB();
    }
    p4[0] += p4[1];
    const f32x2 pa = (f32x2){p4[0][0], p4[0][1]} + (f32x2){p4[0][2], p4[0][3]};
    const u32 ph = __float_as_uint(pa[0] + pa[1]);
    const auto sw = __builtin_amdgcn_permlane32_swap(ph, ph, false, false);
    return __uint_as_float(sw[0]) + __uint_as_float(sw[1]);
  };
  auto place = [&](u32x3 rec, u32x2 ps, u32x2 pd, u32 dl, u32 p) {
    const u32 sh = (rec[2] & 1) * 16;
    const u32 sxy = __builtin_amdgcn_alignbit(ps[1], ps[0], sh), sz = ps[1] >> sh;
    const u32 dxy = pk2(bf2f(pd[0]) - bf2f(sxy), bfhi(pd[0]) - bfhi(sxy));
    const u32 dz1 = pk2(bf2f(pd[1]) - bf2f(sz), 1.0f);
    const float fx = bf2f(dxy), fy = bfhi(dxy), fz = bf2f(dz1);
    const float d2 = fx * fx + fy * fy + fz * fz;
    *(u32x4*)(lds + L_REC + p * 16) = (u32x4){rec[0], rec[1], pk2(d2, d2), dl};
    *(u32x2*)(lds + L_CV + p * 8) = (u32x2){mul16x2(dxy, OHINV * 0x10001u), mul16x2(dz1, OHINV * 0x10001u)};
    *(u16*)(lds + L_OH + (dl & 7) * OHS + 2 * p) = (u16)OH;
  };
  auto count_nodes = [&](int m, int cn) {
    ninc = wave_incl_scan(cn);
    if (w == 7) {
      const int p = m + n32 < ((m + 31) & ~31) ? m + n32 : CAP;
      *(u32x4*)(lds + L_REC + p * 16) = (u32x4)(0u);
      ((u16*)(lds + L_SRC))[p] = 0;
    }
    return ninc - cn;
  };
#define RDG(s, half) (*(const u32x4*)(lds + ga[s] + (half) * 4096))
#define EXP_(lo, hi) _Pragma("unroll") for (int r = lo; r < hi; r++) e[r] = __builtin_amdgcn_exp2f(xc[r]); SB()
#define RCP_(lo, hi) _Pragma("unroll") for (int r = lo; r < hi; r++) e[r] = __builtin_amdgcn_rcpf(e[r]); SB()
#define PACK_() _Pragma("unroll") for (int i = 0; i < 4; i++) { pa[i] = pk2(e[2 * i], e[2 * i + 1]); pb[i] = pk2(e[8 + 2 * i], e[8 + 2 * i + 1]); } \
  asm volatile("" : "+v"(pa), "+v"(pb)); SB()
#define SPROD(o, ca, cb) (u32x4){mul16x2(o[0], ca[0]), mul16x2(o[1], ca[1]), mul16x2(o[2], cb[0]), mul16x2(o[3], cb[1])}

  const int m = mtot;
  const bool third = w * 64 + 1024 < m;
  u32 rk[3], dl[3];
  u32x2 ps[3], pd[3];
  if (!general) {
    VMWAIT0(); SB();
#pragma unroll
    for (int k = 0; k < 3; k++)
      if (k < 2 || third) {
        LDB("dwordx2", ps[k], ((rec[k][2] & 0xffffu) * 6) & ~3u, srd_pos, 0);
        dl[k] = tid + 512 * k < m ? (rec[k][2] >> 16) - n0 : 48u + c16;
        rk[k] = atomicAdd((u32*)(lds + L_REC) + w * 64 + dl[k], 1u);
        pd[k] = *(const u32x2*)(lds + L_NODE + dl[k] * 8);
      }
    __syncthreads();
    int nst;
    {
      u32 cw[8], pre = 0;
#pragma unroll
      for (int j = 0; j < 8; j++) cw[j] = ((const u32*)(lds + L_REC))[j * 64 + lane];
      nst = count_nodes(m, (cw[0] + cw[1] + cw[2]) + (cw[3] + cw[4] + cw[5]) + (cw[6] + cw[7]));
#pragma unroll
      for (int j = 0; j < 7; j++) asm("v_mad_u32_u24 %0, %1, %2, %3" : "=v"(pre) : "v"(cw[j]), "s"(j < w ? 1 : 0), "v"(pre));
      nst += pre;
    }
    const int nprev = __builtin_amdgcn_ds_bpermute(lane * 4 - 28, ninc);
#pragma unroll
    for (int k = 0; k < 3; k++)
      if (k < 2 || third) {
        rk[k] += __builtin_amdgcn_ds_bpermute(dl[k] * 4, nst);
        rk[k] = dl[k] < 48 ? rk[k] : CAP;
        *(u16*)(lds + L_SRC + 2 * rk[k]) = (u16)rec[k][2];
      }
    general = m < 256 || __builtin_amdgcn_ballot_w64(lane >= 7 && lane < nn && ninc - nprev <= 30) != 0;
    if (general) asm volatile("s_waitcnt vmcnt(0)" :: "v"(ps[0]), "v"(ps[1]), "v"(ps[2]) : "memory");
  }
  if (!general) {
    {
      const int nt = (m + 31) >> 5, tq = nt >> 3, trm = nt & 7;
      const int tbeg = w * tq + min(w, trm);
      const int tend = tbeg + tq + (w < trm ? 1 : 0);
      srcoff += tbeg * 64;
      __syncthreads();
      const u32x2 sv0 = *(const u32x2*)(lds + srcoff), sv1 = *(const u32x2*)(lds + srcoff + 64);
      prep(sv0, true);
      if (w < 4) {
        issue_a();
        issue_b();
        VMWAIT(8);
      } else {
        VMWAIT(0);
      }
      SB();
      int base = node_at(tbeg * 32);
      loadw(base);
      const int lnode = node_at(min(tend * 32, m) - 1);
      const int hnode = (tbeg > 0 && node_at(tbeg * 32 - 1) == base) ? base : -1;
      if (hnode >= 0 && lane == 0) __hip_atomic_fetch_or((u32*)(lds + L_HM) + hnode, 1u << w, __ATOMIC_RELAXED, __HIP_MEMORY_SCOPE_WORKGROUP);
      recoff += tbeg * 512; cvoff += tbeg * 256 - 256; ohoff += tbeg * 64 - 64; axoff += tbeg * 64;
#pragma unroll
      for (int k = 0; k < 3; k++)
        if (k < 2 || third) place(rec[k], ps[k], pd[k], dl[k], rk[k]);
      if (w >= 4) {
        issue_a();
        issue_b();
      }
      prep(sv1, tbeg + 1 < tend);
#pragma unroll
      for (int t = 0; t < 4; t++) asm volatile("" : "+v"(wex[t]));
      auto flush = [&](int base, int lim, auto clearc) {
        const int node = base + (((n32 >> 2) - base) & 7), comp = n32 & 3;
        constexpr bool clear = decltype(clearc)::value != 0;
        const float pt = pdot(IC<(clear ? 2 : 16)>{});
        if (node < lim) {
          char* mp = lds + L_IMG + (node == hnode ? 39 + w : node) * 528;
          if (comp == 3) {
#pragma unroll
            for (int q = 0; q < 16; q++)
              *(u32x2*)(mp + 256 + 8 * hh + 16 * q) = (u32x2){pk2(Y[q >> 2][4 * (q & 3)], Y[q >> 2][4 * (q & 3) + 1]),
                                                              pk2(Y[q >> 2][4 * (q & 3) + 2], Y[q >> 2][4 * (q & 3) + 3])};
          } else {
            *(float*)(mp + 512 + 4 * comp) = pt;
          }
        }
        if (clear) {
          const float keep = node < lim ? 0.f : 1.f;
#pragma unroll
          for (int t = 0; t < 4; t++) Y[t] *= keep;
        }
      };
      __syncthreads();
      u32x4 aex;
      {
        const u32x2 t0 = trread(lds + axoff), t1 = trread(lds + axoff + 4 * OHS);
        const u32x4 ra = *(const u32x4*)(lds + recoff);
        aex = hh ? (u32x4){t0[0], t0[1], t1[0], t1[1]} : ra;
      }
      XA = MFMA32(asbf(aex), asbf(wex[0]), (f32x16)(0.f)); PIN(XA);
      VMWAIT(4);
      u32x4 g0 = RDG(0, 0), g1 = RDG(1, 0), g2 = RDG(2, 0), g3 = RDG(3, 0);
      LGKMWAIT0();
      SB();
      XA = MFMA32(asbf(g0), asbf(id0), XA); PIN(XA);
      issue_a();
      XA = MFMA32(asbf(g1), asbf(id1), XA); PIN(XA);
      int fbase = base;
      __builtin_amdgcn_s_setprio(3);
#define EXPX(X, lo, hi) _Pragma("unroll") for (int r = lo; r < hi; r++) e[r] = __builtin_amdgcn_exp2f(X[r]); SB()
#define PACKX(X) _Pragma("unroll") for (int i = 0; i < 4; i++) { pa[i] = pk2(X[2 * i], X[2 * i + 1]); pb[i] = pk2(X[8 + 2 * i], X[8 + 2 * i + 1]); } \
  asm volatile("" : "+v"(pa), "+v"(pb)); SB()
#define SEG(t, p, sp) Y[t] = MFMA32(asbf(p), asbf(sp), Y[t]); PIN(Y[t])
#define PLUS1() e = MFMA32(asbf(one4), asbf(one4), e); PIN(e)
      for (int left = tend - tbeg, x = tbeg * 32 + 32; left > 0; left--, x += 32) {
        if (__builtin_expect(left == 1, 0)) __builtin_amdgcn_s_setprio(1);
        SB();
        SEG(2, pa, sp0);
        EXPX(XA, 0, 8);
        SEG(2, pb, sp1);
        EXPX(XA, 8, 16);
        PLUS1();
        PACKX(XB);
        srcoff += 64; recoff += 512; cvoff += 256; ohoff += 64; axoff += 64;
        asm volatile("" : "+v"(srcoff), "+v"(recoff), "+v"(cvoff), "+v"(ohoff), "+v"(axoff)); SB();
        RCP_(0, 4);
        XB = MFMA32(asbf(aex), asbf(wex[1]), (f32x16)(0.f)); PIN(XB);
        RCP_(4, 8);
        XB = MFMA32(asbf(g2), asbf(id0), XB); PIN(XB);
        RCP_(8, 12);
        XB = MFMA32(asbf(g3), asbf(id1), XB); PIN(XB);
        RCP_(12, 16);
        XA = XA * e; PIN(XA);
        SEG(3, pa, sp0);
        VMWAIT(4);
        g0 = RDG(0, 1); g1 = RDG(1, 1); g2 = RDG(2, 1); g3 = RDG(3, 1);
        const u32x2 sv = *(const u32x2*)(lds + srcoff + 64);
        SB();
        EXPX(XB, 0, 8);
        SEG(3, pb, sp1);
        const u32x2 c0 = trread(lds + cvoff), c1 = trread(lds + cvoff + 32), c2 = trread(lds + cvoff + 128), c3 = trread(lds + cvoff + 160);
        const u32x4 o0 = *(const u32x4*)(lds + ohoff), o1 = *(const u32x4*)(lds + ohoff + 32);
        SB();
        EXPX(XB, 8, 16);
        PLUS1();
        PACKX(XA);
        sp0 = SPROD(o0, c0, c1);
        sp1 = SPROD(o1, c2, c3);
        asm volatile("" : "+v"(sp0), "+v"(sp1));
        LGKMWAIT0();
        SB();
        SETM0(m0b); DMA1(0, ad[0], srd, sfb); SB();
        RCP_(0, 4);
        XA = MFMA32(asbf(aex), asbf(wex[2]), (f32x16)(0.f)); PIN(XA);
        DMA1(1024, ad[1], srd, sfb); SB();
        RCP_(4, 8);
        XA = MFMA32(asbf(g0), asbf(id0), XA); PIN(XA);
        DMA1(2048, ad[2], srd, sfb); SB();
        RCP_(8, 12);
        XA = MFMA32(asbf(g1), asbf(id1), XA); PIN(XA);
        DMA1(3072, ad[3], srd, sfb); SB();
        RCP_(12, 16);
        XB = XB * e; PIN(XB);
        if (__builtin_expect(fbase != base, 0)) {
          flush(fbase, base, IC<1>{});
          fbase = base;
        }
        if (__builtin_expect(left <= 2, 0)) {
          if (left == 2) __builtin_amdgcn_s_setprio(2);
          else __builtin_amdgcn_s_setprio(0);
          prep(sv, false);
        } else {
          prep(sv, true);
        }
        asm volatile("" : "+v"(ad[0]), "+v"(ad[1]), "+v"(ad[2]), "+v"(ad[3]));
        SB();
        SEG(0, pa, sp0);
        const int nlast = node_at(min(x + 31, m - 1));
        SB();
        EXPX(XA, 0, 8);
        SEG(0, pb, sp1);
        EXPX(XA, 8, 16);
        PLUS1();
        PACKX(XB);
        RCP_(0, 4);
        XB = MFMA32(asbf(aex), asbf(wex[3]), (f32x16)(0.f)); PIN(XB);
        RCP_(4, 8);
        XB = MFMA32(asbf(g2), asbf(id0), XB); PIN(XB);
        RCP_(8, 12);
        XB = MFMA32(asbf(g3), asbf(id1), XB); PIN(XB);
        RCP_(12, 16);
        XA = XA * e; PIN(XA);
        if (__builtin_expect(left > 1 && nlast >= base + 8, 0)) {
          base = node_at(x);
          loadw(base);
        }
        SB();
        SEG(1, pa, sp0);
        VMWAIT(4);
        const u32x2 t0 = trread(lds + axoff), t1 = trread(lds + axoff + 4 * OHS);
        const u32x4 ra = *(const u32x4*)(lds + recoff);
        g0 = RDG(0, 0); g1 = RDG(1, 0); g2 = RDG(2, 0); g3 = RDG(3, 0);
        SB();
        EXPX(XB, 0, 8);
        SEG(1, pb, sp1);
        EXPX(XB, 8, 16);
        PLUS1();
        PACKX(XA);
        LGKMWAIT0();
        SB();
        aex = hh ? (u32x4){t0[0], t0[1], t1[0], t1[1]} : ra;
        PIN(aex);
        SETM0(m0a); DMA1(0, ad[0], srd, 0); SB();
        RCP_(0, 4);
        XA = MFMA32(asbf(aex), asbf(wex[0]), (f32x16)(0.f)); PIN(XA);
        DMA1(1024, ad[1], srd, 0); SB();
        RCP_(4, 8);
        XA = MFMA32(asbf(g0), asbf(id0), XA); PIN(XA);
        DMA1(2048, ad[2], srd, 0); SB();
        RCP_(8, 12);
        XA = MFMA32(asbf(g1), asbf(id1), XA); PIN(XA);
        DMA1(3072, ad[3], srd, 0); SB();
        RCP_(12, 16);
        XB = XB * e; PIN(XB);
      }
      SEG(2, pa, sp0);
      SEG(2, pb, sp1);
      PACKX(XB);
      SEG(3, pa, sp0);
      SEG(3, pb, sp1);
      flush(base, lnode + 1, IC<0>{});
    }
  } else {
    tid = threadIdx.x;
    asm volatile("" : "+v"(tid));
    lane = tid & 63; gq = lane >> 4; c16 = lane & 15; hh = lane >> 5; n32 = lane & 31;
    lane_consts();
    t4 = *(const u32x4*)(ws + WS_T + b * 1024 + lane * 16);
    decode();
    const rsrc_t rs_rec = mkrs(ws + WS_R, NE * 12), rs_pos = mkrs(pos, NN * 6);
    for (int q = tid; q < 40 * 33; q += 512) *(u32x4*)(lds + L_MG + q * 16) = (u32x4)(0u);
    for (int r0 = 0; r0 < mtot; r0 += CAP) {
      const int m = min(mtot - r0, CAP);
      __syncthreads();
      for (int q = tid; q < (L_HM - L_OH) / 16; q += 512) *(u32x4*)(lds + L_OH + q * 16) = (u32x4)(0u);
      const int lim = min(scnt, r0 + CAP - sexc);
      for (int i = max(r0 - sexc, 0) + (w >> 2); i < lim; i += 2)
        *(u32x3*)(lds + L_STG + (sexc + i - r0) * 16) = __builtin_amdgcn_raw_buffer_load_b96(rs_rec, sbase + i * 12, 0, 0);
      __syncthreads();
      for (int f = tid; f < m; f += 512) {
        u32* rp = (u32*)(lds + L_STG + f * 16);
        rp[3] = atomicAdd((u32*)(lds + L_CNT) + ((rp[2] >> 16) - n0), 1u);
      }
      __syncthreads();
      const int nst = count_nodes(m, ((const u32*)(lds + L_CNT))[lane]);
      for (int f0 = w * 64; f0 < m; f0 += 512) {
        const bool ok = f0 + lane < m;
        const u32x4 rc = *(const u32x4*)(lds + L_STG + (f0 + lane) * 16);
        const u32 dl = ok ? (rc[2] >> 16) - n0 : 0u;
        const u32 pp = rc[3] + __builtin_amdgcn_ds_bpermute(dl * 4, nst), p = ok ? pp : CAP;
        const u32x2 ps = __builtin_amdgcn_raw_buffer_load_b64(rs_pos, ((rc[2] & 0xffffu) * 6) & ~3u, 0, 0);
        *(u16*)(lds + L_SRC + 2 * p) = (u16)rc[2];
        place((u32x3){rc[0], rc[1], rc[2]}, ps, *(const u32x2*)(lds + L_NODE + dl * 8), dl, p);
      }
      __syncthreads();
      const int nt = w < 4 ? (m + 31) >> 5 : 0;
      for (int tile = w; tile < nt; tile += 4) {
        const int last = node_at(min(tile * 32 + 31, m - 1));
        prep(*(const u32x2*)(lds + srcoff + tile * 64), true);
        SETM0(m0a); DMA1(0, ad[0], srd_g, 0); DMA1(1024, ad[1], srd_g, 0); DMA1(2048, ad[2], srd_g, 0); DMA1(3072, ad[3], srd_g, 0);
        SETM0(m0b); DMA1(0, ad[0], srd_g, 128); DMA1(1024, ad[1], srd_g, 128); DMA1(2048, ad[2], srd_g, 128); DMA1(3072, ad[3], srd_g, 128);
        const u32x2 t0 = trread(lds + axoff + tile * 64), t1 = trread(lds + axoff + tile * 64 + 4 * OHS);
        const u32x4 ra = *(const u32x4*)(lds + recoff + tile * 512);
        const u32x4 aex = hh ? (u32x4){t0[0], t0[1], t1[0], t1[1]} : ra;
        for (int base = node_at(tile * 32); base <= last; base += 8) {
          loadw(base);
          u32x4 sp[2];
#pragma unroll
          for (int s = 0; s < 2; s++) {
            const u32x2 c0 = trread(lds + cvoff + tile * 256 + s * 128), c1 = trread(lds + cvoff + tile * 256 + s * 128 + 32);
            const u32x4 oh = *(const u32x4*)(lds + ohoff + tile * 64 + 32 * s);
            sp[s] = SPROD(oh, c0, c1);
          }
#pragma unroll 1
          for (int j = 0; j < 16; j++) {
            const int dn = *(const u32*)(lds + L_REC + (tile * 32 + 16 * (j >> 3) + 8 * hh + (j & 7)) * 16 + 12);
            const u32 mk = (dn < base || dn >= base + 8) ? ((j & 1) ? 0x0000ffffu : 0xffff0000u) : 0xffffffffu;
#pragma unroll
            for (int i = 0; i < 8; i++) sp[i >> 2][i & 3] &= (i == (j >> 1)) ? mk : 0xffffffffu;
          }
          VMWAIT0();
#pragma unroll
          for (int t = 0; t < 4; t++) {
            const u32x4 ga_ = RDG((2 * t) & 3, t >> 1), gb_ = RDG((2 * t + 1) & 3, t >> 1);
            f32x16 xc = MFMA32(asbf(aex), asbf(wex[t]), (f32x16)(0.f)), e;
            xc = MFMA32(asbf(ga_), asbf(id0), xc);
            xc = MFMA32(asbf(gb_), asbf(id1), xc);
            u32x4 pa, pb;
            EXP_(0, 16);
            e = e + 1.0f;
            RCP_(0, 16);
            e = e * xc;
            PACK_();
            Y[t] = MFMA32(asbf(pa), asbf(sp[0]), (f32x16)(0.f));
            Y[t] = MFMA32(asbf(pb), asbf(sp[1]), Y[t]);
          }
          const int node = base + (((n32 >> 2) - base) & 7), comp = n32 & 3;
          const float pt = pdot(IC<4>{});
          if (node <= last) {
            float* mp = (float*)(lds + L_MG) + node * 132;
            if (comp == 3) {
#pragma unroll
              for (int t = 0; t < 4; t++)
#pragma unroll
                for (int r = 0; r < 16; r++) atomicAdd(mp + 4 * hh + 32 * t + 8 * (r >> 2) + (r & 3), Y[t][r]);
            } else if (hh == 0) {
              atomicAdd(mp + 128 + comp, pt);
            }
          }
        }
        LGKMWAIT0();
      }
    }
    __syncthreads();
    for (int q = tid; q < 40 * 66; q += 512) {
      const int row = q / 66, c = q - row * 66;
      const f32x2 v = *(const f32x2*)(lds + L_MG + row * 528 + c * 8);
      if (c < 64) *(u32*)(lds + L_IMG + row * 528 + 256 + c * 4) = pk2(v[0], v[1]);
      else *(f32x2*)(lds + L_IMG + row * 528 + c * 8) = v;
    }
    __syncthreads();
    const u32x4 srd_wh = mksrd(wh, 65536);
    SETM0(m0a); DMA1(0, vsrc, srd_wh, 0); DMA1(1024, vsrc, srd_wh, 7168); DMA1(2048, vsrc, srd_wh, 14336); DMA1(3072, vsrc, srd_wh, 21504);
    SETM0(m0b); DMA1(0, vsrc, srd_wh, 32768); DMA1(1024, vsrc, srd_wh, 39936); DMA1(2048, vsrc, srd_wh, 47104); DMA1(3072, vsrc, srd_wh, 54272);
    VMWAIT0();
  }

  int tid2 = threadIdx.x;
  asm volatile("" : "+v"(tid2));
  const int c16e = tid2 & 15, gqe = (tid2 >> 4) & 3;
  const char* const xbe = lds + L_IMG + c16e * 528 + 16 * gqe;
  f32x4 acc[3], acm[3];
  const char* const wfp = lds + L_STG + w * 8192 + (8 * gqe + (c16e >> 2)) * 32 + 8 * (c16e & 3);
  VMWAIT(4);
  u32x4 wf2[4];
#pragma unroll
  for (int rt = 0; rt < 3; rt++) acc[rt] = acm[rt] = (f32x4)(0.f);
  {
    u32x4 wf1[4], xf[4][3];
#pragma unroll
    for (int s = 0; s < 4; s++) {
      const u32x2 x0 = trread(wfp + s * 1024), x1 = trread(wfp + s * 1024 + 128);
      const u32x2 y0 = trread(wfp + 4096 + s * 1024), y1 = trread(wfp + 4096 + s * 1024 + 128);
      wf1[s] = (u32x4){x0[0], x0[1], x1[0], x1[1]};
      wf2[s] = (u32x4){y0[0], y0[1], y1[0], y1[1]};
#pragma unroll
      for (int rt = 0; rt < 3; rt++) xf[s][rt] = *(const u32x4*)(xbe + rt * 8448 + 64 * s);
    }
    SB();
#pragma unroll
    for (int s = 0; s < 4; s++)
#pragma unroll
      for (int rt = 0; rt < 3; rt++) acc[rt] = MFMA16(asbf(wf1[s]), asbf(xf[s][rt]), acc[rt]);
  }
  u32x2 hr[3];
  u32 hm[3], hl[3];
#pragma unroll
  for (int rt = 0; rt < 3; rt++) {
    hr[rt] = *(const u32x2*)(lds + L_IMG + c16e * 528 + 8 * gqe + rt * 8448 + 32 * w);
    hm[rt] = 16 * rt + c16e < 40 ? ((const u32*)(lds + L_HM))[16 * rt + c16e] : 0u;
    hl[rt] = 64 * gqe + 28 + 4 * __builtin_ctz(hm[rt] | 256u);
    hm[rt] &= hm[rt] - 1;
  }
  __syncthreads();
  {
    u32x4 xf[3][4];
#pragma unroll
    for (int s = 0; s < 4; s++)
#pragma unroll
      for (int rt = 2; rt >= 0; rt--) xf[rt][s] = *(const u32x4*)(xbe + 256 + rt * 8448 + 64 * s);
    SB();
#pragma unroll
    for (int s = 0; s < 4; s++)
#pragma unroll
      for (int rt = 2; rt >= 0; rt--) acm[rt] = MFMA16(asbf(wf2[s]), asbf(xf[rt][s]), acm[rt]);
  }
  const f32x4 hd = acm[2];
  for (;;) {
#pragma unroll
    for (int rt = 0; rt < 3; rt++)
#pragma unroll
      for (int r = 0; r < 4; r++) {
        const float hv_ = hd[r];
        acm[rt][r] += __uint_as_float(__builtin_amdgcn_ds_bpermute(hl[rt], __float_as_uint(hv_)));
      }
    if (__builtin_expect(__builtin_amdgcn_ballot_w64((hm[0] | hm[1] | hm[2]) != 0) == 0, 1)) break;
#pragma unroll
    for (int rt = 0; rt < 3; rt++) {
      hl[rt] = 64 * gqe + 28 + 4 * __builtin_ctz(hm[rt] | 256u);
      hm[rt] &= hm[rt] - 1;
    }
  }
  {
    const rsrc_t rs_o = mkrs(out + (size_t)n0 * 128, nn * 256);
#pragma unroll
    for (int rt = 0; rt < 3; rt++) {
      u32 o[2];
#pragma unroll
      for (int r = 0; r < 2; r++) {
        const f32x2 z = (f32x2){acc[rt][2 * r], acc[rt][2 * r + 1]} + (f32x2){acm[rt][2 * r], acm[rt][2 * r + 1]} * NLN2;
        const f32x2 t = z * NLOG2E;
        const f32x2 d = (f32x2){__builtin_amdgcn_exp2f(t[0]), __builtin_amdgcn_exp2f(t[1])} + 1.0f;
        const f32x2 v = z * (f32x2){__builtin_amdgcn_rcpf(d[0]), __builtin_amdgcn_rcpf(d[1])} + (f32x2){bf2f(hr[rt][r]), bfhi(hr[rt][r])};
        o[r] = pk2(v[0], v[1]);
      }
      __builtin_amdgcn_raw_buffer_store_b64((u32x2){o[0], o[1]}, rs_o, c16e * 256 + 8 * gqe, rt * 4096 + 32 * w, 0);
    }
  }
  if (w < 2) {
    const int nl = __umul24(tid2, 21846) >> 16, k = tid2 - 3 * nl;
    if (tid2 < 3 * nn) {
      const char* mp = lds + L_IMG + 512 + 4 * k;
      u32 hq = ((const u32*)(lds + L_HM))[nl];
      float pv = *(const float*)(mp + nl * 528) + bf2f(*(const u16*)(lds + L_NODE + nl * 2 + tid2 * 2));
      while (hq) {
        pv += *(const float*)(mp + (39 + __builtin_ctz(hq)) * 528);
        hq &= hq - 1;
      }
      out[(size_t)NN * 128 + n0 * 3 + tid2] = (u16)pk2(pv, 0.f);
    }
  }
  VMWAIT0();
}

extern "C" void kernel_launch(void* const* d_in, const int* in_sizes, int n_in, void* d_out, int out_size,
                              void* d_ws, size_t ws_size, hipStream_t stream) {
  const u16* h = (const u16*)d_in[0];
  const u16* pos = (const u16*)d_in[1];
  const u16* ea = (const u16*)d_in[2];
  const int* src = (const int*)d_in[3];
  const int* dst = (const int*)d_in[4];
  const u16* we = (const u16*)d_in[5];
  const u16* wx = (const u16*)d_in[6];
  const u16* wh = (const u16*)d_in[7];
  egnn_k1<<<NB, 512, 0, stream>>>(h, ea, src, dst, we, (char*)d_ws, pos, wh, (const u16*)d_out);
  egnn_k2<<<NB, 512, 0, stream>>>(h, pos, we, wx, wh, (const char*)d_ws, (u16*)d_out);
}
```
